```python
import torch

def _softmax(x, axis=-1):
    return torch.softmax(x, dim=axis)
def _gelu(x):
    return torch.nn.functional.gelu(x, approximate="tanh")
def _silu(x):
    return torch.nn.functional.silu(x)
def _sigmoid(x):
    return torch.sigmoid(x)
def _layernorm(x, axis=-1, eps=1e-5):
    m = x.mean(dim=axis, keepdim=True)
    v = x.var(dim=axis, keepdim=True, unbiased=False)
    return (x - m) / torch.sqrt(v + eps)
def _scatter_add(tgt, idx, src, axis=0):
    return tgt.clone().index_add_(axis, idx, src)
def _segment_sum(src, seg, n):
    out = torch.zeros((n,) + src.shape[1:], dtype=src.dtype)
    return out.index_add_(0, seg, src)
def _pad(x, pw):
    flat = tuple(v for pair in reversed(pw) for v in pair)
    return torch.nn.functional.pad(x, flat)

kernel_name = 'parallel_sum__gqa_block_cached__geglu_ffn__decode'

def setup_inputs():

    _w = (lambda a: (lambda t: t.to(torch.bfloat16) if t.is_floating_point() else t)(torch.as_tensor(a)))
    def _A_setup():
        B, Sq, D = 2, 1, 576
        Hq, Hkv, Sk, Dh = 32, 16, 32768, 128
        assert Hq % Hkv == 0, (Hq, Hkv)
        return {
            "x": _w((lambda *s: torch.randn(*s, dtype=torch.bfloat16) * 0.05)(B, Sq, D)),
            "k_cache": _w((lambda *s: torch.randn(*s, dtype=torch.bfloat16) * 3.2)(B, Hkv, Sk, Dh)),
            "v_cache": _w((lambda *s: torch.randn(*s, dtype=torch.bfloat16) * 1.6)(B, Hkv, Sk, Dh)),
            "wq": _w((lambda *s: torch.randn(*s, dtype=torch.bfloat16) * 3.2)(D, Hq * Dh) * (D ** -0.5)),
            "wo": _w((lambda *s: torch.randn(*s, dtype=torch.bfloat16) * 1.6)(Hq * Dh, D) * ((Hq * Dh) ** -0.5)),
        }
    _A_in = _A_setup()

    def _B_setup():
        B, S, D, F = 2, 1, 576, 4608
        return {
            "x": _w((lambda *s: torch.randn(*s, dtype=torch.bfloat16) * 0.05)(B, S, D)),
            "w_gate": _w((lambda *s: torch.randn(*s, dtype=torch.bfloat16) * 0.05)(D, F)),
            "w_up": _w((lambda *s: torch.randn(*s, dtype=torch.bfloat16) * 0.05)(D, F)),
            "w_down": _w((lambda *s: torch.randn(*s, dtype=torch.bfloat16) * 0.05)(F, D)),
        }
    _B_in = _B_setup()

    return {
        "A_x": _A_in["x"],
        "A_k_cache": _A_in["k_cache"],
        "A_v_cache": _A_in["v_cache"],
        "A_wq": _A_in["wq"],
        "A_wo": _A_in["wo"],
        "B_x": _B_in["x"],
        "B_w_gate": _B_in["w_gate"],
        "B_w_up": _B_in["w_up"],
        "B_w_down": _B_in["w_down"],
    }

def reference(A_x, A_k_cache, A_v_cache, A_wq, A_wo, B_x, B_w_gate, B_w_up, B_w_down):
    def _A_ref(x, k_cache, v_cache, wq, wo):
        B, Sq, D = x.shape
        Hq, Dh = 32, 128
        Hkv = k_cache.shape[1]
        rep = Hq // Hkv
        q = (lambda x, *d: x.permute(*d))((x @ wq).reshape(B, Sq, Hq, Dh), 0, 2, 1, 3)
        k = (lambda x, n, axis: torch.repeat_interleave(x, n, dim=axis))(k_cache, rep, axis=1)
        v = (lambda x, n, axis: torch.repeat_interleave(x, n, dim=axis))(v_cache, rep, axis=1)
        scores = torch.einsum("bhqd,bhkd->bhqk", q, k) * (Dh ** -0.5)
        p = _softmax(scores, axis=-1)
        o = torch.einsum("bhqk,bhkd->bhqd", p, v)
        return (lambda x, *d: x.permute(*d))(o, 0, 2, 1, 3).reshape(B, Sq, Hq * Dh) @ wo

    def _B_ref(x, w_gate, w_up, w_down):
        return (_gelu(x @ w_gate) * (x @ w_up)) @ w_down

    x = A_x
    xn = _layernorm(x, axis=-1)
    return x + _A_ref(xn, A_k_cache, A_v_cache, A_wq, A_wo) + _B_ref(xn, B_w_gate, B_w_up, B_w_down)
```

```cpp
#include <hip/hip_runtime.h>
#include <hip/hip_bf16.h>
#include <stdint.h>

typedef unsigned u32;
typedef unsigned short u16;
typedef unsigned long long u64;
typedef u32 u32x4 __attribute__((ext_vector_type(4)));
typedef float f32x4 __attribute__((ext_vector_type(4)));
typedef __bf16 bf16x2 __attribute__((ext_vector_type(2)));
#define GAS __attribute__((address_space(1)))
typedef GAS u64 gu64;
typedef GAS u32 gu32;
#define RLX_AGENT __ATOMIC_RELAXED, __HIP_MEMORY_SCOPE_AGENT

constexpr int DM = 576, HKV = 16, SK = 32768, DH = 128, FF = 4608, NQ = 4096;
constexpr int NTHR = 512, NW = 8, U = 4;
constexpr int UNIT_BYTES = NW * U * 1024;
constexpr int NFT = 72;
constexpr int AUX_NT = 2;
constexpr float LOG2E = 1.4426950408889634f;
constexpr u32 SPIN_CAP = 1u << 16;

constexpr size_t WS_EPOCH = 0;
constexpr size_t WS_P1 = 4096;
constexpr int P1_STRIDE = 264;
constexpr size_t WS_P2 = WS_P1 + (size_t)256 * P1_STRIDE * 8;
constexpr size_t WS_PF = WS_P2 + (size_t)9 * 16 * 128 * 8;
constexpr size_t WS_END = WS_PF + (size_t)9 * NFT * 128 * 8;

__device__ __forceinline__ float bf2f(u32 lo16) { return __builtin_bit_cast(float, lo16 << 16); }
__device__ __forceinline__ float rbf(float f) { return (float)(__bf16)f; }
__device__ __forceinline__ u32 f2bf(float f) { return (u32)__builtin_bit_cast(u16, (__bf16)f); }
__device__ __forceinline__ float dot2(u32 a, u32 b, float c) {
  return __builtin_amdgcn_fdot2_f32_bf16(__builtin_bit_cast(bf16x2, a), __builtin_bit_cast(bf16x2, b), c, false);
}
template <int CTRL> __device__ __forceinline__ float dpp(float x) {
  return __builtin_bit_cast(float, __builtin_amdgcn_update_dpp(0, __builtin_bit_cast(int, x), CTRL, 0xf, 0xf, true));
}
constexpr int DPP_XOR1 = 0xB1, DPP_XOR2 = 0x4E, DPP_HMIR = 0x141, DPP_ROR8 = 0x128, DPP_ROR4 = 0x124, DPP_BC = 0x150;
__device__ __forceinline__ void swap32(float& a, float& b) {
  auto r = __builtin_amdgcn_permlane32_swap(__builtin_bit_cast(u32, a), __builtin_bit_cast(u32, b), false, false);
  a = __uint_as_float(r[0]); b = __uint_as_float(r[1]);
}
__device__ __forceinline__ void swap16(float& a, float& b) {
  auto r = __builtin_amdgcn_permlane16_swap(__builtin_bit_cast(u32, a), __builtin_bit_cast(u32, b), false, false);
  a = __uint_as_float(r[0]); b = __uint_as_float(r[1]);
}
__device__ __forceinline__ float wave_sum(float x) {
  x += dpp<DPP_XOR1>(x); x += dpp<DPP_XOR2>(x); x += dpp<DPP_ROR4>(x); x += dpp<DPP_ROR8>(x);
  float a = x, b = x; swap16(a, b); x = a + b;
  a = x; b = x; swap32(a, b); return a + b;
}
__device__ __forceinline__ __amdgpu_buffer_rsrc_t mkrs(const void* p, u32 bytes) {
  return __builtin_amdgcn_make_buffer_rsrc(const_cast<void*>(p), 0, bytes, 0x00020000);
}
template <int AUX> __device__ __forceinline__ u32x4 bld(__amdgpu_buffer_rsrc_t rs, int voff, int soff) {
  return __builtin_amdgcn_raw_buffer_load_b128(rs, voff, soff, AUX);
}
__device__ __forceinline__ void pair_mac(const u32x4& a, const u32x4& b, u32 xp, float* acc) {
#pragma unroll
  for (int d = 0; d < 4; ++d) {
    const u32 lo = __builtin_amdgcn_perm(b[d], a[d], 0x05040100u), hi = __builtin_amdgcn_perm(b[d], a[d], 0x07060302u);
    acc[2 * d] = dot2(lo, xp, acc[2 * d]); acc[2 * d + 1] = dot2(hi, xp, acc[2 * d + 1]);
  }
}
__device__ __forceinline__ void pair_mac2(const u32x4& a, const u32x4& b, u32 xp0, u32 xp1, float* acc0, float* acc1) {
#pragma unroll
  for (int d = 0; d < 4; ++d) {
    const u32 lo = __builtin_amdgcn_perm(b[d], a[d], 0x05040100u), hi = __builtin_amdgcn_perm(b[d], a[d], 0x07060302u);
    acc0[2 * d] = dot2(lo, xp0, acc0[2 * d]); acc0[2 * d + 1] = dot2(hi, xp0, acc0[2 * d + 1]);
    acc1[2 * d] = dot2(lo, xp1, acc1[2 * d]); acc1[2 * d + 1] = dot2(hi, xp1, acc1[2 * d + 1]);
  }
}
__device__ __forceinline__ float rs8_hi(float* v, int lane) {
#pragma unroll
  for (int k = 0; k < 4; ++k) { swap32(v[k], v[k + 4]); v[k] += v[k + 4]; }
#pragma unroll
  for (int k = 0; k < 2; ++k) { swap16(v[k], v[k + 2]); v[k] += v[k + 2]; }
  const bool up = lane & 8;
  const float keep = up ? v[1] : v[0], send = up ? v[0] : v[1];
  return keep + dpp<DPP_ROR8>(send);
}
__device__ __forceinline__ void st_gran(gu64* g, u32 tag, float v) {
  __hip_atomic_store(g, ((u64)tag << 32) | (u64)__builtin_bit_cast(u32, v), RLX_AGENT);
}

constexpr int L_XP = 0;
constexpr int L_Q = L_XP + 576;
constexpr int L_H = L_Q + 128;
constexpr int L_ML = L_H + 256;
constexpr int L_RED = L_ML + 32;
constexpr int L_RED_WORDS = 8 * 2 * 576;
constexpr int L_TOTAL = L_RED + L_RED_WORDS;

__global__ void __launch_bounds__(NTHR, 2)
fused_decode_kernel(const u16* __restrict__ x, const u16* __restrict__ kc, const u16* __restrict__ vc, const u16* __restrict__ wq,
                    const u16* __restrict__ wo, const u16* __restrict__ wg, const u16* __restrict__ wu, const u16* __restrict__ wd,
                    u16* __restrict__ out, unsigned char* __restrict__ ws) {
  __shared__ __attribute__((aligned(16))) u32 lds[L_TOTAL];
  float* ldsf = reinterpret_cast<float*>(lds);
  const int tid = threadIdx.x, lane = tid & 63, w = __builtin_amdgcn_readfirstlane(tid >> 6);
  const int bid = blockIdx.x;
  const int xg = bid & 7, gi = bid >> 6, s = (bid >> 3) & 7;
  const int g = xg + 8 * gi, b = g >> 4, kvh = g & 15;
  const int ii = b * 8 + s;
  const bool is_red = (b == 1 && s == 7 && kvh < 9);
  const int nf = gi == 0 ? 3 : 2;
  const int cf = (gi == 0 || (b == 1 && kvh < 9)) ? 30 : 29;
  const bool is_ffn = s < nf;
  const int ustart = is_ffn ? cf * s : cf * nf + 33 * (s - nf);
  const int n = is_ffn ? cf : (s == 7 ? 256 - ustart : 33);
  const int task = s < 2 ? g * 2 + s : 64 + xg;

  u32 epoch_v = __hip_atomic_load((gu32*)(ws + WS_EPOCH), RLX_AGENT);

  u32 xr[5] = {0, 0, 0, 0, 0};
  if (w < 2) {
    const u32* xw = reinterpret_cast<const u32*>(x) + w * 288;
#pragma unroll
    for (int j = 0; j < 5; ++j) if (j < 4 || lane < 32) xr[j] = xw[j * 64 + lane];
  }
  const __amdgpu_buffer_rsrc_t rs_wq = mkrs(wq, DM * NQ * 2);
  u32x4 wqv[36];
  {
    const int vo = (36 * (lane >> 5)) * (NQ * 2) + (lane & 31) * 16;
    const int so = 72 * w * (NQ * 2) + kvh * 512;
#pragma unroll
    for (int j = 0; j < 36; ++j) wqv[j] = bld<0>(rs_wq, vo, so + j * (NQ * 2));
  }
  __builtin_amdgcn_sched_barrier(0);
  const __amdgpu_buffer_rsrc_t rs_k = mkrs(kc + (size_t)g * SK * DH, SK * DH * 2);
  const __amdgpu_buffer_rsrc_t rs_v = mkrs(vc + (size_t)g * SK * DH, SK * DH * 2);
  const int kv_vo = lane * 16;
  const int kv_so = ustart * UNIT_BYTES + w * (U * 1024);
  u32x4 KA[U], VA[U], KB[U], VB[U];
#define ISSUE_K(BUF, T) _Pragma("unroll") for (int i_ = 0; i_ < U; ++i_) BUF[i_] = bld<AUX_NT>(rs_k, kv_vo + i_ * 1024, kv_so + (T) * UNIT_BYTES); __builtin_amdgcn_sched_barrier(0)
#define ISSUE_V(BUF, T) _Pragma("unroll") for (int i_ = 0; i_ < U; ++i_) BUF[i_] = bld<AUX_NT>(rs_v, kv_vo + i_ * 1024, kv_so + (T) * UNIT_BYTES); __builtin_amdgcn_sched_barrier(0)
  ISSUE_K(KA, 0); ISSUE_V(VA, 0); ISSUE_K(KB, 1); ISSUE_V(VB, 1);

  if (w < 2) {
    float v[10]; float sum = 0.f;
#pragma unroll
    for (int j = 0; j < 5; ++j) { v[2 * j] = bf2f(xr[j] & 0xffffu); v[2 * j + 1] = bf2f(xr[j] >> 16); sum += v[2 * j] + v[2 * j + 1]; }
    const float mean = wave_sum(sum) * (1.0f / DM);
    float sq = 0.f;
#pragma unroll
    for (int j = 0; j < 5; ++j) if (j < 4 || lane < 32) { const float d0 = v[2 * j] - mean, d1 = v[2 * j + 1] - mean; sq += d0 * d0 + d1 * d1; }
    const float var = wave_sum(sq) * (1.0f / DM);
    const float mb = rbf(mean), sd = rbf(sqrtf(rbf(rbf(var) + 1e-5f)));
#pragma unroll
    for (int j = 0; j < 5; ++j) if (j < 4 || lane < 32)
      lds[L_XP + w * 288 + j * 64 + lane] = f2bf(rbf(v[2 * j] - mb) / sd) | (f2bf(rbf(v[2 * j + 1] - mb) / sd) << 16);
  }
  __syncthreads();

  {
    float acc[8];
#pragma unroll
    for (int k = 0; k < 8; ++k) acc[k] = 0.f;
    const u32* xp = lds + L_XP + b * 288 + 36 * w + 18 * (lane >> 5);
#pragma unroll
    for (int j = 0; j < 18; ++j) pair_mac(wqv[2 * j], wqv[2 * j + 1], xp[j], acc);
#pragma unroll
    for (int k = 0; k < 4; ++k) { swap32(acc[k], acc[k + 4]); acc[k] += acc[k + 4]; }
    *reinterpret_cast<f32x4*>(ldsf + L_RED + w * 256 + (lane & 31) * 8 + (lane >> 5) * 4) = f32x4{acc[0], acc[1], acc[2], acc[3]};
  }
  __syncthreads();
  if (tid < 256) {
    float a = 0.f;
#pragma unroll
    for (int k = 0; k < 8; ++k) a += ldsf[L_RED + k * 256 + tid];
    reinterpret_cast<u16*>(lds + L_Q)[tid] = (u16)f2bf(a);
  }
  __syncthreads();
  asm volatile("" : "+v"(epoch_v));
  const u32 tag = __builtin_amdgcn_readfirstlane(epoch_v) + 0x9E3779B9u;
  const int r = lane >> 4, c = lane & 15;
  const u32x4 q0 = *reinterpret_cast<const u32x4*>(lds + L_Q + c * 4);
  const u32x4 q1 = *reinterpret_cast<const u32x4*>(lds + L_Q + 64 + c * 4);

  if (is_ffn) {
    const __amdgpu_buffer_rsrc_t rs_gu = mkrs(w < 4 ? wg : wu, DM * FF * 2);
    const __amdgpu_buffer_rsrc_t rs_d = mkrs(wd + (size_t)task * 64 * DM, 64 * DM * 2);
    u32x4 gv[18], dv[10];
    const int rr = lane >> 3, cc = lane & 7;
    {
      const int vo = rr * 18 * (FF * 2) + cc * 16, so = (w & 3) * 144 * (FF * 2) + task * 128;
#pragma unroll
      for (int j = 0; j < 18; ++j) gv[j] = bld<AUX_NT>(rs_gu, vo, so + j * (FF * 2));
#pragma unroll
      for (int j = 0; j < 8; ++j) dv[j] = bld<AUX_NT>(rs_d, lane * 16, (8 * w + j) * (DM * 2));
      const int vo2 = lane < 32 ? (rr * 2) * (DM * 2) + 1024 + cc * 16 : 0x7fffff00;
      dv[8] = bld<AUX_NT>(rs_d, vo2, 8 * w * (DM * 2));
      dv[9] = bld<AUX_NT>(rs_d, vo2, (8 * w + 1) * (DM * 2));
    }
    __builtin_amdgcn_sched_barrier(0);
    float a0[8], a1[8];
#pragma unroll
    for (int k = 0; k < 8; ++k) { a0[k] = 0.f; a1[k] = 0.f; }
    const u32* xp = lds + L_XP + (w & 3) * 72 + rr * 9;
#pragma unroll
    for (int j = 0; j < 9; ++j) pair_mac2(gv[2 * j], gv[2 * j + 1], xp[j], xp[288 + j], a0, a1);
    const float s0 = rs8_hi(a0, lane), s1 = rs8_hi(a1, lane);
    const int col = cc * 8 + rr;
    ldsf[L_RED + (w * 2 + 0) * 64 + col] = s0;
    ldsf[L_RED + (w * 2 + 1) * 64 + col] = s1;
    __syncthreads();
    if (tid < 128) {
      const int bb = tid >> 6, cl = tid & 63;
      float gs = 0.f, us = 0.f;
#pragma unroll
      for (int k = 0; k < 4; ++k) { gs += ldsf[L_RED + (k * 2 + bb) * 64 + cl]; us += ldsf[L_RED + ((k + 4) * 2 + bb) * 64 + cl]; }
      const float gg = rbf(gs), uu = rbf(us);
      const float inner = 0.7978845608028654f * (gg + 0.044715f * gg * gg * gg);
      const float ge = rbf(0.5f * gg * (1.0f + tanhf(inner)));
      reinterpret_cast<u16*>(lds + L_H)[tid] = (u16)f2bf(ge * uu);
    }
    __syncthreads();
#pragma unroll
    for (int k = 0; k < 8; ++k) { a0[k] = 0.f; a1[k] = 0.f; }
    const u32* hp = lds + L_H + 4 * w;
#pragma unroll
    for (int j = 0; j < 4; ++j) pair_mac2(dv[2 * j], dv[2 * j + 1], hp[j], hp[32 + j], a0, a1);
    float* rw = ldsf + L_RED + w * 2 * DM;
    *reinterpret_cast<f32x4*>(rw + lane * 8) = f32x4{a0[0], a0[1], a0[2], a0[3]};
    *reinterpret_cast<f32x4*>(rw + lane * 8 + 4) = f32x4{a0[4], a0[5], a0[6], a0[7]};
    *reinterpret_cast<f32x4*>(rw + DM + lane * 8) = f32x4{a1[0], a1[1], a1[2], a1[3]};
    *reinterpret_cast<f32x4*>(rw + DM + lane * 8 + 4) = f32x4{a1[4], a1[5], a1[6], a1[7]};
#pragma unroll
    for (int k = 0; k < 8; ++k) { a0[k] = 0.f; a1[k] = 0.f; }
    pair_mac2(dv[8], dv[9], hp[rr & 3], hp[32 + (rr & 3)], a0, a1);
    const float e0 = rs8_hi(a0, lane), e1 = rs8_hi(a1, lane);
    rw[512 + col] = e0; rw[DM + 512 + col] = e1;
    __syncthreads();
    for (int o = tid; o < 2 * DM; o += NTHR) {
      float a = 0.f;
#pragma unroll
      for (int k = 0; k < 8; ++k) a += ldsf[L_RED + k * 2 * DM + o];
      const int bb = o >= DM ? 1 : 0, cl = o - bb * DM;
      st_gran((gu64*)(ws + WS_PF) + ((size_t)(cl >> 6) * NFT + task) * 128 + bb * 64 + (cl & 63), tag, a);
    }
    __syncthreads();
  }

  u32x4 wov[4];
  {
    const __amdgpu_buffer_rsrc_t rs_wo = mkrs(wo + (size_t)kvh * 256 * DM, ii < 9 ? 256 * DM * 2 : 0);
    const int vo = (lane >> 3) * 4 * (DM * 2) + (lane & 7) * 16, so = 32 * w * (DM * 2) + ii * 128;
#pragma unroll
    for (int j = 0; j < 4; ++j) wov[j] = bld<AUX_NT>(rs_wo, vo + j * (DM * 2), so);
  }
  __builtin_amdgcn_sched_barrier(0);

  float acc0[8], acc1[8];
#pragma unroll
  for (int k = 0; k < 8; ++k) { acc0[k] = 0.f; acc1[k] = 0.f; }
  float mrun = -INFINITY, lrun = 0.f, p;
  auto qk = [&](const u32x4* Kt) {
    float xv[8];
#pragma unroll
    for (int i = 0; i < U; ++i) {
      float d0 = 0.f, d1 = 0.f;
#pragma unroll
      for (int k = 0; k < 4; ++k) { d0 = dot2(Kt[i][k], q0[k], d0); d1 = dot2(Kt[i][k], q1[k], d1); }
      xv[2 * i] = d0; xv[2 * i + 1] = d1;
    }
#pragma unroll
    for (int k = 0; k < 4; ++k) { const bool up = c & 8; const float keep = up ? xv[k + 4] : xv[k], send = up ? xv[k] : xv[k + 4]; xv[k] = keep + dpp<DPP_ROR8>(send); }
#pragma unroll
    for (int k = 0; k < 2; ++k) { const bool up = c & 4; const float keep = up ? xv[k + 2] : xv[k], send = up ? xv[k] : xv[k + 2]; xv[k] = keep + dpp<DPP_HMIR>(send); }
    { const bool up = c & 2; const float keep = up ? xv[1] : xv[0], send = up ? xv[0] : xv[1]; xv[0] = keep + dpp<DPP_XOR2>(send); }
    float sc = xv[0] + dpp<DPP_XOR1>(xv[0]);
    sc = rbf(rbf(sc) * 0.08838834764831845f);
    float pm = fmaxf(sc, dpp<DPP_ROR4>(sc)); pm = fmaxf(pm, dpp<DPP_ROR8>(pm));
    if (__any(pm > mrun)) {
      const float mn = fmaxf(mrun, pm);
      const float al = __builtin_amdgcn_exp2f((mrun - mn) * LOG2E);
      mrun = mn; lrun *= al;
      const float al0 = dpp<DPP_BC + 0>(al), al1 = dpp<DPP_BC + 2>(al);
#pragma unroll
      for (int k = 0; k < 8; ++k) { acc0[k] *= al0; acc1[k] *= al1; }
    }
    p = __builtin_amdgcn_exp2f((sc - mrun) * LOG2E);
    lrun += p;
  };
  auto pv1 = [&](const u32x4& Vt, float p0, float p1) {
#pragma unroll
    for (int k = 0; k < 4; ++k) {
      const float lo = __builtin_bit_cast(float, Vt[k] << 16), hi = __builtin_bit_cast(float, Vt[k] & 0xffff0000u);
      acc0[2 * k] = fmaf(p0, lo, acc0[2 * k]); acc0[2 * k + 1] = fmaf(p0, hi, acc0[2 * k + 1]);
      acc1[2 * k] = fmaf(p1, lo, acc1[2 * k]); acc1[2 * k + 1] = fmaf(p1, hi, acc1[2 * k + 1]);
    }
  };
  auto pv = [&](const u32x4* Vt) {
    pv1(Vt[0], dpp<DPP_BC + 0>(p), dpp<DPP_BC + 2>(p));
    pv1(Vt[1], dpp<DPP_BC + 4>(p), dpp<DPP_BC + 6>(p));
    pv1(Vt[2], dpp<DPP_BC + 8>(p), dpp<DPP_BC + 10>(p));
    pv1(Vt[3], dpp<DPP_BC + 12>(p), dpp<DPP_BC + 14>(p));
  };
#define SB __builtin_amdgcn_sched_barrier(0)
  int t = 2;
  for (; t + 1 < n; t += 2) {
    qk(KA); SB; ISSUE_K(KA, t); pv(VA); SB; ISSUE_V(VA, t);
    qk(KB); SB; ISSUE_K(KB, t + 1); pv(VB); SB; ISSUE_V(VB, t + 1);
  }
  if (t < n) {
    qk(KA); SB; ISSUE_K(KA, t); pv(VA); SB; ISSUE_V(VA, t);
    qk(KB); pv(VB);
    qk(KA); pv(VA);
  } else {
    qk(KA); pv(VA);
    qk(KB); pv(VB);
  }

  {
    float lr = lrun + dpp<DPP_ROR4>(lrun); lr += dpp<DPP_ROR8>(lr);
    float a = mrun, bb = mrun; swap16(a, bb); float mw = fmaxf(a, bb);
    a = mw; bb = mw; swap32(a, bb); mw = fmaxf(a, bb);
    const float f = __builtin_amdgcn_exp2f((mrun - mw) * LOG2E);
    lr *= f;
    a = lr; bb = lr; swap16(a, bb); lr = a + bb;
    a = lr; bb = lr; swap32(a, bb); lr = a + bb;
    const float f0 = dpp<DPP_BC + 0>(f), f1 = dpp<DPP_BC + 2>(f);
#pragma unroll
    for (int k = 0; k < 8; ++k) { acc0[k] *= f0; acc1[k] *= f1; }
#pragma unroll
    for (int k = 0; k < 8; ++k) { swap32(acc0[k], acc1[k]); acc0[k] += acc1[k]; }
#pragma unroll
    for (int k = 0; k < 4; ++k) { swap16(acc0[k], acc0[k + 4]); acc0[k] += acc0[k + 4]; }
    *reinterpret_cast<f32x4*>(ldsf + L_RED + w * 256 + (r >> 1) * 128 + c * 8 + (r & 1) * 4) = f32x4{acc0[0], acc0[1], acc0[2], acc0[3]};
    if (lane == 0 || lane == 2) { ldsf[L_ML + w * 4 + lane] = mw; ldsf[L_ML + w * 4 + lane + 1] = lr; }
  }
  __syncthreads();
  gu64* p1 = (gu64*)(ws + WS_P1);
  if (tid < 256) {
    const int h = tid >> 7;
    float M = -INFINITY;
#pragma unroll
    for (int k = 0; k < 8; ++k) M = fmaxf(M, ldsf[L_ML + k * 4 + h * 2]);
    float o = 0.f, L = 0.f;
#pragma unroll
    for (int k = 0; k < 8; ++k) {
      const float f = __builtin_amdgcn_exp2f((ldsf[L_ML + k * 4 + h * 2] - M) * LOG2E);
      o = fmaf(f, ldsf[L_RED + k * 256 + tid], o); L = fmaf(f, ldsf[L_ML + k * 4 + h * 2 + 1], L);
    }
    gu64* mine = p1 + (size_t)(kvh * 16 + ii) * P1_STRIDE;
    st_gran(mine + tid, tag, o);
    if ((tid & 127) < 2) st_gran(mine + 256 + h * 2 + (tid & 1), tag, (tid & 1) ? L : M);
  }

  if (ii < 9) {
    const int bb = w >> 2, h = (w >> 1) & 1, idx = (w & 3) * 64 + lane;
    const gu64* src = p1 + (size_t)(kvh * 16 + bb * 8) * P1_STRIDE;
    float av[8]; float ml = 0.f;
    for (u32 spins = 0;; ++spins) {
      bool ok = true;
#pragma unroll
      for (int k = 0; k < 8; ++k) {
        const u64 v = __hip_atomic_load(src + k * P1_STRIDE + idx, RLX_AGENT);
        av[k] = __builtin_bit_cast(float, (u32)v); ok &= (u32)(v >> 32) == tag;
      }
      { const u64 v = __hip_atomic_load(src + ((lane >> 1) & 7) * P1_STRIDE + 256 + h * 2 + (lane & 1), RLX_AGENT);
        ml = __builtin_bit_cast(float, (u32)v); ok &= (u32)(v >> 32) == tag; }
      if (__all(ok) || spins > SPIN_CAP) break;
      __builtin_amdgcn_s_sleep(8);
    }
    const float mv = dpp<0xA0>(ml);
    float M = fmaxf(mv, dpp<DPP_XOR2>(mv)); M = fmaxf(M, dpp<DPP_ROR4>(M)); M = fmaxf(M, dpp<DPP_ROR8>(M));
    const float f = __builtin_amdgcn_exp2f((mv - M) * LOG2E);
    float L = f * dpp<0xF5>(ml);
    L += dpp<DPP_XOR2>(L); L += dpp<DPP_ROR4>(L); L += dpp<DPP_ROR8>(L);
    float o = av[0] * dpp<DPP_BC + 0>(f);
    o = fmaf(av[1], dpp<DPP_BC + 2>(f), o); o = fmaf(av[2], dpp<DPP_BC + 4>(f), o); o = fmaf(av[3], dpp<DPP_BC + 6>(f), o);
    o = fmaf(av[4], dpp<DPP_BC + 8>(f), o); o = fmaf(av[5], dpp<DPP_BC + 10>(f), o); o = fmaf(av[6], dpp<DPP_BC + 12>(f), o);
    o = fmaf(av[7], dpp<DPP_BC + 14>(f), o);
    reinterpret_cast<u16*>(lds + L_H)[tid] = (u16)f2bf(o / L);
    __syncthreads();
    float a0[8], a1[8];
#pragma unroll
    for (int k = 0; k < 8; ++k) { a0[k] = 0.f; a1[k] = 0.f; }
    const u32* op = lds + L_H + 16 * w + 2 * (lane >> 3);
    pair_mac2(wov[0], wov[1], op[0], op[128], a0, a1);
    pair_mac2(wov[2], wov[3], op[1], op[129], a0, a1);
    const float s0 = rs8_hi(a0, lane), s1 = rs8_hi(a1, lane);
    const int col = (lane & 7) * 8 + (lane >> 3);
    ldsf[L_RED + 2048 + w * 128 + col] = s0; ldsf[L_RED + 2048 + w * 128 + 64 + col] = s1;
    __syncthreads();
    if (tid < 128) {
      float a = 0.f;
#pragma unroll
      for (int k = 0; k < 8; ++k) a += ldsf[L_RED + 2048 + k * 128 + tid];
      st_gran((gu64*)(ws + WS_P2) + (size_t)(ii * 16 + kvh) * 128 + tid, tag, a);
    }
  }

  if (is_red) {
    const int kq = tid >> 7, o = tid & 127;
    const gu64* fs = (const gu64*)(ws + WS_PF) + ((size_t)kvh * NFT + kq * 18) * 128 + o;
    float fsum;
    for (u32 spins = 0;; ++spins) {
      bool ok = true; fsum = 0.f;
#pragma unroll
      for (int k = 0; k < 18; ++k) {
        const u64 v = __hip_atomic_load(fs + k * 128, RLX_AGENT);
        fsum += __builtin_bit_cast(float, (u32)v); ok &= (u32)(v >> 32) == tag;
      }
      if (__all(ok) || spins > SPIN_CAP) break;
      __builtin_amdgcn_s_sleep(8);
    }
    ldsf[L_RED + 4096 + tid] = fsum;
    __syncthreads();
    if (tid < 128) {
      const int oi = (tid >> 6) * DM + kvh * 64 + (tid & 63);
      const float xv = bf2f(x[oi]);
      const float ffn = rbf((ldsf[L_RED + 4096 + tid] + ldsf[L_RED + 4096 + 128 + tid]) + (ldsf[L_RED + 4096 + 256 + tid] + ldsf[L_RED + 4096 + 384 + tid]));
      const gu64* ys = (const gu64*)(ws + WS_P2) + (size_t)kvh * 16 * 128 + tid;
      float ysum;
      for (u32 spins = 0;; ++spins) {
        bool ok = true; ysum = 0.f;
#pragma unroll
        for (int k = 0; k < 16; ++k) {
          const u64 v = __hip_atomic_load(ys + k * 128, RLX_AGENT);
          ysum += __builtin_bit_cast(float, (u32)v); ok &= (u32)(v >> 32) == tag;
        }
        if (__all(ok) || spins > SPIN_CAP) break;
        __builtin_amdgcn_s_sleep(4);
      }
      out[oi] = (u16)f2bf(rbf(xv + rbf(ysum)) + ffn);
      if (kvh == 0 && tid == 0) __hip_atomic_store((gu32*)(ws + WS_EPOCH), tag, RLX_AGENT);
    }
  }
}

extern "C" void kernel_launch(void* const* d_in, const int* in_sizes, int n_in, void* d_out, int out_size, void* d_ws, size_t ws_size,
                              hipStream_t stream) {
  (void)in_sizes; (void)n_in; (void)out_size; (void)ws_size;
  hipLaunchKernelGGL(fused_decode_kernel, dim3(256), dim3(NTHR), 0, stream, (const u16*)d_in[0], (const u16*)d_in[1], (const u16*)d_in[2],
                     (const u16*)d_in[3], (const u16*)d_in[4], (const u16*)d_in[6], (const u16*)d_in[7], (const u16*)d_in[8], (u16*)d_out,
                     (unsigned char*)d_ws);
}
```

```cpp
#define NSTAT 48
#include <hip/hip_runtime.h>
#include <hip/hip_bf16.h>
#include <stdint.h>

typedef unsigned u32;
typedef unsigned short u16;
typedef unsigned long long u64;
typedef u32 u32x4 __attribute__((ext_vector_type(4)));
typedef float f32x4 __attribute__((ext_vector_type(4)));
typedef __bf16 bf16x2 __attribute__((ext_vector_type(2)));
#define GAS __attribute__((address_space(1)))
#define LAS __attribute__((address_space(3)))
typedef GAS u64 gu64;
typedef GAS u32 gu32;
#define RLX_AGENT __ATOMIC_RELAXED, __HIP_MEMORY_SCOPE_AGENT
#define SB __builtin_amdgcn_sched_barrier(0)

constexpr int DM = 576, SK = 32768, DH = 128, FF = 4608, NQ = 4096;
constexpr int NSW = 4, NTHR = (NSW + 1) * 64, U = 4;
constexpr int TB = U * 1024;
constexpr int NT = SK / 16;
constexpr int RT = NT / 8;
#define THR 4
#define WQ_AUX 2
#define ABL_WO 9
#define ABL_WQ (DM * NQ * 2)
#define ABL_PF (9 * NFT * 1024)
#define ABL_GU (DM * FF * 2)
#define NE 4
constexpr int NS = NSTAT;
constexpr int NPOOL = (RT - NS * NSW) * 8;
constexpr int NFT = 72;
constexpr int AUX_NT = 2, AUX_SC1 = 16;
constexpr float LOG2E = 1.4426950408889634f;
constexpr u32 SPIN_CAP = 1u << 16;

constexpr size_t WS_EPOCH = 0;
constexpr size_t WS_CTR = 4096;
constexpr size_t WS_QG = WS_CTR + 32 * 256;
constexpr size_t WS_P1 = WS_QG + 2 * 2048 * 8;
constexpr int P1_STRIDE = 264;
constexpr size_t WS_P2 = WS_P1 + (size_t)256 * P1_STRIDE * 8;
constexpr size_t WS_PF = WS_P2 + (size_t)9 * 16 * 128 * 8;
constexpr size_t WS_END = WS_PF + (size_t)9 * NFT * 128 * 8;

__device__ __forceinline__ float bf2f(u32 lo16) { return __uint_as_float(lo16 << 16); }
__device__ __forceinline__ float rbf(float f) { return (float)(__bf16)f; }
__device__ __forceinline__ u32 f2bf(float f) { const __bf16 h = (__bf16)f; return (u32)__builtin_bit_cast(u16, h); }
__device__ __forceinline__ float dot2(u32 a, u32 b, float c) {
  return __builtin_amdgcn_fdot2_f32_bf16(__builtin_bit_cast(bf16x2, a), __builtin_bit_cast(bf16x2, b), c, false);
}
template <int CTRL> __device__ __forceinline__ float dpp(float x) {
  return __int_as_float(__builtin_amdgcn_update_dpp(0, __float_as_int(x), CTRL, 0xf, 0xf, true));
}
constexpr int DPP_XOR1 = 0xB1, DPP_XOR2 = 0x4E, DPP_HMIR = 0x141, DPP_ROR8 = 0x128, DPP_ROR4 = 0x124, DPP_BC = 0x150;
__device__ __forceinline__ void swap32(float& a, float& b) {
  auto r = __builtin_amdgcn_permlane32_swap(__float_as_uint(a), __float_as_uint(b), false, false);
  a = __uint_as_float(r[0]); b = __uint_as_float(r[1]);
}
__device__ __forceinline__ void swap16(float& a, float& b) {
  auto r = __builtin_amdgcn_permlane16_swap(__float_as_uint(a), __float_as_uint(b), false, false);
  a = __uint_as_float(r[0]); b = __uint_as_float(r[1]);
}
__device__ __forceinline__ float half_sum(float x) {
  x += dpp<DPP_XOR1>(x); x += dpp<DPP_XOR2>(x); x += dpp<DPP_ROR4>(x); x += dpp<DPP_ROR8>(x);
  float a = x, b = x; swap16(a, b); return a + b;
}
__device__ __forceinline__ __amdgpu_buffer_rsrc_t mkrs(const void* p, u32 bytes) {
  return __builtin_amdgcn_make_buffer_rsrc(const_cast<void*>(p), 0, bytes, 0x00020000);
}
template <int AUX> __device__ __forceinline__ u32x4 bld(__amdgpu_buffer_rsrc_t rs, int voff, int soff) {
  return __builtin_amdgcn_raw_buffer_load_b128(rs, voff, soff, AUX);
}
__device__ __forceinline__ void pair_mac2(const u32x4& a, const u32x4& b, u32 xp0, u32 xp1, float* acc0, float* acc1) {
#pragma unroll
  for (int d = 0; d < 4; ++d) {
    const u32 lo = __builtin_amdgcn_perm(b[d], a[d], 0x05040100u), hi = __builtin_amdgcn_perm(b[d], a[d], 0x07060302u);
    acc0[2 * d] = dot2(lo, xp0, acc0[2 * d]); acc0[2 * d + 1] = dot2(hi, xp0, acc0[2 * d + 1]);
    acc1[2 * d] = dot2(lo, xp1, acc1[2 * d]); acc1[2 * d + 1] = dot2(hi, xp1, acc1[2 * d + 1]);
  }
}
__device__ __forceinline__ float rs8_hi(float* v, int lane) {
#pragma unroll
  for (int k = 0; k < 4; ++k) { swap32(v[k], v[k + 4]); v[k] += v[k + 4]; }
#pragma unroll
  for (int k = 0; k < 2; ++k) { swap16(v[k], v[k + 2]); v[k] += v[k + 2]; }
  const bool up = lane & 8;
  const float keep = up ? v[1] : v[0], send = up ? v[0] : v[1];
  return keep + dpp<DPP_ROR8>(send);
}
__device__ __forceinline__ void pin8(float* a) {
  asm volatile("" : "+v"(a[0]), "+v"(a[1]), "+v"(a[2]), "+v"(a[3]), "+v"(a[4]), "+v"(a[5]), "+v"(a[6]), "+v"(a[7]));
}
__device__ __forceinline__ void st_gran(gu64* g, u32 tag, u32 v) { __hip_atomic_store(g, ((u64)tag << 32) | (u64)v, RLX_AGENT); }

#define STAMP(k) do {} while (0)

constexpr int L_XP = 0;
constexpr int L_Q = L_XP + 576;
constexpr int L_FLAG = L_Q + 128;
constexpr int L_H = L_FLAG + 4;
constexpr int L_O = L_H + 64;
constexpr int L_ML = L_O + 256;
constexpr int L_RED = L_ML + 16;
constexpr int L_RED2 = L_RED + 1024;
constexpr int L_FO = L_RED2 + 512;
constexpr int L_STASH = L_FO + 1152;
constexpr int L_TOTAL = L_STASH + NSW * NE * 2048;

__global__ void __launch_bounds__(NTHR, 2)
fused_decode_kernel(const u16* __restrict__ x, const u16* __restrict__ kc, const u16* __restrict__ vc, const u16* __restrict__ wq,
                    const u16* __restrict__ wo, const u16* __restrict__ wg, const u16* __restrict__ wu, const u16* __restrict__ wd,
                    u16* __restrict__ out, unsigned char* __restrict__ ws) {
  __shared__ __attribute__((aligned(16))) u32 lds[L_TOTAL];
  float* ldsf = reinterpret_cast<float*>(lds);
  const int tid = (int)threadIdx.x - 64, lane = threadIdx.x & 63;
  const int w = __builtin_amdgcn_readfirstlane((int)(threadIdx.x >> 6)) - 1;
  const int bid = blockIdx.x;
  const int g = bid >> 3, s = bid & 7, b = g >> 4, kvh = g & 15;
  const int ii = b * 8 + s;

  const __amdgpu_buffer_rsrc_t rs_k = mkrs(kc + (size_t)g * SK * DH, SK * DH * 2);
  const __amdgpu_buffer_rsrc_t rs_v = mkrs(vc + (size_t)g * SK * DH, SK * DH * 2);
  const int kv_vo = lane * 16;
  const int t0 = s * RT + w;
  const int hf = lane >> 5, l5 = lane & 31;
  const int qs = s * 32 + g;
  u32x4 KA[U], VA[U];
#define ISSUE_K(BUF, T) _Pragma("unroll") for (int i_ = 0; i_ < U; ++i_) BUF[i_] = bld<AUX_NT>(rs_k, kv_vo + i_ * 1024, (T) * TB); SB
#define ISSUE_V(BUF, T) _Pragma("unroll") for (int i_ = 0; i_ < U; ++i_) BUF[i_] = bld<AUX_NT>(rs_v, kv_vo + i_ * 1024, (T) * TB); SB
  if (w == 0) STAMP(0);
  { const int tf = w < 0 ? NT : t0; ISSUE_K(KA, tf); ISSUE_V(VA, tf); }
  volatile LAS u32* lflag = (volatile LAS u32*)(lds + L_FLAG);
  if (threadIdx.x == 0) lflag[1] = 0u;
  asm volatile("; ROLE SPLIT 1" ::: "memory");
  if (w < 0) {
    __builtin_amdgcn_s_setprio(3);
    u32x4 wqv[18];
    u32 xr[9];
    const u32* xw = reinterpret_cast<const u32*>(x) + hf * 288 + l5;
#pragma unroll
    for (int j = 0; j < 9; ++j) xr[j] = xw[j * 32];
    const u32 epoch_v = __hip_atomic_load((gu32*)(ws + WS_EPOCH), RLX_AGENT);
    const __amdgpu_buffer_rsrc_t rs_wq = mkrs(wq, ABL_WQ);
    const int vo = (lane >> 1) * 18 * (NQ * 2) + (lane & 1) * 16;
#pragma unroll
    for (int j = 0; j < 18; ++j) wqv[j] = bld<WQ_AUX>(rs_wq, vo, qs * 32 + j * (NQ * 2));
    SB;
    __syncthreads();
    {
      float v[18]; float sum = 0.f;
#pragma unroll
      for (int j = 0; j < 9; ++j) { v[2 * j] = bf2f(xr[j] & 0xffffu); v[2 * j + 1] = bf2f(xr[j] >> 16); sum += v[2 * j] + v[2 * j + 1]; }
      const float mean = half_sum(sum) * (1.0f / DM);
      float sq = 0.f;
#pragma unroll
      for (int j = 0; j < 18; ++j) { const float d = v[j] - mean; sq = fmaf(d, d, sq); }
      const float var = half_sum(sq) * (1.0f / DM);
      const float mb = rbf(mean), rsd = 1.0f / rbf(sqrtf(rbf(rbf(var) + 1e-5f)));
#pragma unroll
      for (int j = 0; j < 9; ++j)
        lds[L_XP + hf * 288 + j * 32 + l5] = f2bf(rbf(v[2 * j] - mb) * rsd) | (f2bf(rbf(v[2 * j + 1] - mb) * rsd) << 16);
    }
    __builtin_amdgcn_fence(__ATOMIC_RELEASE, "wavefront"); __builtin_amdgcn_wave_barrier(); __builtin_amdgcn_fence(__ATOMIC_ACQUIRE, "wavefront");
    const u32 tag = __builtin_amdgcn_readfirstlane(epoch_v) + 0x9E3779B9u;
    const u32 ctag = tag & 0xfffff000u;
    STAMP(9);
    float a0[8], a1[8];
#pragma unroll
    for (int k = 0; k < 8; ++k) { a0[k] = 0.f; a1[k] = 0.f; }
    {
      const u32* xp = lds + L_XP + (lane >> 1) * 9;
#pragma unroll
      for (int j = 0; j < 9; ++j) pair_mac2(wqv[2 * j], wqv[2 * j + 1], xp[j], xp[288 + j], a0, a1);
      float s0 = rs8_hi(a0, lane), s1 = rs8_hi(a1, lane);
      const bool up = lane & 4;
      const float keep = up ? s1 : s0, send = up ? s0 : s1;
      float qv = keep + dpp<0x1B>(dpp<DPP_HMIR>(send));
      qv += dpp<DPP_XOR2>(qv);
      const u32 qb = f2bf(qv);
      const u32 other = (u32)__builtin_amdgcn_update_dpp(0, (int)qb, DPP_ROR8, 0xf, 0xf, true);
      if ((lane & 0xA) == 0)
        st_gran((gu64*)(ws + WS_QG) + ((lane >> 2) & 1) * 2048 + qs * 8 + (lane & 1) * 4 + (lane >> 4), tag, qb | (other << 16));
    }
    if (s == 0 && lane == 0) __hip_atomic_store((gu32*)(ws + WS_CTR + g * 256), ctag, RLX_AGENT);
    STAMP(10);
    {
      const gu64* qsrc = (const gu64*)(ws + WS_QG) + b * 2048 + kvh * 128 + lane;
      u32 qa, qb;
      for (u32 spins = 0;; ++spins) {
        const u64 v0 = __hip_atomic_load(qsrc, RLX_AGENT), v1 = __hip_atomic_load(qsrc + 64, RLX_AGENT);
        const u32 cw = __hip_atomic_load((const gu32*)(ws + WS_CTR + g * 256), RLX_AGENT);
        qa = (u32)v0; qb = (u32)v1;
        const bool ok = (u32)(v0 >> 32) == tag && (u32)(v1 >> 32) == tag && (cw & 0xfffff000u) == ctag;
        if (__all(ok) || spins > SPIN_CAP) break;
        __builtin_amdgcn_s_sleep(1);
      }
      lds[L_Q + lane] = qa; lds[L_Q + 64 + lane] = qb;
      lds[L_FLAG] = tag;
      __builtin_amdgcn_fence(__ATOMIC_RELEASE, "workgroup");
      lflag[1] = 1u;
    }
  }
  asm volatile("; ROLE SPLIT 2" ::: "memory");

  if (w >= 0) {
    u32x4 KB[U], VB[U], wov[8];
#define WAITV(N) __builtin_amdgcn_s_waitcnt(0x0f70 | ((N) & 15) | (((N) >> 4) << 14)); SB
    __syncthreads();
    u32x4* stash = reinterpret_cast<u32x4*>(lds + L_STASH + w * NE * 2048) + lane;
    int j = 1, ne = 0;
#pragma unroll 1
    while (ne < NE && lflag[1] == 0u) {
      WAITV(4);
#pragma unroll
      for (int i = 0; i < U; ++i) stash[ne * 512 + i * 64] = KA[i];
      SB; ISSUE_K(KA, t0 + j * NSW);
      WAITV(4);
#pragma unroll
      for (int i = 0; i < U; ++i) stash[ne * 512 + 256 + i * 64] = VA[i];
      SB; ISSUE_V(VA, t0 + j * NSW);
      ++ne; ++j;
    }
    if (w == 0) STAMP(1);
    while (lflag[1] == 0u) __builtin_amdgcn_s_sleep(1);
    __builtin_amdgcn_fence(__ATOMIC_ACQUIRE, "workgroup");
    if (w == 0) STAMP(2);
    const u32 tag = __builtin_amdgcn_readfirstlane(lds[L_FLAG]);
    const int r = lane >> 4, c = lane & 15;
    const u32x4 q0 = *reinterpret_cast<const u32x4*>(lds + L_Q + c * 4);
    const u32x4 q1 = *reinterpret_cast<const u32x4*>(lds + L_Q + 64 + c * 4);

    float acc0[8], acc1[8];
#pragma unroll
    for (int k = 0; k < 8; ++k) { acc0[k] = 0.f; acc1[k] = 0.f; }
    float mrun = -INFINITY, lrun = 0.f, p;
    auto qk = [&](const u32x4* Kt) {
      float xv[8];
#pragma unroll
      for (int i = 0; i < U; ++i) {
        float d0 = 0.f, d1 = 0.f;
#pragma unroll
        for (int k = 0; k < 4; ++k) { d0 = dot2(Kt[i][k], q0[k], d0); d1 = dot2(Kt[i][k], q1[k], d1); }
        xv[2 * i] = d0; xv[2 * i + 1] = d1;
      }
#pragma unroll
      for (int k = 0; k < 4; ++k) { const bool up = c & 8; const float keep = up ? xv[k + 4] : xv[k], send = up ? xv[k] : xv[k + 4]; xv[k] = keep + dpp<DPP_ROR8>(send); }
#pragma unroll
      for (int k = 0; k < 2; ++k) { const bool up = c & 4; const float keep = up ? xv[k + 2] : xv[k], send = up ? xv[k] : xv[k + 2]; xv[k] = keep + dpp<DPP_HMIR>(send); }
      { const bool up = c & 2; const float keep = up ? xv[1] : xv[0], send = up ? xv[0] : xv[1]; xv[0] = keep + dpp<DPP_XOR2>(send); }
      float sc = xv[0] + dpp<DPP_XOR1>(xv[0]);
      sc = rbf(rbf(sc) * 0.08838834764831845f);
      float pm = fmaxf(sc, dpp<DPP_ROR4>(sc)); pm = fmaxf(pm, dpp<DPP_ROR8>(pm));
      if (__any(pm > mrun)) {
        const float mn = fmaxf(mrun, pm);
        const float al = __builtin_amdgcn_exp2f((mrun - mn) * LOG2E);
        mrun = mn; lrun *= al;
        const float al0 = dpp<DPP_BC + 0>(al), al1 = dpp<DPP_BC + 2>(al);
#pragma unroll
        for (int k = 0; k < 8; ++k) { acc0[k] *= al0; acc1[k] *= al1; }
      }
      p = __builtin_amdgcn_exp2f((sc - mrun) * LOG2E);
      lrun += p;
    };
    auto pv1 = [&](const u32x4& Vt, float p0, float p1) {
#pragma unroll
      for (int k = 0; k < 4; ++k) {
        const float lo = __uint_as_float(Vt[k] << 16), hi = __uint_as_float(Vt[k] & 0xffff0000u);
        acc0[2 * k] = fmaf(p0, lo, acc0[2 * k]); acc0[2 * k + 1] = fmaf(p0, hi, acc0[2 * k + 1]);
        acc1[2 * k] = fmaf(p1, lo, acc1[2 * k]); acc1[2 * k + 1] = fmaf(p1, hi, acc1[2 * k + 1]);
      }
    };
    auto pv = [&](const u32x4* Vt) {
      pv1(Vt[0], dpp<DPP_BC + 0>(p), dpp<DPP_BC + 2>(p));
      pv1(Vt[1], dpp<DPP_BC + 4>(p), dpp<DPP_BC + 6>(p));
      pv1(Vt[2], dpp<DPP_BC + 8>(p), dpp<DPP_BC + 10>(p));
      pv1(Vt[3], dpp<DPP_BC + 12>(p), dpp<DPP_BC + 14>(p));
      pin8(acc0); pin8(acc1);
    };
    const int cvo = lane * 4;
    int claim = 0, ia = 0, ib;
    {
      const __amdgpu_buffer_rsrc_t rs_wo = mkrs(wo + (size_t)kvh * 256 * DM, ii < ABL_WO ? 256 * DM * 2 : 0);
      const int vo = (lane >> 3) * 8 * (DM * 2) + (lane & 7) * 16, so = 64 * w * (DM * 2) + ii * 128;
#pragma unroll
      for (int k = 0; k < 8; ++k) wov[k] = bld<AUX_NT>(rs_wo, vo, so + k * (DM * 2));
      SB;
    }
#define NEXT_TILE(CUR, NXT) { const int pi_ = __builtin_amdgcn_readfirstlane(claim) & 0xfff; \
      NXT = j < NS ? t0 + j * NSW : ((CUR) < NT && pi_ < NPOOL ? (pi_ & 7) * RT + NS * NSW + (pi_ >> 3) : NT); ++j; \
      claim = __builtin_amdgcn_raw_ptr_buffer_atomic_add_i32(1, mkrs(ws + WS_CTR + g * 256, (j >= NS && NXT < NT) ? 4 : 0), cvo, 0, 0); SB; }
    do {
      WAITV(4); NEXT_TILE(ia, ib); ISSUE_K(KB, ib); qk(KA); SB;
      WAITV(5); ISSUE_V(VB, ib); pv(VA); SB;
      if (ne > 0) {
        --ne;
        u32x4 KE[U], VE[U];
#pragma unroll
        for (int i = 0; i < U; ++i) { KE[i] = stash[ne * 512 + i * 64]; VE[i] = stash[ne * 512 + 256 + i * 64]; }
        qk(KE); pv(VE); SB;
      }
      WAITV(4); NEXT_TILE(ib, ia); ISSUE_K(KA, ia); if (ib < NT) qk(KB); SB;
      WAITV(5); ISSUE_V(VA, ia); if (ib < NT) pv(VB); SB;
    } while (ia < NT);
    if (w == 0) STAMP(5);

    {
      float lr = lrun + dpp<DPP_ROR4>(lrun); lr += dpp<DPP_ROR8>(lr);
      float a = mrun, bb = mrun; swap16(a, bb); float mw = fmaxf(a, bb);
      a = mw; bb = mw; swap32(a, bb); mw = fmaxf(a, bb);
      const float f = __builtin_amdgcn_exp2f((mrun - mw) * LOG2E);
      lr *= f;
      a = lr; bb = lr; swap16(a, bb); lr = a + bb;
      a = lr; bb = lr; swap32(a, bb); lr = a + bb;
      const float f0 = dpp<DPP_BC + 0>(f), f1 = dpp<DPP_BC + 2>(f);
#pragma unroll
      for (int k = 0; k < 8; ++k) { acc0[k] *= f0; acc1[k] *= f1; }
#pragma unroll
      for (int k = 0; k < 8; ++k) { swap32(acc0[k], acc1[k]); acc0[k] += acc1[k]; }
#pragma unroll
      for (int k = 0; k < 4; ++k) { swap16(acc0[k], acc0[k + 4]); acc0[k] += acc0[k + 4]; }
      *reinterpret_cast<f32x4*>(ldsf + L_RED + w * 256 + (r >> 1) * 128 + c * 8 + (r & 1) * 4) = f32x4{acc0[0], acc0[1], acc0[2], acc0[3]};
      if (lane == 0 || lane == 2) { ldsf[L_ML + w * 4 + lane] = mw; ldsf[L_ML + w * 4 + lane + 1] = lr; }
    }
    __syncthreads();
    gu64* p1 = (gu64*)(ws + WS_P1);
    const int h = w >> 1;
    {
      float M = -INFINITY;
#pragma unroll
      for (int k = 0; k < NSW; ++k) M = fmaxf(M, ldsf[L_ML + k * 4 + h * 2]);
      float o = 0.f, L = 0.f;
#pragma unroll
      for (int k = 0; k < NSW; ++k) {
        const float f = __builtin_amdgcn_exp2f((ldsf[L_ML + k * 4 + h * 2] - M) * LOG2E);
        o = fmaf(f, ldsf[L_RED + k * 256 + tid], o); L = fmaf(f, ldsf[L_ML + k * 4 + h * 2 + 1], L);
      }
      gu64* mine = p1 + (size_t)(kvh * 16 + ii) * P1_STRIDE;
      st_gran(mine + tid, tag, __float_as_uint(o));
      if ((tid & 127) < 2) st_gran(mine + 256 + h * 2 + (tid & 1), tag, __float_as_uint((tid & 1) ? L : M));
    }
    if (w == 0) STAMP(6);
    if (ii >= 9) return;

    {
      const gu64* src = p1 + (size_t)(kvh * 16) * P1_STRIDE;
      float av[16], ml[2];
      for (u32 spins = 0;; ++spins) {
        bool ok = true;
#pragma unroll
        for (int k = 0; k < 16; ++k) {
          const u64 v = __hip_atomic_load(src + k * P1_STRIDE + tid, RLX_AGENT);
          av[k] = __uint_as_float((u32)v); ok &= (u32)(v >> 32) == tag;
        }
#pragma unroll
        for (int bb = 0; bb < 2; ++bb) {
          const u64 v = __hip_atomic_load(src + (bb * 8 + (c >> 1)) * P1_STRIDE + 256 + h * 2 + (c & 1), RLX_AGENT);
          ml[bb] = __uint_as_float((u32)v); ok &= (u32)(v >> 32) == tag;
        }
        if (__all(ok) || spins > SPIN_CAP) break;
        __builtin_amdgcn_s_sleep(4);
      }
      if (w == 0) STAMP(7);
#pragma unroll
      for (int bb = 0; bb < 2; ++bb) {
        const float mv = dpp<0xA0>(ml[bb]);
        float M = fmaxf(mv, dpp<DPP_XOR2>(mv)); M = fmaxf(M, dpp<DPP_ROR4>(M)); M = fmaxf(M, dpp<DPP_ROR8>(M));
        const float f = __builtin_amdgcn_exp2f((mv - M) * LOG2E);
        float L = f * dpp<0xF5>(ml[bb]);
        L += dpp<DPP_XOR2>(L); L += dpp<DPP_ROR4>(L); L += dpp<DPP_ROR8>(L);
        const float* a = av + bb * 8;
        float o = a[0] * dpp<DPP_BC + 0>(f);
        o = fmaf(a[1], dpp<DPP_BC + 2>(f), o); o = fmaf(a[2], dpp<DPP_BC + 4>(f), o); o = fmaf(a[3], dpp<DPP_BC + 6>(f), o);
        o = fmaf(a[4], dpp<DPP_BC + 8>(f), o); o = fmaf(a[5], dpp<DPP_BC + 10>(f), o); o = fmaf(a[6], dpp<DPP_BC + 12>(f), o);
        o = fmaf(a[7], dpp<DPP_BC + 14>(f), o);
        reinterpret_cast<u16*>(lds + L_O)[bb * 256 + tid] = (u16)f2bf(o / L);
      }
      __builtin_amdgcn_fence(__ATOMIC_RELEASE, "wavefront"); __builtin_amdgcn_wave_barrier(); __builtin_amdgcn_fence(__ATOMIC_ACQUIRE, "wavefront");
      float a0[8], a1[8];
#pragma unroll
      for (int k = 0; k < 8; ++k) { a0[k] = 0.f; a1[k] = 0.f; }
      const u32x4 o0 = *reinterpret_cast<const u32x4*>(lds + L_O + 32 * w + 4 * (lane >> 3));
      const u32x4 o1 = *reinterpret_cast<const u32x4*>(lds + L_O + 128 + 32 * w + 4 * (lane >> 3));
#pragma unroll
      for (int k = 0; k < 4; ++k) pair_mac2(wov[2 * k], wov[2 * k + 1], o0[k], o1[k], a0, a1);
      const float s0 = rs8_hi(a0, lane), s1 = rs8_hi(a1, lane);
      const int col = (lane & 7) * 8 + (lane >> 3);
      ldsf[L_RED2 + w * 128 + col] = s0; ldsf[L_RED2 + w * 128 + 64 + col] = s1;
      __syncthreads();
      if (tid < 128) {
        const float a = (ldsf[L_RED2 + tid] + ldsf[L_RED2 + 128 + tid]) + (ldsf[L_RED2 + 256 + tid] + ldsf[L_RED2 + 384 + tid]);
        st_gran((gu64*)(ws + WS_P2) + (size_t)(ii * 16 + kvh) * 128 + tid, tag, __float_as_uint(a));
      }
      if (w == 0) STAMP(8);
    }
    return;
  }

  {
    const u32 tag = __builtin_amdgcn_readfirstlane(lds[L_FLAG]);
    float a0[8], a1[8];
    STAMP(11);

    int task = -1;
    if ((s & 3) == (g & 3)) task = g * 2 + (s >> 2);
    else if ((g >> 2) == s && (g & 3) == ((s + 1) & 3)) task = 64 + s;
    if (task >= 0) {
      const int rr = lane >> 3, cc = lane & 7;
      const int vo = rr * 72 * (FF * 2) + cc * 16;
      const u32* xp = lds + L_XP + rr * 36;
      u32x4 A[8], Bf[8];
      float c0[8], c1[8];
      const __amdgpu_buffer_rsrc_t rs_g = mkrs(wg, ABL_GU), rs_u = mkrs(wu, ABL_GU);
      const __amdgpu_buffer_rsrc_t rs_d = mkrs(wd + (size_t)task * 64 * DM, 64 * DM * 2);
#define FISSUE(BUF, RS, KB) { const int so_ = (KB) < 9 ? task * 128 + (KB) * 8 * (FF * 2) : DM * FF * 2; \
      _Pragma("unroll") for (int j_ = 0; j_ < 8; ++j_) BUF[j_] = bld<AUX_NT>(RS, vo, so_ + j_ * (FF * 2)); } SB
#define MAC4(BUF, X0, X1, C0, C1) { _Pragma("unroll") for (int j_ = 0; j_ < 4; ++j_) { pair_mac2(BUF[2 * j_], BUF[2 * j_ + 1], X0[j_], X1[j_], C0, C1); pin8(C0); pin8(C1); SB; } }
#define FZERO(C0, C1) _Pragma("unroll") for (int k_ = 0; k_ < 8; ++k_) { C0[k_] = 0.f; C1[k_] = 0.f; }
#define DISSUE(BUF, R0) _Pragma("unroll") for (int j_ = 0; j_ < 8; ++j_) BUF[j_] = bld<AUX_NT>(rs_d, lane * 16, ((R0) + j_) * (DM * 2)); SB
      FISSUE(A, rs_g, 0); FISSUE(Bf, rs_u, 0);
      FZERO(a0, a1); FZERO(c0, c1);
#pragma unroll 1
      for (int k = 0; k < 9; ++k) {
        const u32x4 x0 = *reinterpret_cast<const u32x4*>(xp + 4 * k), x1 = *reinterpret_cast<const u32x4*>(xp + 288 + 4 * k);
        MAC4(A, x0, x1, a0, a1); FISSUE(A, rs_g, k + 1);
        MAC4(Bf, x0, x1, c0, c1); FISSUE(Bf, rs_u, k + 1);
      }
      DISSUE(A, 0); DISSUE(Bf, 8);
      {
        const float gs0 = rs8_hi(a0, lane), gs1 = rs8_hi(a1, lane), us0 = rs8_hi(c0, lane), us1 = rs8_hi(c1, lane);
        const int col = cc * 8 + rr;
        const float g0 = rbf(gs0), g1 = rbf(gs1);
        const float e0 = rbf(0.5f * g0 * (1.0f + tanhf(0.7978845608028654f * (g0 + 0.044715f * g0 * g0 * g0))));
        const float e1 = rbf(0.5f * g1 * (1.0f + tanhf(0.7978845608028654f * (g1 + 0.044715f * g1 * g1 * g1))));
        reinterpret_cast<u16*>(lds + L_H)[col] = (u16)f2bf(e0 * rbf(us0));
        reinterpret_cast<u16*>(lds + L_H)[64 + col] = (u16)f2bf(e1 * rbf(us1));
      }
      __builtin_amdgcn_fence(__ATOMIC_RELEASE, "wavefront"); __builtin_amdgcn_wave_barrier(); __builtin_amdgcn_fence(__ATOMIC_ACQUIRE, "wavefront");
      const u32* hp = lds + L_H;
      FZERO(a0, a1);
#pragma unroll 1
      for (int k = 0; k < 4; ++k) {
        const u32x4 h0 = *reinterpret_cast<const u32x4*>(hp + 8 * k), h1 = *reinterpret_cast<const u32x4*>(hp + 32 + 8 * k);
        const u32x4 h2 = *reinterpret_cast<const u32x4*>(hp + 8 * k + 4), h3 = *reinterpret_cast<const u32x4*>(hp + 32 + 8 * k + 4);
        MAC4(A, h0, h1, a0, a1); DISSUE(A, 16 * k + 16);
        MAC4(Bf, h2, h3, a0, a1); DISSUE(Bf, 16 * k + 24);
      }
#pragma unroll
      for (int j = 0; j < 8; ++j) A[j] = bld<AUX_NT>(rs_d, rr * 8 * (DM * 2) + 1024 + cc * 16, j * (DM * 2));
      SB;
      float* fo = ldsf + L_FO;
      *reinterpret_cast<f32x4*>(fo + rr * 128 + cc * 8) = f32x4{a0[0], a0[1], a0[2], a0[3]};
      *reinterpret_cast<f32x4*>(fo + rr * 128 + cc * 8 + 4) = f32x4{a0[4], a0[5], a0[6], a0[7]};
      *reinterpret_cast<f32x4*>(fo + rr * 128 + 64 + cc * 8) = f32x4{a1[0], a1[1], a1[2], a1[3]};
      *reinterpret_cast<f32x4*>(fo + rr * 128 + 64 + cc * 8 + 4) = f32x4{a1[4], a1[5], a1[6], a1[7]};
      FZERO(a0, a1);
      {
        const u32x4 h0 = *reinterpret_cast<const u32x4*>(hp + 4 * rr), h1 = *reinterpret_cast<const u32x4*>(hp + 32 + 4 * rr);
        MAC4(A, h0, h1, a0, a1);
      }
      const float e0 = rs8_hi(a0, lane), e1 = rs8_hi(a1, lane);
      fo[1024 + cc * 8 + rr] = e0; fo[1024 + 64 + cc * 8 + rr] = e1;
      __builtin_amdgcn_fence(__ATOMIC_RELEASE, "wavefront"); __builtin_amdgcn_wave_barrier(); __builtin_amdgcn_fence(__ATOMIC_ACQUIRE, "wavefront");
      const __amdgpu_buffer_rsrc_t rs_pf = mkrs(ws + WS_PF, ABL_PF);
#pragma unroll
      for (int k = 0; k < 9; ++k) {
        const float2 v = *reinterpret_cast<const float2*>(fo + k * 128 + lane * 2);
        __builtin_amdgcn_raw_buffer_store_b128(u32x4{__float_as_uint(v.x), tag, __float_as_uint(v.y), tag}, rs_pf, lane * 16, (k * NFT + task) * 1024, AUX_SC1);
      }
      STAMP(12);
    }

    const bool is_red = (ii == 15 && kvh < 9);
    float f0 = 0.f, f1 = 0.f; u32 xpair = 0;
    if (is_red) {
      xpair = reinterpret_cast<const u32*>(x)[hf * 288 + kvh * 32 + l5];
      const __amdgpu_buffer_rsrc_t rs_f = mkrs(ws + WS_PF + (size_t)kvh * NFT * 1024, NFT * 1024);
      for (int k0 = 0; k0 < NFT; k0 += 12) {
        float p0, p1;
        for (u32 spins = 0;; ++spins) {
          bool ok = true; p0 = 0.f; p1 = 0.f;
          u32x4 v[12];
#pragma unroll
          for (int k = 0; k < 12; ++k) v[k] = bld<AUX_SC1>(rs_f, lane * 16, (k0 + k) * 1024);
#pragma unroll
          for (int k = 0; k < 12; ++k) { p0 += __uint_as_float(v[k][0]); p1 += __uint_as_float(v[k][2]); ok &= v[k][1] == tag && v[k][3] == tag; }
          if (__all(ok) || spins > SPIN_CAP) break;
          __builtin_amdgcn_s_sleep(64);
        }
        f0 += p0; f1 += p1;
      }
      STAMP(13);
    }
    __syncthreads();
    if (is_red) {
      const __amdgpu_buffer_rsrc_t rs_y = mkrs(ws + WS_P2 + (size_t)kvh * 16 * 1024, 16 * 1024);
      float y0, y1;
      for (u32 spins = 0;; ++spins) {
        bool ok = true; y0 = 0.f; y1 = 0.f;
        u32x4 v[16];
#pragma unroll
        for (int k = 0; k < 16; ++k) v[k] = bld<AUX_SC1>(rs_y, lane * 16, k * 1024);
#pragma unroll
        for (int k = 0; k < 16; ++k) { y0 += __uint_as_float(v[k][0]); y1 += __uint_as_float(v[k][2]); ok &= v[k][1] == tag && v[k][3] == tag; }
        if (__all(ok) || spins > SPIN_CAP) break;
        __builtin_amdgcn_s_sleep(2);
      }
      const u32 o0 = f2bf(rbf(bf2f(xpair & 0xffffu) + rbf(y0)) + rbf(f0)), o1 = f2bf(rbf(bf2f(xpair >> 16) + rbf(y1)) + rbf(f1));
      reinterpret_cast<u32*>(out)[hf * 288 + kvh * 32 + l5] = o0 | (o1 << 16);
      if (kvh == 0 && lane == 0) __hip_atomic_store((gu32*)(ws + WS_EPOCH), tag, RLX_AGENT);
      STAMP(14);
    }
  }
}

extern "C" void kernel_launch(void* const* d_in, const int* in_sizes, int n_in, void* d_out, int out_size, void* d_ws, size_t ws_size,
                              hipStream_t stream) {
  (void)in_sizes; (void)n_in; (void)out_size; (void)ws_size;
  hipLaunchKernelGGL(fused_decode_kernel, dim3(256), dim3(NTHR), 0, stream, (const u16*)d_in[0], (const u16*)d_in[1], (const u16*)d_in[2],
                     (const u16*)d_in[3], (const u16*)d_in[4], (const u16*)d_in[6], (const u16*)d_in[7], (const u16*)d_in[8], (u16*)d_out,
                     (unsigned char*)d_ws);
}
```

```cpp
#define STAMPS
#define NSTAT 48
#include <cstdio>
#include <hip/hip_runtime.h>
#include <hip/hip_bf16.h>
#include <stdint.h>

typedef unsigned u32;
typedef unsigned short u16;
typedef unsigned long long u64;
typedef u32 u32x4 __attribute__((ext_vector_type(4)));
typedef float f32x4 __attribute__((ext_vector_type(4)));
typedef __bf16 bf16x2 __attribute__((ext_vector_type(2)));
#define GAS __attribute__((address_space(1)))
#define LAS __attribute__((address_space(3)))
typedef GAS u64 gu64;
typedef GAS u32 gu32;
#define RLX_AGENT __ATOMIC_RELAXED, __HIP_MEMORY_SCOPE_AGENT
#define SB __builtin_amdgcn_sched_barrier(0)

constexpr int DM = 576, SK = 32768, DH = 128, FF = 4608, NQ = 4096;
constexpr int NSW = 4, NTHR = (NSW + 1) * 64, U = 4;
constexpr int TB = U * 1024;
constexpr int NT = SK / 16;
constexpr int RT = NT / 8;
#define THR 4
#define WQ_AUX 2
#define ABL_WO 9
#define ABL_WQ (DM * NQ * 2)
#define ABL_PF (9 * NFT * 1024)
#define ABL_GU (DM * FF * 2)
#define NE 4
constexpr int NS = NSTAT;
constexpr int NPOOL = (RT - NS * NSW) * 8;
constexpr int NFT = 72;
constexpr int AUX_NT = 2, AUX_SC1 = 16;
constexpr float LOG2E = 1.4426950408889634f;
constexpr u32 SPIN_CAP = 1u << 16;

constexpr size_t WS_EPOCH = 0;
constexpr size_t WS_CTR = 4096;
constexpr size_t WS_QG = WS_CTR + 32 * 256;
constexpr size_t WS_P1 = WS_QG + 2 * 2048 * 8;
constexpr int P1_STRIDE = 264;
constexpr size_t WS_P2 = WS_P1 + (size_t)256 * P1_STRIDE * 8;
constexpr size_t WS_PF = WS_P2 + (size_t)9 * 16 * 128 * 8;
constexpr size_t WS_END = WS_PF + (size_t)9 * NFT * 128 * 8;

__device__ __forceinline__ float bf2f(u32 lo16) { return __uint_as_float(lo16 << 16); }
__device__ __forceinline__ float rbf(float f) { return (float)(__bf16)f; }
__device__ __forceinline__ u32 f2bf(float f) { const __bf16 h = (__bf16)f; return (u32)__builtin_bit_cast(u16, h); }
__device__ __forceinline__ float dot2(u32 a, u32 b, float c) {
  return __builtin_amdgcn_fdot2_f32_bf16(__builtin_bit_cast(bf16x2, a), __builtin_bit_cast(bf16x2, b), c, false);
}
template <int CTRL> __device__ __forceinline__ float dpp(float x) {
  return __int_as_float(__builtin_amdgcn_update_dpp(0, __float_as_int(x), CTRL, 0xf, 0xf, true));
}
constexpr int DPP_XOR1 = 0xB1, DPP_XOR2 = 0x4E, DPP_HMIR = 0x141, DPP_ROR8 = 0x128, DPP_ROR4 = 0x124, DPP_BC = 0x150;
__device__ __forceinline__ void swap32(float& a, float& b) {
  auto r = __builtin_amdgcn_permlane32_swap(__float_as_uint(a), __float_as_uint(b), false, false);
  a = __uint_as_float(r[0]); b = __uint_as_float(r[1]);
}
__device__ __forceinline__ void swap16(float& a, float& b) {
  auto r = __builtin_amdgcn_permlane16_swap(__float_as_uint(a), __float_as_uint(b), false, false);
  a = __uint_as_float(r[0]); b = __uint_as_float(r[1]);
}
__device__ __forceinline__ float half_sum(float x) {
  x += dpp<DPP_XOR1>(x); x += dpp<DPP_XOR2>(x); x += dpp<DPP_ROR4>(x); x += dpp<DPP_ROR8>(x);
  float a = x, b = x; swap16(a, b); return a + b;
}
__device__ __forceinline__ __amdgpu_buffer_rsrc_t mkrs(const void* p, u32 bytes) {
  return __builtin_amdgcn_make_buffer_rsrc(const_cast<void*>(p), 0, bytes, 0x00020000);
}
template <int AUX> __device__ __forceinline__ u32x4 bld(__amdgpu_buffer_rsrc_t rs, int voff, int soff) {
  return __builtin_amdgcn_raw_buffer_load_b128(rs, voff, soff, AUX);
}
__device__ __forceinline__ void pair_mac2(const u32x4& a, const u32x4& b, u32 xp0, u32 xp1, float* acc0, float* acc1) {
#pragma unroll
  for (int d = 0; d < 4; ++d) {
    const u32 lo = __builtin_amdgcn_perm(b[d], a[d], 0x05040100u), hi = __builtin_amdgcn_perm(b[d], a[d], 0x07060302u);
    acc0[2 * d] = dot2(lo, xp0, acc0[2 * d]); acc0[2 * d + 1] = dot2(hi, xp0, acc0[2 * d + 1]);
    acc1[2 * d] = dot2(lo, xp1, acc1[2 * d]); acc1[2 * d + 1] = dot2(hi, xp1, acc1[2 * d + 1]);
  }
}
__device__ __forceinline__ float rs8_hi(float* v, int lane) {
#pragma unroll
  for (int k = 0; k < 4; ++k) { swap32(v[k], v[k + 4]); v[k] += v[k + 4]; }
#pragma unroll
  for (int k = 0; k < 2; ++k) { swap16(v[k], v[k + 2]); v[k] += v[k + 2]; }
  const bool up = lane & 8;
  const float keep = up ? v[1] : v[0], send = up ? v[0] : v[1];
  return keep + dpp<DPP_ROR8>(send);
}
__device__ __forceinline__ void pin8(float* a) {
  asm volatile("" : "+v"(a[0]), "+v"(a[1]), "+v"(a[2]), "+v"(a[3]), "+v"(a[4]), "+v"(a[5]), "+v"(a[6]), "+v"(a[7]));
}
__device__ __forceinline__ void st_gran(gu64* g, u32 tag, u32 v) { __hip_atomic_store(g, ((u64)tag << 32) | (u64)v, RLX_AGENT); }

constexpr size_t STAMP_OFF = (size_t)16 << 20;
#define STAMP(k) do { if (lane == 0) { ((u64*)(ws + STAMP_OFF))[blockIdx.x * 16 + (k)] = __builtin_amdgcn_s_memrealtime(); } } while (0)

constexpr int L_XP = 0;
constexpr int L_Q = L_XP + 576;
constexpr int L_FLAG = L_Q + 128;
constexpr int L_H = L_FLAG + 4;
constexpr int L_O = L_H + 64;
constexpr int L_ML = L_O + 256;
constexpr int L_RED = L_ML + 16;
constexpr int L_RED2 = L_RED + 1024;
constexpr int L_FO = L_RED2 + 512;
constexpr int L_STASH = L_FO + 1152;
constexpr int L_TOTAL = L_STASH + NSW * NE * 2048;

__global__ void __launch_bounds__(NTHR, 2)
fused_decode_kernel(const u16* __restrict__ x, const u16* __restrict__ kc, const u16* __restrict__ vc, const u16* __restrict__ wq,
                    const u16* __restrict__ wo, const u16* __restrict__ wg, const u16* __restrict__ wu, const u16* __restrict__ wd,
                    u16* __restrict__ out, unsigned char* __restrict__ ws) {
  __shared__ __attribute__((aligned(16))) u32 lds[L_TOTAL];
  float* ldsf = reinterpret_cast<float*>(lds);
  const int tid = (int)threadIdx.x - 64, lane = threadIdx.x & 63;
  const int w = __builtin_amdgcn_readfirstlane((int)(threadIdx.x >> 6)) - 1;
  const int bid = blockIdx.x;
  const int g = bid >> 3, s = bid & 7, b = g >> 4, kvh = g & 15;
  const int ii = b * 8 + s;

  const __amdgpu_buffer_rsrc_t rs_k = mkrs(kc + (size_t)g * SK * DH, SK * DH * 2);
  const __amdgpu_buffer_rsrc_t rs_v = mkrs(vc + (size_t)g * SK * DH, SK * DH * 2);
  const int kv_vo = lane * 16;
  const int t0 = s * RT + w;
  const int hf = lane >> 5, l5 = lane & 31;
  const int qs = s * 32 + g;
  u32x4 KA[U], VA[U];
#define ISSUE_K(BUF, T) _Pragma("unroll") for (int i_ = 0; i_ < U; ++i_) BUF[i_] = bld<AUX_NT>(rs_k, kv_vo + i_ * 1024, (T) * TB); SB
#define ISSUE_V(BUF, T) _Pragma("unroll") for (int i_ = 0; i_ < U; ++i_) BUF[i_] = bld<AUX_NT>(rs_v, kv_vo + i_ * 1024, (T) * TB); SB
  if (w == 0) STAMP(0);
  { const int tf = w < 0 ? NT : t0; ISSUE_K(KA, tf); ISSUE_V(VA, tf); }
  volatile LAS u32* lflag = (volatile LAS u32*)(lds + L_FLAG);
  if (threadIdx.x == 0) lflag[1] = 0u;
  asm volatile("; ROLE SPLIT 1" ::: "memory");
  if (w < 0) {
    __builtin_amdgcn_s_setprio(3);
    u32x4 wqv[18];
    u32 xr[9];
    const u32* xw = reinterpret_cast<const u32*>(x) + hf * 288 + l5;
#pragma unroll
    for (int j = 0; j < 9; ++j) xr[j] = xw[j * 32];
    const u32 epoch_v = __hip_atomic_load((gu32*)(ws + WS_EPOCH), RLX_AGENT);
    const __amdgpu_buffer_rsrc_t rs_wq = mkrs(wq, ABL_WQ);
    const int vo = (lane >> 1) * 18 * (NQ * 2) + (lane & 1) * 16;
#pragma unroll
    for (int j = 0; j < 18; ++j) wqv[j] = bld<WQ_AUX>(rs_wq, vo, qs * 32 + j * (NQ * 2));
    SB;
    __syncthreads();
    {
      float v[18]; float sum = 0.f;
#pragma unroll
      for (int j = 0; j < 9; ++j) { v[2 * j] = bf2f(xr[j] & 0xffffu); v[2 * j + 1] = bf2f(xr[j] >> 16); sum += v[2 * j] + v[2 * j + 1]; }
      const float mean = half_sum(sum) * (1.0f / DM);
      float sq = 0.f;
#pragma unroll
      for (int j = 0; j < 18; ++j) { const float d = v[j] - mean; sq = fmaf(d, d, sq); }
      const float var = half_sum(sq) * (1.0f / DM);
      const float mb = rbf(mean), rsd = 1.0f / rbf(sqrtf(rbf(rbf(var) + 1e-5f)));
#pragma unroll
      for (int j = 0; j < 9; ++j)
        lds[L_XP + hf * 288 + j * 32 + l5] = f2bf(rbf(v[2 * j] - mb) * rsd) | (f2bf(rbf(v[2 * j + 1] - mb) * rsd) << 16);
    }
    __builtin_amdgcn_fence(__ATOMIC_RELEASE, "wavefront"); __builtin_amdgcn_wave_barrier(); __builtin_amdgcn_fence(__ATOMIC_ACQUIRE, "wavefront");
    const u32 tag = __builtin_amdgcn_readfirstlane(epoch_v) + 0x9E3779B9u;
    const u32 ctag = tag & 0xfffff000u;
    STAMP(9);
    float a0[8], a1[8];
#pragma unroll
    for (int k = 0; k < 8; ++k) { a0[k] = 0.f; a1[k] = 0.f; }
    {
      const u32* xp = lds + L_XP + (lane >> 1) * 9;
#pragma unroll
      for (int j = 0; j < 9; ++j) pair_mac2(wqv[2 * j], wqv[2 * j + 1], xp[j], xp[288 + j], a0, a1);
      float s0 = rs8_hi(a0, lane), s1 = rs8_hi(a1, lane);
      const bool up = lane & 4;
      const float keep = up ? s1 : s0, send = up ? s0 : s1;
      float qv = keep + dpp<0x1B>(dpp<DPP_HMIR>(send));
      qv += dpp<DPP_XOR2>(qv);
      const u32 qb = f2bf(qv);
      const u32 other = (u32)__builtin_amdgcn_update_dpp(0, (int)qb, DPP_ROR8, 0xf, 0xf, true);
      if ((lane & 0xA) == 0)
        st_gran((gu64*)(ws + WS_QG) + ((lane >> 2) & 1) * 2048 + qs * 8 + (lane & 1) * 4 + (lane >> 4), tag, qb | (other << 16));
    }
    if (s == 0 && lane == 0) __hip_atomic_store((gu32*)(ws + WS_CTR + g * 256), ctag, RLX_AGENT);
    STAMP(10);
    {
      const gu64* qsrc = (const gu64*)(ws + WS_QG) + b * 2048 + kvh * 128 + lane;
      u32 qa, qb;
      for (u32 spins = 0;; ++spins) {
        const u64 v0 = __hip_atomic_load(qsrc, RLX_AGENT), v1 = __hip_atomic_load(qsrc + 64, RLX_AGENT);
        const u32 cw = __hip_atomic_load((const gu32*)(ws + WS_CTR + g * 256), RLX_AGENT);
        qa = (u32)v0; qb = (u32)v1;
        const bool ok = (u32)(v0 >> 32) == tag && (u32)(v1 >> 32) == tag && (cw & 0xfffff000u) == ctag;
        if (__all(ok) || spins > SPIN_CAP) break;
        __builtin_amdgcn_s_sleep(1);
      }
      lds[L_Q + lane] = qa; lds[L_Q + 64 + lane] = qb;
      lds[L_FLAG] = tag;
      __builtin_amdgcn_fence(__ATOMIC_RELEASE, "workgroup");
      lflag[1] = 1u;
    }
  }
  asm volatile("; ROLE SPLIT 2" ::: "memory");

  if (w >= 0) {
    u32x4 KB[U], VB[U], wov[8];
#define WAITV(N) __builtin_amdgcn_s_waitcnt(0x0f70 | ((N) & 15) | (((N) >> 4) << 14)); SB
    __syncthreads();
    u32x4* stash = reinterpret_cast<u32x4*>(lds + L_STASH + w * NE * 2048) + lane;
    int j = 1, ne = 0;
#pragma unroll 1
    while (ne < NE && lflag[1] == 0u) {
      WAITV(4);
#pragma unroll
      for (int i = 0; i < U; ++i) stash[ne * 512 + i * 64] = KA[i];
      SB; ISSUE_K(KA, t0 + j * NSW);
      WAITV(4);
#pragma unroll
      for (int i = 0; i < U; ++i) stash[ne * 512 + 256 + i * 64] = VA[i];
      SB; ISSUE_V(VA, t0 + j * NSW);
      ++ne; ++j;
    }
    if (w == 0) STAMP(1);
    while (lflag[1] == 0u) __builtin_amdgcn_s_sleep(1);
    __builtin_amdgcn_fence(__ATOMIC_ACQUIRE, "workgroup");
    if (w == 0) STAMP(2);
    const u32 tag = __builtin_amdgcn_readfirstlane(lds[L_FLAG]);
    const int r = lane >> 4, c = lane & 15;
    const u32x4 q0 = *reinterpret_cast<const u32x4*>(lds + L_Q + c * 4);
    const u32x4 q1 = *reinterpret_cast<const u32x4*>(lds + L_Q + 64 + c * 4);

    float acc0[8], acc1[8];
#pragma unroll
    for (int k = 0; k < 8; ++k) { acc0[k] = 0.f; acc1[k] = 0.f; }
    float mrun = -INFINITY, lrun = 0.f, p;
    auto qk = [&](const u32x4* Kt) {
      float xv[8];
#pragma unroll
      for (int i = 0; i < U; ++i) {
        float d0 = 0.f, d1 = 0.f;
#pragma unroll
        for (int k = 0; k < 4; ++k) { d0 = dot2(Kt[i][k], q0[k], d0); d1 = dot2(Kt[i][k], q1[k], d1); }
        xv[2 * i] = d0; xv[2 * i + 1] = d1;
      }
#pragma unroll
      for (int k = 0; k < 4; ++k) { const bool up = c & 8; const float keep = up ? xv[k + 4] : xv[k], send = up ? xv[k] : xv[k + 4]; xv[k] = keep + dpp<DPP_ROR8>(send); }
#pragma unroll
      for (int k = 0; k < 2; ++k) { const bool up = c & 4; const float keep = up ? xv[k + 2] : xv[k], send = up ? xv[k] : xv[k + 2]; xv[k] = keep + dpp<DPP_HMIR>(send); }
      { const bool up = c & 2; const float keep = up ? xv[1] : xv[0], send = up ? xv[0] : xv[1]; xv[0] = keep + dpp<DPP_XOR2>(send); }
      float sc = xv[0] + dpp<DPP_XOR1>(xv[0]);
      sc = rbf(rbf(sc) * 0.08838834764831845f);
      float pm = fmaxf(sc, dpp<DPP_ROR4>(sc)); pm = fmaxf(pm, dpp<DPP_ROR8>(pm));
      if (__any(pm > mrun)) {
        const float mn = fmaxf(mrun, pm);
        const float al = __builtin_amdgcn_exp2f((mrun - mn) * LOG2E);
        mrun = mn; lrun *= al;
        const float al0 = dpp<DPP_BC + 0>(al), al1 = dpp<DPP_BC + 2>(al);
#pragma unroll
        for (int k = 0; k < 8; ++k) { acc0[k] *= al0; acc1[k] *= al1; }
      }
      p = __builtin_amdgcn_exp2f((sc - mrun) * LOG2E);
      lrun += p;
    };
    auto pv1 = [&](const u32x4& Vt, float p0, float p1) {
#pragma unroll
      for (int k = 0; k < 4; ++k) {
        const float lo = __uint_as_float(Vt[k] << 16), hi = __uint_as_float(Vt[k] & 0xffff0000u);
        acc0[2 * k] = fmaf(p0, lo, acc0[2 * k]); acc0[2 * k + 1] = fmaf(p0, hi, acc0[2 * k + 1]);
        acc1[2 * k] = fmaf(p1, lo, acc1[2 * k]); acc1[2 * k + 1] = fmaf(p1, hi, acc1[2 * k + 1]);
      }
    };
    auto pv = [&](const u32x4* Vt) {
      pv1(Vt[0], dpp<DPP_BC + 0>(p), dpp<DPP_BC + 2>(p));
      pv1(Vt[1], dpp<DPP_BC + 4>(p), dpp<DPP_BC + 6>(p));
      pv1(Vt[2], dpp<DPP_BC + 8>(p), dpp<DPP_BC + 10>(p));
      pv1(Vt[3], dpp<DPP_BC + 12>(p), dpp<DPP_BC + 14>(p));
      pin8(acc0); pin8(acc1);
    };
    const int cvo = lane * 4;
    int claim = 0, ia = 0, ib;
    {
      const __amdgpu_buffer_rsrc_t rs_wo = mkrs(wo + (size_t)kvh * 256 * DM, ii < ABL_WO ? 256 * DM * 2 : 0);
      const int vo = (lane >> 3) * 8 * (DM * 2) + (lane & 7) * 16, so = 64 * w * (DM * 2) + ii * 128;
#pragma unroll
      for (int k = 0; k < 8; ++k) wov[k] = bld<AUX_NT>(rs_wo, vo, so + k * (DM * 2));
      SB;
    }
#define NEXT_TILE(CUR, NXT) { const int pi_ = __builtin_amdgcn_readfirstlane(claim) & 0xfff; \
      NXT = j < NS ? t0 + j * NSW : ((CUR) < NT && pi_ < NPOOL ? (pi_ & 7) * RT + NS * NSW + (pi_ >> 3) : NT); ++j; \
      claim = __builtin_amdgcn_raw_ptr_buffer_atomic_add_i32(1, mkrs(ws + WS_CTR + g * 256, (j >= NS && NXT < NT) ? 4 : 0), cvo, 0, 0); SB; }
    do {
      WAITV(4); NEXT_TILE(ia, ib); ISSUE_K(KB, ib); qk(KA); SB;
      WAITV(5); ISSUE_V(VB, ib); pv(VA); SB;
      if (ne > 0) {
        --ne;
        u32x4 KE[U], VE[U];
#pragma unroll
        for (int i = 0; i < U; ++i) { KE[i] = stash[ne * 512 + i * 64]; VE[i] = stash[ne * 512 + 256 + i * 64]; }
        qk(KE); pv(VE); SB;
        if (ne == 0 && w == 0) STAMP(3);
      }
      WAITV(4); NEXT_TILE(ib, ia); ISSUE_K(KA, ia); if (ib < NT) qk(KB); SB;
      WAITV(5); ISSUE_V(VA, ia); if (ib < NT) pv(VB); SB;
      if ((j == NS || j == NS + 1) && w == 0) STAMP(4);
    } while (ia < NT);
    if (w == 0) STAMP(5);

    {
      float lr = lrun + dpp<DPP_ROR4>(lrun); lr += dpp<DPP_ROR8>(lr);
      float a = mrun, bb = mrun; swap16(a, bb); float mw = fmaxf(a, bb);
      a = mw; bb = mw; swap32(a, bb); mw = fmaxf(a, bb);
      const float f = __builtin_amdgcn_exp2f((mrun - mw) * LOG2E);
      lr *= f;
      a = lr; bb = lr; swap16(a, bb); lr = a + bb;
      a = lr; bb = lr; swap32(a, bb); lr = a + bb;
      const float f0 = dpp<DPP_BC + 0>(f), f1 = dpp<DPP_BC + 2>(f);
#pragma unroll
      for (int k = 0; k < 8; ++k) { acc0[k] *= f0; acc1[k] *= f1; }
#pragma unroll
      for (int k = 0; k < 8; ++k) { swap32(acc0[k], acc1[k]); acc0[k] += acc1[k]; }
#pragma unroll
      for (int k = 0; k < 4; ++k) { swap16(acc0[k], acc0[k + 4]); acc0[k] += acc0[k + 4]; }
      *reinterpret_cast<f32x4*>(ldsf + L_RED + w * 256 + (r >> 1) * 128 + c * 8 + (r & 1) * 4) = f32x4{acc0[0], acc0[1], acc0[2], acc0[3]};
      if (lane == 0 || lane == 2) { ldsf[L_ML + w * 4 + lane] = mw; ldsf[L_ML + w * 4 + lane + 1] = lr; }
    }
    __syncthreads();
    gu64* p1 = (gu64*)(ws + WS_P1);
    const int h = w >> 1;
    {
      float M = -INFINITY;
#pragma unroll
      for (int k = 0; k < NSW; ++k) M = fmaxf(M, ldsf[L_ML + k * 4 + h * 2]);
      float o = 0.f, L = 0.f;
#pragma unroll
      for (int k = 0; k < NSW; ++k) {
        const float f = __builtin_amdgcn_exp2f((ldsf[L_ML + k * 4 + h * 2] - M) * LOG2E);
        o = fmaf(f, ldsf[L_RED + k * 256 + tid], o); L = fmaf(f, ldsf[L_ML + k * 4 + h * 2 + 1], L);
      }
      gu64* mine = p1 + (size_t)(kvh * 16 + ii) * P1_STRIDE;
      st_gran(mine + tid, tag, __float_as_uint(o));
      if ((tid & 127) < 2) st_gran(mine + 256 + h * 2 + (tid & 1), tag, __float_as_uint((tid & 1) ? L : M));
    }
    if (w == 0) STAMP(6);
    if (ii >= 9) return;

    {
      const gu64* src = p1 + (size_t)(kvh * 16) * P1_STRIDE;
      float av[16], ml[2];
      for (u32 spins = 0;; ++spins) {
        bool ok = true;
#pragma unroll
        for (int k = 0; k < 16; ++k) {
          const u64 v = __hip_atomic_load(src + k * P1_STRIDE + tid, RLX_AGENT);
          av[k] = __uint_as_float((u32)v); ok &= (u32)(v >> 32) == tag;
        }
#pragma unroll
        for (int bb = 0; bb < 2; ++bb) {
          const u64 v = __hip_atomic_load(src + (bb * 8 + (c >> 1)) * P1_STRIDE + 256 + h * 2 + (c & 1), RLX_AGENT);
          ml[bb] = __uint_as_float((u32)v); ok &= (u32)(v >> 32) == tag;
        }
        if (__all(ok) || spins > SPIN_CAP) break;
        __builtin_amdgcn_s_sleep(4);
      }
      if (w == 0) STAMP(7);
#pragma unroll
      for (int bb = 0; bb < 2; ++bb) {
        const float mv = dpp<0xA0>(ml[bb]);
        float M = fmaxf(mv, dpp<DPP_XOR2>(mv)); M = fmaxf(M, dpp<DPP_ROR4>(M)); M = fmaxf(M, dpp<DPP_ROR8>(M));
        const float f = __builtin_amdgcn_exp2f((mv - M) * LOG2E);
        float L = f * dpp<0xF5>(ml[bb]);
        L += dpp<DPP_XOR2>(L); L += dpp<DPP_ROR4>(L); L += dpp<DPP_ROR8>(L);
        const float* a = av + bb * 8;
        float o = a[0] * dpp<DPP_BC + 0>(f);
        o = fmaf(a[1], dpp<DPP_BC + 2>(f), o); o = fmaf(a[2], dpp<DPP_BC + 4>(f), o); o = fmaf(a[3], dpp<DPP_BC + 6>(f), o);
        o = fmaf(a[4], dpp<DPP_BC + 8>(f), o); o = fmaf(a[5], dpp<DPP_BC + 10>(f), o); o = fmaf(a[6], dpp<DPP_BC + 12>(f), o);
        o = fmaf(a[7], dpp<DPP_BC + 14>(f), o);
        reinterpret_cast<u16*>(lds + L_O)[bb * 256 + tid] = (u16)f2bf(o / L);
      }
      __builtin_amdgcn_fence(__ATOMIC_RELEASE, "wavefront"); __builtin_amdgcn_wave_barrier(); __builtin_amdgcn_fence(__ATOMIC_ACQUIRE, "wavefront");
      float a0[8], a1[8];
#pragma unroll
      for (int k = 0; k < 8; ++k) { a0[k] = 0.f; a1[k] = 0.f; }
      const u32x4 o0 = *reinterpret_cast<const u32x4*>(lds + L_O + 32 * w + 4 * (lane >> 3));
      const u32x4 o1 = *reinterpret_cast<const u32x4*>(lds + L_O + 128 + 32 * w + 4 * (lane >> 3));
#pragma unroll
      for (int k = 0; k < 4; ++k) pair_mac2(wov[2 * k], wov[2 * k + 1], o0[k], o1[k], a0, a1);
      const float s0 = rs8_hi(a0, lane), s1 = rs8_hi(a1, lane);
      const int col = (lane & 7) * 8 + (lane >> 3);
      ldsf[L_RED2 + w * 128 + col] = s0; ldsf[L_RED2 + w * 128 + 64 + col] = s1;
      __syncthreads();
      if (tid < 128) {
        const float a = (ldsf[L_RED2 + tid] + ldsf[L_RED2 + 128 + tid]) + (ldsf[L_RED2 + 256 + tid] + ldsf[L_RED2 + 384 + tid]);
        st_gran((gu64*)(ws + WS_P2) + (size_t)(ii * 16 + kvh) * 128 + tid, tag, __float_as_uint(a));
      }
      if (w == 0) STAMP(8);
    }
    return;
  }

  {
    const u32 tag = __builtin_amdgcn_readfirstlane(lds[L_FLAG]);
    float a0[8], a1[8];
    STAMP(11);

    int task = -1;
    if ((s & 3) == (g & 3)) task = g * 2 + (s >> 2);
    else if ((g >> 2) == s && (g & 3) == ((s + 1) & 3)) task = 64 + s;
    if (task >= 0) {
      const int rr = lane >> 3, cc = lane & 7;
      const int vo = rr * 72 * (FF * 2) + cc * 16;
      const u32* xp = lds + L_XP + rr * 36;
      u32x4 A[8], Bf[8];
      float c0[8], c1[8];
      const __amdgpu_buffer_rsrc_t rs_g = mkrs(wg, ABL_GU), rs_u = mkrs(wu, ABL_GU);
      const __amdgpu_buffer_rsrc_t rs_d = mkrs(wd + (size_t)task * 64 * DM, 64 * DM * 2);
#define FISSUE(BUF, RS, KB) { const int so_ = (KB) < 9 ? task * 128 + (KB) * 8 * (FF * 2) : DM * FF * 2; \
      _Pragma("unroll") for (int j_ = 0; j_ < 8; ++j_) BUF[j_] = bld<AUX_NT>(RS, vo, so_ + j_ * (FF * 2)); } SB
#define MAC4(BUF, X0, X1, C0, C1) { _Pragma("unroll") for (int j_ = 0; j_ < 4; ++j_) { pair_mac2(BUF[2 * j_], BUF[2 * j_ + 1], X0[j_], X1[j_], C0, C1); pin8(C0); pin8(C1); SB; } }
#define FZERO(C0, C1) _Pragma("unroll") for (int k_ = 0; k_ < 8; ++k_) { C0[k_] = 0.f; C1[k_] = 0.f; }
#define DISSUE(BUF, R0) _Pragma("unroll") for (int j_ = 0; j_ < 8; ++j_) BUF[j_] = bld<AUX_NT>(rs_d, lane * 16, ((R0) + j_) * (DM * 2)); SB
      FISSUE(A, rs_g, 0); FISSUE(Bf, rs_u, 0);
      FZERO(a0, a1); FZERO(c0, c1);
#pragma unroll 1
      for (int k = 0; k < 9; ++k) {
        const u32x4 x0 = *reinterpret_cast<const u32x4*>(xp + 4 * k), x1 = *reinterpret_cast<const u32x4*>(xp + 288 + 4 * k);
        MAC4(A, x0, x1, a0, a1); FISSUE(A, rs_g, k + 1);
        MAC4(Bf, x0, x1, c0, c1); FISSUE(Bf, rs_u, k + 1);
      }
      DISSUE(A, 0); DISSUE(Bf, 8);
      {
        const float gs0 = rs8_hi(a0, lane), gs1 = rs8_hi(a1, lane), us0 = rs8_hi(c0, lane), us1 = rs8_hi(c1, lane);
        const int col = cc * 8 + rr;
        const float g0 = rbf(gs0), g1 = rbf(gs1);
        const float e0 = rbf(0.5f * g0 * (1.0f + tanhf(0.7978845608028654f * (g0 + 0.044715f * g0 * g0 * g0))));
        const float e1 = rbf(0.5f * g1 * (1.0f + tanhf(0.7978845608028654f * (g1 + 0.044715f * g1 * g1 * g1))));
        reinterpret_cast<u16*>(lds + L_H)[col] = (u16)f2bf(e0 * rbf(us0));
        reinterpret_cast<u16*>(lds + L_H)[64 + col] = (u16)f2bf(e1 * rbf(us1));
      }
      __builtin_amdgcn_fence(__ATOMIC_RELEASE, "wavefront"); __builtin_amdgcn_wave_barrier(); __builtin_amdgcn_fence(__ATOMIC_ACQUIRE, "wavefront");
      const u32* hp = lds + L_H;
      FZERO(a0, a1);
#pragma unroll 1
      for (int k = 0; k < 4; ++k) {
        const u32x4 h0 = *reinterpret_cast<const u32x4*>(hp + 8 * k), h1 = *reinterpret_cast<const u32x4*>(hp + 32 + 8 * k);
        const u32x4 h2 = *reinterpret_cast<const u32x4*>(hp + 8 * k + 4), h3 = *reinterpret_cast<const u32x4*>(hp + 32 + 8 * k + 4);
        MAC4(A, h0, h1, a0, a1); DISSUE(A, 16 * k + 16);
        MAC4(Bf, h2, h3, a0, a1); DISSUE(Bf, 16 * k + 24);
      }
#pragma unroll
      for (int j = 0; j < 8; ++j) A[j] = bld<AUX_NT>(rs_d, rr * 8 * (DM * 2) + 1024 + cc * 16, j * (DM * 2));
      SB;
      float* fo = ldsf + L_FO;
      *reinterpret_cast<f32x4*>(fo + rr * 128 + cc * 8) = f32x4{a0[0], a0[1], a0[2], a0[3]};
      *reinterpret_cast<f32x4*>(fo + rr * 128 + cc * 8 + 4) = f32x4{a0[4], a0[5], a0[6], a0[7]};
      *reinterpret_cast<f32x4*>(fo + rr * 128 + 64 + cc * 8) = f32x4{a1[0], a1[1], a1[2], a1[3]};
      *reinterpret_cast<f32x4*>(fo + rr * 128 + 64 + cc * 8 + 4) = f32x4{a1[4], a1[5], a1[6], a1[7]};
      FZERO(a0, a1);
      {
        const u32x4 h0 = *reinterpret_cast<const u32x4*>(hp + 4 * rr), h1 = *reinterpret_cast<const u32x4*>(hp + 32 + 4 * rr);
        MAC4(A, h0, h1, a0, a1);
      }
      const float e0 = rs8_hi(a0, lane), e1 = rs8_hi(a1, lane);
      fo[1024 + cc * 8 + rr] = e0; fo[1024 + 64 + cc * 8 + rr] = e1;
      __builtin_amdgcn_fence(__ATOMIC_RELEASE, "wavefront"); __builtin_amdgcn_wave_barrier(); __builtin_amdgcn_fence(__ATOMIC_ACQUIRE, "wavefront");
      const __amdgpu_buffer_rsrc_t rs_pf = mkrs(ws + WS_PF, ABL_PF);
#pragma unroll
      for (int k = 0; k < 9; ++k) {
        const float2 v = *reinterpret_cast<const float2*>(fo + k * 128 + lane * 2);
        __builtin_amdgcn_raw_buffer_store_b128(u32x4{__float_as_uint(v.x), tag, __float_as_uint(v.y), tag}, rs_pf, lane * 16, (k * NFT + task) * 1024, AUX_SC1);
      }
      STAMP(12);
    }

    const bool is_red = (ii == 15 && kvh < 9);
    float f0 = 0.f, f1 = 0.f; u32 xpair = 0;
    if (is_red) {
      xpair = reinterpret_cast<const u32*>(x)[hf * 288 + kvh * 32 + l5];
      const __amdgpu_buffer_rsrc_t rs_f = mkrs(ws + WS_PF + (size_t)kvh * NFT * 1024, NFT * 1024);
      for (int k0 = 0; k0 < NFT; k0 += 12) {
        float p0, p1;
        for (u32 spins = 0;; ++spins) {
          bool ok = true; p0 = 0.f; p1 = 0.f;
          u32x4 v[12];
#pragma unroll
          for (int k = 0; k < 12; ++k) v[k] = bld<AUX_SC1>(rs_f, lane * 16, (k0 + k) * 1024);
#pragma unroll
          for (int k = 0; k < 12; ++k) { p0 += __uint_as_float(v[k][0]); p1 += __uint_as_float(v[k][2]); ok &= v[k][1] == tag && v[k][3] == tag; }
          if (__all(ok) || spins > SPIN_CAP) break;
          __builtin_amdgcn_s_sleep(64);
        }
        f0 += p0; f1 += p1;
      }
      STAMP(13);
    }
    __syncthreads();
    if (is_red) {
      const __amdgpu_buffer_rsrc_t rs_y = mkrs(ws + WS_P2 + (size_t)kvh * 16 * 1024, 16 * 1024);
      float y0, y1;
      for (u32 spins = 0;; ++spins) {
        bool ok = true; y0 = 0.f; y1 = 0.f;
        u32x4 v[16];
#pragma unroll
        for (int k = 0; k < 16; ++k) v[k] = bld<AUX_SC1>(rs_y, lane * 16, k * 1024);
#pragma unroll
        for (int k = 0; k < 16; ++k) { y0 += __uint_as_float(v[k][0]); y1 += __uint_as_float(v[k][2]); ok &= v[k][1] == tag && v[k][3] == tag; }
        if (__all(ok) || spins > SPIN_CAP) break;
        __builtin_amdgcn_s_sleep(2);
      }
      const u32 o0 = f2bf(rbf(bf2f(xpair & 0xffffu) + rbf(y0)) + rbf(f0)), o1 = f2bf(rbf(bf2f(xpair >> 16) + rbf(y1)) + rbf(f1));
      reinterpret_cast<u32*>(out)[hf * 288 + kvh * 32 + l5] = o0 | (o1 << 16);
      if (kvh == 0 && lane == 0) __hip_atomic_store((gu32*)(ws + WS_EPOCH), tag, RLX_AGENT);
      STAMP(14);
    }
  }
}

__global__ void probe_k(const unsigned* __restrict__ p, unsigned* lat, int n, int stride_dw, int target_xcc){
  unsigned xcc; asm volatile("s_getreg_b32 %0, hwreg(HW_REG_XCC_ID)" : "=s"(xcc));
  xcc &= 15;
  if ((int)xcc != target_xcc) return;
  unsigned acc = 0;
  for (int i=0;i<n;i++){
    const unsigned* a = p + (size_t)i*stride_dw;
    unsigned long long t0, t1; unsigned v;
    asm volatile("s_waitcnt vmcnt(0) lgkmcnt(0)\n\ts_memtime %0\n\ts_waitcnt lgkmcnt(0)\n\tglobal_load_dword %1, %3, off nt\n\ts_waitcnt vmcnt(0)\n\ts_memtime %2\n\ts_waitcnt lgkmcnt(0)" : "=&s"(t0), "=&v"(v), "=&s"(t1) : "v"(a) : "memory");
    acc += v;
    lat[i] = (unsigned)(t1-t0);
  }
  if (acc == 0x1234567) lat[4000]=acc;
}
__global__ void report_k(unsigned char* ws){
  const u64* st = (const u64*)(ws + STAMP_OFF);
  u64 t0 = ~0ull;
#pragma unroll 1
  for (int b=0;b<256;b++) t0 = st[b*16] < t0 ? st[b*16] : t0;
  const int x = threadIdx.x & 7, q = threadIdx.x >> 3;
  const int k = q == 0 ? 1 : q == 1 ? 2 : q == 2 ? 4 : q == 3 ? 5 : q == 4 ? 6 : 10;
  if (q < 6) { float a=0,m=0;
#pragma unroll 1
    for (int b=x;b<256;b+=8){ float v=(st[b*16+k]-t0)*0.01f; a+=v/32; m=v>m?v:m; }
    printf("stamp %d xcd %d avg %d max %d\n", k, x, (int)(a*10), (int)(m*10)); }
  if (threadIdx.x == 48) printf("end %d\n", (int)((st[(16*8+7)*16+14]-t0)/10));
  const unsigned* lat = (const unsigned*)(ws + (32<<20));
  if (threadIdx.x >= 56 && threadIdx.x < 60) { int t = threadIdx.x - 56; const unsigned* a = lat + t*8192; const unsigned* b = a + 4096; unsigned long long m0=0,m1=0; unsigned s0=0,s1=0;
#pragma unroll 1
    for (int i=0;i<64;i++){ m0 |= (unsigned long long)(a[i]>b[i])<<i; m1 |= (unsigned long long)(a[64+i]>b[64+i])<<i; }
#pragma unroll 1
    for (int i=0;i<128;i++){ s0+=a[i]; s1+=b[i]; }
    printf("probe %d sides %016llx %016llx lat %u %u\n", t, m0, m1, s0/128, s1/128); }
}
extern "C" void kernel_launch(void* const* d_in, const int* in_sizes, int n_in, void* d_out, int out_size, void* d_ws, size_t ws_size,
                              hipStream_t stream) {
  (void)in_sizes; (void)n_in; (void)out_size; (void)ws_size;
  hipLaunchKernelGGL(fused_decode_kernel, dim3(256), dim3(NTHR), 0, stream, (const u16*)d_in[0], (const u16*)d_in[1], (const u16*)d_in[2],
                     (const u16*)d_in[3], (const u16*)d_in[4], (const u16*)d_in[6], (const u16*)d_in[7], (const u16*)d_in[8], (u16*)d_out,
                     (unsigned char*)d_ws);
  fprintf(stderr, "PTRS x %p K %p V %p wq %p wo %p wg %p wu %p wd %p out %p ws %p\n", d_in[0], d_in[1], d_in[2], d_in[3], d_in[4], d_in[6], d_in[7], d_in[8], d_out, d_ws);
  unsigned* lat = (unsigned*)((char*)d_ws + (32<<20));
  for (int t=0;t<2;t++){
    probe_k<<<8,1,0,stream>>>((const unsigned*)d_in[1+t] + 64, lat + t*8192, 128, (2<<20)/4, 0);
    probe_k<<<8,1,0,stream>>>((const unsigned*)d_in[1+t] + 192, lat + t*8192 + 4096, 128, (2<<20)/4, 4);
    probe_k<<<8,1,0,stream>>>((const unsigned*)d_in[1+t] + 320, lat + (2+t)*8192, 128, (64<<10)/4, 0);
    probe_k<<<8,1,0,stream>>>((const unsigned*)d_in[1+t] + 448, lat + (2+t)*8192 + 4096, 128, (64<<10)/4, 4);
  }
  report_k<<<1,64,0,stream>>>((unsigned char*)d_ws);
}
```

```cpp
#define DEFK_LO 0
#define DEFK_HI 8
#define DEFV_LO 0
#define DEFV_HI 0
#include <hip/hip_runtime.h>
#include <hip/hip_bf16.h>
#include <stdint.h>

typedef unsigned u32;
typedef unsigned short u16;
typedef unsigned long long u64;
typedef u32 u32x4 __attribute__((ext_vector_type(4)));
typedef float f32x4 __attribute__((ext_vector_type(4)));
typedef __bf16 bf16x2 __attribute__((ext_vector_type(2)));
#define GAS __attribute__((address_space(1)))
#define LAS __attribute__((address_space(3)))
typedef GAS u64 gu64;
typedef GAS u32 gu32;
#define RLX_AGENT __ATOMIC_RELAXED, __HIP_MEMORY_SCOPE_AGENT
#define SB __builtin_amdgcn_sched_barrier(0)

constexpr int DM = 576, SK = 32768, DH = 128, FF = 4608, NQ = 4096;
constexpr int NSW = 4, NTHR = (NSW + 1) * 64, U = 4;
constexpr int TB = U * 1024;
constexpr int NT = SK / 16;
constexpr int RT = NT / 8;
#define NSTAT 48
#define THR 4
#define WQ_AUX 2
#define ABL_WO 9
#define ABL_WQ (DM * NQ * 2)
#define ABL_PF (9 * NFT * 1024)
#define ABL_GU (DM * FF * 2)
#define NE 4
constexpr int NS = NSTAT;
constexpr int NPOOL = (RT - NS * NSW) * 8;
constexpr int NFT = 72;
constexpr int AUX_NT = 2, AUX_SC1 = 16;
constexpr float LOG2E = 1.4426950408889634f;
constexpr u32 SPIN_CAP = 1u << 16;

constexpr size_t WS_EPOCH = 0;
constexpr size_t WS_CTR = 4096;
constexpr size_t WS_QG = WS_CTR + 32 * 256;
constexpr size_t WS_P1 = WS_QG + 2 * 2048 * 8;
constexpr int P1_STRIDE = 264;
constexpr size_t WS_P2 = WS_P1 + (size_t)256 * P1_STRIDE * 8;
constexpr size_t WS_PF = WS_P2 + (size_t)9 * 16 * 128 * 8;
constexpr size_t WS_END = WS_PF + (size_t)9 * NFT * 128 * 8;

__device__ __forceinline__ float bf2f(u32 lo16) { return __uint_as_float(lo16 << 16); }
__device__ __forceinline__ float rbf(float f) { return (float)(__bf16)f; }
__device__ __forceinline__ u32 f2bf(float f) { const __bf16 h = (__bf16)f; return (u32)__builtin_bit_cast(u16, h); }
__device__ __forceinline__ float dot2(u32 a, u32 b, float c) {
  return __builtin_amdgcn_fdot2_f32_bf16(__builtin_bit_cast(bf16x2, a), __builtin_bit_cast(bf16x2, b), c, false);
}
template <int CTRL> __device__ __forceinline__ float dpp(float x) {
  return __int_as_float(__builtin_amdgcn_update_dpp(0, __float_as_int(x), CTRL, 0xf, 0xf, true));
}
constexpr int DPP_XOR1 = 0xB1, DPP_XOR2 = 0x4E, DPP_HMIR = 0x141, DPP_ROR8 = 0x128, DPP_ROR4 = 0x124, DPP_BC = 0x150;
__device__ __forceinline__ void swap32(float& a, float& b) {
  auto r = __builtin_amdgcn_permlane32_swap(__float_as_uint(a), __float_as_uint(b), false, false);
  a = __uint_as_float(r[0]); b = __uint_as_float(r[1]);
}
__device__ __forceinline__ void swap16(float& a, float& b) {
  auto r = __builtin_amdgcn_permlane16_swap(__float_as_uint(a), __float_as_uint(b), false, false);
  a = __uint_as_float(r[0]); b = __uint_as_float(r[1]);
}
__device__ __forceinline__ float half_sum(float x) {
  x += dpp<DPP_XOR1>(x); x += dpp<DPP_XOR2>(x); x += dpp<DPP_ROR4>(x); x += dpp<DPP_ROR8>(x);
  float a = x, b = x; swap16(a, b); return a + b;
}
__device__ __forceinline__ __amdgpu_buffer_rsrc_t mkrs(const void* p, u32 bytes) {
  return __builtin_amdgcn_make_buffer_rsrc(const_cast<void*>(p), 0, bytes, 0x00020000);
}
template <int AUX> __device__ __forceinline__ u32x4 bld(__amdgpu_buffer_rsrc_t rs, int voff, int soff) {
  return __builtin_amdgcn_raw_buffer_load_b128(rs, voff, soff, AUX);
}
__device__ __forceinline__ void pair_mac2(const u32x4& a, const u32x4& b, u32 xp0, u32 xp1, float* acc0, float* acc1) {
#pragma unroll
  for (int d = 0; d < 4; ++d) {
    const u32 lo = __builtin_amdgcn_perm(b[d], a[d], 0x05040100u), hi = __builtin_amdgcn_perm(b[d], a[d], 0x07060302u);
    acc0[2 * d] = dot2(lo, xp0, acc0[2 * d]); acc0[2 * d + 1] = dot2(hi, xp0, acc0[2 * d + 1]);
    acc1[2 * d] = dot2(lo, xp1, acc1[2 * d]); acc1[2 * d + 1] = dot2(hi, xp1, acc1[2 * d + 1]);
  }
}
__device__ __forceinline__ float rs8_hi(float* v, int lane) {
#pragma unroll
  for (int k = 0; k < 4; ++k) { swap32(v[k], v[k + 4]); v[k] += v[k + 4]; }
#pragma unroll
  for (int k = 0; k < 2; ++k) { swap16(v[k], v[k + 2]); v[k] += v[k + 2]; }
  const bool up = lane & 8;
  const float keep = up ? v[1] : v[0], send = up ? v[0] : v[1];
  return keep + dpp<DPP_ROR8>(send);
}
__device__ __forceinline__ void pin8(float* a) {
  asm volatile("" : "+v"(a[0]), "+v"(a[1]), "+v"(a[2]), "+v"(a[3]), "+v"(a[4]), "+v"(a[5]), "+v"(a[6]), "+v"(a[7]));
}
__device__ __forceinline__ void st_gran(gu64* g, u32 tag, u32 v) { __hip_atomic_store(g, ((u64)tag << 32) | (u64)v, RLX_AGENT); }

#define STAMP(k) do {} while (0)

constexpr int L_XP = 0;
constexpr int L_Q = L_XP + 576;
constexpr int L_FLAG = L_Q + 128;
constexpr int L_H = L_FLAG + 4;
constexpr int L_O = L_H + 64;
constexpr int L_ML = L_O + 256;
constexpr int L_RED = L_ML + 16;
constexpr int L_RED2 = L_RED + 1024;
constexpr int L_FO = L_RED2 + 512;
constexpr int L_STASH = L_FO + 1152;
constexpr int L_TOTAL = L_STASH + NSW * NE * 2048;

__global__ void __launch_bounds__(NTHR, 2)
fused_decode_kernel(const u16* __restrict__ x, const u16* __restrict__ kc, const u16* __restrict__ vc, const u16* __restrict__ wq,
                    const u16* __restrict__ wo, const u16* __restrict__ wg, const u16* __restrict__ wu, const u16* __restrict__ wd,
                    u16* __restrict__ out, unsigned char* __restrict__ ws) {
  __shared__ __attribute__((aligned(16))) u32 lds[L_TOTAL];
  float* ldsf = reinterpret_cast<float*>(lds);
  const int tid = (int)threadIdx.x - 64, lane = threadIdx.x & 63;
  const int w = __builtin_amdgcn_readfirstlane((int)(threadIdx.x >> 6)) - 1;
  const int bid = blockIdx.x;
  const int g = bid >> 3, s = bid & 7, b = g >> 4, kvh = g & 15;
  const int ii = b * 8 + s;

  const __amdgpu_buffer_rsrc_t rs_k = mkrs(kc + (size_t)g * SK * DH, SK * DH * 2);
  const __amdgpu_buffer_rsrc_t rs_v = mkrs(vc + (size_t)g * SK * DH, SK * DH * 2);
  const int kv_vo = lane * 16;
  const int t0 = s * RT + w;
  const int hf = lane >> 5, l5 = lane & 31;
  const int qs = s * 32 + g;
  u32x4 KA[U], VA[U];
  const bool kdef = g >= DEFK_LO && g < DEFK_HI, vdef = g >= DEFV_LO && g < DEFV_HI;
#define ISSUE_K(BUF, T) if (kdef) { _Pragma("unroll") for (int i_ = 0; i_ < U; ++i_) BUF[i_] = bld<0>(rs_k, kv_vo + i_ * 1024, (T) * TB); } else { _Pragma("unroll") for (int i_ = 0; i_ < U; ++i_) BUF[i_] = bld<AUX_NT>(rs_k, kv_vo + i_ * 1024, (T) * TB); } SB
#define ISSUE_V(BUF, T) if (vdef) { _Pragma("unroll") for (int i_ = 0; i_ < U; ++i_) BUF[i_] = bld<0>(rs_v, kv_vo + i_ * 1024, (T) * TB); } else { _Pragma("unroll") for (int i_ = 0; i_ < U; ++i_) BUF[i_] = bld<AUX_NT>(rs_v, kv_vo + i_ * 1024, (T) * TB); } SB
  if (w == 0) STAMP(0);
  { const int tf = w < 0 ? NT : t0; ISSUE_K(KA, tf); ISSUE_V(VA, tf); }
  volatile LAS u32* lflag = (volatile LAS u32*)(lds + L_FLAG);
  if (threadIdx.x == 0) lflag[1] = 0u;
  asm volatile("; ROLE SPLIT 1" ::: "memory");
  if (w < 0) {
    __builtin_amdgcn_s_setprio(3);
    u32x4 wqv[18];
    u32 xr[9];
    const u32* xw = reinterpret_cast<const u32*>(x) + hf * 288 + l5;
#pragma unroll
    for (int j = 0; j < 9; ++j) xr[j] = xw[j * 32];
    const u32 epoch_v = __hip_atomic_load((gu32*)(ws + WS_EPOCH), RLX_AGENT);
    const __amdgpu_buffer_rsrc_t rs_wq = mkrs(wq, ABL_WQ);
    const int vo = (lane >> 1) * 18 * (NQ * 2) + (lane & 1) * 16;
#pragma unroll
    for (int j = 0; j < 18; ++j) wqv[j] = bld<WQ_AUX>(rs_wq, vo, qs * 32 + j * (NQ * 2));
    SB;
    __syncthreads();
    {
      float v[18]; float sum = 0.f;
#pragma unroll
      for (int j = 0; j < 9; ++j) { v[2 * j] = bf2f(xr[j] & 0xffffu); v[2 * j + 1] = bf2f(xr[j] >> 16); sum += v[2 * j] + v[2 * j + 1]; }
      const float mean = half_sum(sum) * (1.0f / DM);
      float sq = 0.f;
#pragma unroll
      for (int j = 0; j < 18; ++j) { const float d = v[j] - mean; sq = fmaf(d, d, sq); }
      const float var = half_sum(sq) * (1.0f / DM);
      const float mb = rbf(mean), rsd = 1.0f / rbf(sqrtf(rbf(rbf(var) + 1e-5f)));
#pragma unroll
      for (int j = 0; j < 9; ++j)
        lds[L_XP + hf * 288 + j * 32 + l5] = f2bf(rbf(v[2 * j] - mb) * rsd) | (f2bf(rbf(v[2 * j + 1] - mb) * rsd) << 16);
    }
    __builtin_amdgcn_fence(__ATOMIC_RELEASE, "wavefront"); __builtin_amdgcn_wave_barrier(); __builtin_amdgcn_fence(__ATOMIC_ACQUIRE, "wavefront");
    const u32 tag = __builtin_amdgcn_readfirstlane(epoch_v) + 0x9E3779B9u;
    const u32 ctag = tag & 0xfffff000u;
    STAMP(9);
    float a0[8], a1[8];
#pragma unroll
    for (int k = 0; k < 8; ++k) { a0[k] = 0.f; a1[k] = 0.f; }
    {
      const u32* xp = lds + L_XP + (lane >> 1) * 9;
#pragma unroll
      for (int j = 0; j < 9; ++j) pair_mac2(wqv[2 * j], wqv[2 * j + 1], xp[j], xp[288 + j], a0, a1);
      float s0 = rs8_hi(a0, lane), s1 = rs8_hi(a1, lane);
      const bool up = lane & 4;
      const float keep = up ? s1 : s0, send = up ? s0 : s1;
      float qv = keep + dpp<0x1B>(dpp<DPP_HMIR>(send));
      qv += dpp<DPP_XOR2>(qv);
      const u32 qb = f2bf(qv);
      const u32 other = (u32)__builtin_amdgcn_update_dpp(0, (int)qb, DPP_ROR8, 0xf, 0xf, true);
      if ((lane & 0xA) == 0)
        st_gran((gu64*)(ws + WS_QG) + ((lane >> 2) & 1) * 2048 + qs * 8 + (lane & 1) * 4 + (lane >> 4), tag, qb | (other << 16));
    }
    if (s == 0 && lane == 0) __hip_atomic_store((gu32*)(ws + WS_CTR + g * 256), ctag, RLX_AGENT);
    STAMP(10);
    {
      const gu64* qsrc = (const gu64*)(ws + WS_QG) + b * 2048 + kvh * 128 + lane;
      u32 qa, qb;
      for (u32 spins = 0;; ++spins) {
        const u64 v0 = __hip_atomic_load(qsrc, RLX_AGENT), v1 = __hip_atomic_load(qsrc + 64, RLX_AGENT);
        const u32 cw = __hip_atomic_load((const gu32*)(ws + WS_CTR + g * 256), RLX_AGENT);
        qa = (u32)v0; qb = (u32)v1;
        const bool ok = (u32)(v0 >> 32) == tag && (u32)(v1 >> 32) == tag && (cw & 0xfffff000u) == ctag;
        if (__all(ok) || spins > SPIN_CAP) break;
        __builtin_amdgcn_s_sleep(1);
      }
      lds[L_Q + lane] = qa; lds[L_Q + 64 + lane] = qb;
      lds[L_FLAG] = tag;
      __builtin_amdgcn_fence(__ATOMIC_RELEASE, "workgroup");
      lflag[1] = 1u;
    }
  }
  asm volatile("; ROLE SPLIT 2" ::: "memory");

  if (w >= 0) {
    u32x4 KB[U], VB[U], wov[8];
#define WAITV(N) __builtin_amdgcn_s_waitcnt(0x0f70 | ((N) & 15) | (((N) >> 4) << 14)); SB
    __syncthreads();
    u32x4* stash = reinterpret_cast<u32x4*>(lds + L_STASH + w * NE * 2048) + lane;
    int j = 1, ne = 0;
#pragma unroll 1
    while (ne < NE && lflag[1] == 0u) {
      WAITV(4);
#pragma unroll
      for (int i = 0; i < U; ++i) stash[ne * 512 + i * 64] = KA[i];
      SB; ISSUE_K(KA, t0 + j * NSW);
      WAITV(4);
#pragma unroll
      for (int i = 0; i < U; ++i) stash[ne * 512 + 256 + i * 64] = VA[i];
      SB; ISSUE_V(VA, t0 + j * NSW);
      ++ne; ++j;
    }
    if (w == 0) STAMP(1);
    while (lflag[1] == 0u) __builtin_amdgcn_s_sleep(1);
    __builtin_amdgcn_fence(__ATOMIC_ACQUIRE, "workgroup");
    if (w == 0) STAMP(2);
    const u32 tag = __builtin_amdgcn_readfirstlane(lds[L_FLAG]);
    const int r = lane >> 4, c = lane & 15;
    const u32x4 q0 = *reinterpret_cast<const u32x4*>(lds + L_Q + c * 4);
    const u32x4 q1 = *reinterpret_cast<const u32x4*>(lds + L_Q + 64 + c * 4);

    float acc0[8], acc1[8];
#pragma unroll
    for (int k = 0; k < 8; ++k) { acc0[k] = 0.f; acc1[k] = 0.f; }
    float mrun = -INFINITY, lrun = 0.f, p;
    auto qk = [&](const u32x4* Kt) {
      float xv[8];
#pragma unroll
      for (int i = 0; i < U; ++i) {
        float d0 = 0.f, d1 = 0.f;
#pragma unroll
        for (int k = 0; k < 4; ++k) { d0 = dot2(Kt[i][k], q0[k], d0); d1 = dot2(Kt[i][k], q1[k], d1); }
        xv[2 * i] = d0; xv[2 * i + 1] = d1;
      }
#pragma unroll
      for (int k = 0; k < 4; ++k) { const bool up = c & 8; const float keep = up ? xv[k + 4] : xv[k], send = up ? xv[k] : xv[k + 4]; xv[k] = keep + dpp<DPP_ROR8>(send); }
#pragma unroll
      for (int k = 0; k < 2; ++k) { const bool up = c & 4; const float keep = up ? xv[k + 2] : xv[k], send = up ? xv[k] : xv[k + 2]; xv[k] = keep + dpp<DPP_HMIR>(send); }
      { const bool up = c & 2; const float keep = up ? xv[1] : xv[0], send = up ? xv[0] : xv[1]; xv[0] = keep + dpp<DPP_XOR2>(send); }
      float sc = xv[0] + dpp<DPP_XOR1>(xv[0]);
      sc = rbf(rbf(sc) * 0.08838834764831845f);
      float pm = fmaxf(sc, dpp<DPP_ROR4>(sc)); pm = fmaxf(pm, dpp<DPP_ROR8>(pm));
      if (__any(pm > mrun)) {
        const float mn = fmaxf(mrun, pm);
        const float al = __builtin_amdgcn_exp2f((mrun - mn) * LOG2E);
        mrun = mn; lrun *= al;
        const float al0 = dpp<DPP_BC + 0>(al), al1 = dpp<DPP_BC + 2>(al);
#pragma unroll
        for (int k = 0; k < 8; ++k) { acc0[k] *= al0; acc1[k] *= al1; }
      }
      p = __builtin_amdgcn_exp2f((sc - mrun) * LOG2E);
      lrun += p;
    };
    auto pv1 = [&](const u32x4& Vt, float p0, float p1) {
#pragma unroll
      for (int k = 0; k < 4; ++k) {
        const float lo = __uint_as_float(Vt[k] << 16), hi = __uint_as_float(Vt[k] & 0xffff0000u);
        acc0[2 * k] = fmaf(p0, lo, acc0[2 * k]); acc0[2 * k + 1] = fmaf(p0, hi, acc0[2 * k + 1]);
        acc1[2 * k] = fmaf(p1, lo, acc1[2 * k]); acc1[2 * k + 1] = fmaf(p1, hi, acc1[2 * k + 1]);
      }
    };
    auto pv = [&](const u32x4* Vt) {
      pv1(Vt[0], dpp<DPP_BC + 0>(p), dpp<DPP_BC + 2>(p));
      pv1(Vt[1], dpp<DPP_BC + 4>(p), dpp<DPP_BC + 6>(p));
      pv1(Vt[2], dpp<DPP_BC + 8>(p), dpp<DPP_BC + 10>(p));
      pv1(Vt[3], dpp<DPP_BC + 12>(p), dpp<DPP_BC + 14>(p));
      pin8(acc0); pin8(acc1);
    };
    const int cvo = lane * 4;
    int claim = 0, ia = 0, ib;
    {
      const __amdgpu_buffer_rsrc_t rs_wo = mkrs(wo + (size_t)kvh * 256 * DM, ii < ABL_WO ? 256 * DM * 2 : 0);
      const int vo = (lane >> 3) * 8 * (DM * 2) + (lane & 7) * 16, so = 64 * w * (DM * 2) + ii * 128;
#pragma unroll
      for (int k = 0; k < 8; ++k) wov[k] = bld<AUX_NT>(rs_wo, vo, so + k * (DM * 2));
      SB;
    }
#define NEXT_TILE(CUR, NXT) { const int pi_ = __builtin_amdgcn_readfirstlane(claim) & 0xfff; \
      NXT = j < NS ? t0 + j * NSW : ((CUR) < NT && pi_ < NPOOL ? (pi_ & 7) * RT + NS * NSW + (pi_ >> 3) : NT); ++j; \
      claim = __builtin_amdgcn_raw_ptr_buffer_atomic_add_i32(1, mkrs(ws + WS_CTR + g * 256, (j >= NS && NXT < NT) ? 4 : 0), cvo, 0, 0); SB; }
    do {
      WAITV(4); NEXT_TILE(ia, ib); ISSUE_K(KB, ib); qk(KA); SB;
      WAITV(5); ISSUE_V(VB, ib); pv(VA); SB;
      if (ne > 0) {
        --ne;
        u32x4 KE[U], VE[U];
#pragma unroll
        for (int i = 0; i < U; ++i) { KE[i] = stash[ne * 512 + i * 64]; VE[i] = stash[ne * 512 + 256 + i * 64]; }
        qk(KE); pv(VE); SB;
      }
      WAITV(4); NEXT_TILE(ib, ia); ISSUE_K(KA, ia); if (ib < NT) qk(KB); SB;
      WAITV(5); ISSUE_V(VA, ia); if (ib < NT) pv(VB); SB;
    } while (ia < NT);
    if (w == 0) STAMP(5);

    {
      float lr = lrun + dpp<DPP_ROR4>(lrun); lr += dpp<DPP_ROR8>(lr);
      float a = mrun, bb = mrun; swap16(a, bb); float mw = fmaxf(a, bb);
      a = mw; bb = mw; swap32(a, bb); mw = fmaxf(a, bb);
      const float f = __builtin_amdgcn_exp2f((mrun - mw) * LOG2E);
      lr *= f;
      a = lr; bb = lr; swap16(a, bb); lr = a + bb;
      a = lr; bb = lr; swap32(a, bb); lr = a + bb;
      const float f0 = dpp<DPP_BC + 0>(f), f1 = dpp<DPP_BC + 2>(f);
#pragma unroll
      for (int k = 0; k < 8; ++k) { acc0[k] *= f0; acc1[k] *= f1; }
#pragma unroll
      for (int k = 0; k < 8; ++k) { swap32(acc0[k], acc1[k]); acc0[k] += acc1[k]; }
#pragma unroll
      for (int k = 0; k < 4; ++k) { swap16(acc0[k], acc0[k + 4]); acc0[k] += acc0[k + 4]; }
      *reinterpret_cast<f32x4*>(ldsf + L_RED + w * 256 + (r >> 1) * 128 + c * 8 + (r & 1) * 4) = f32x4{acc0[0], acc0[1], acc0[2], acc0[3]};
      if (lane == 0 || lane == 2) { ldsf[L_ML + w * 4 + lane] = mw; ldsf[L_ML + w * 4 + lane + 1] = lr; }
    }
    __syncthreads();
    gu64* p1 = (gu64*)(ws + WS_P1);
    const int h = w >> 1;
    {
      float M = -INFINITY;
#pragma unroll
      for (int k = 0; k < NSW; ++k) M = fmaxf(M, ldsf[L_ML + k * 4 + h * 2]);
      float o = 0.f, L = 0.f;
#pragma unroll
      for (int k = 0; k < NSW; ++k) {
        const float f = __builtin_amdgcn_exp2f((ldsf[L_ML + k * 4 + h * 2] - M) * LOG2E);
        o = fmaf(f, ldsf[L_RED + k * 256 + tid], o); L = fmaf(f, ldsf[L_ML + k * 4 + h * 2 + 1], L);
      }
      gu64* mine = p1 + (size_t)(kvh * 16 + ii) * P1_STRIDE;
      st_gran(mine + tid, tag, __float_as_uint(o));
      if ((tid & 127) < 2) st_gran(mine + 256 + h * 2 + (tid & 1), tag, __float_as_uint((tid & 1) ? L : M));
    }
    if (w == 0) STAMP(6);
    if (ii >= 9) return;

    {
      const gu64* src = p1 + (size_t)(kvh * 16) * P1_STRIDE;
      float av[16], ml[2];
      for (u32 spins = 0;; ++spins) {
        bool ok = true;
#pragma unroll
        for (int k = 0; k < 16; ++k) {
          const u64 v = __hip_atomic_load(src + k * P1_STRIDE + tid, RLX_AGENT);
          av[k] = __uint_as_float((u32)v); ok &= (u32)(v >> 32) == tag;
        }
#pragma unroll
        for (int bb = 0; bb < 2; ++bb) {
          const u64 v = __hip_atomic_load(src + (bb * 8 + (c >> 1)) * P1_STRIDE + 256 + h * 2 + (c & 1), RLX_AGENT);
          ml[bb] = __uint_as_float((u32)v); ok &= (u32)(v >> 32) == tag;
        }
        if (__all(ok) || spins > SPIN_CAP) break;
        __builtin_amdgcn_s_sleep(4);
      }
      if (w == 0) STAMP(7);
#pragma unroll
      for (int bb = 0; bb < 2; ++bb) {
        const float mv = dpp<0xA0>(ml[bb]);
        float M = fmaxf(mv, dpp<DPP_XOR2>(mv)); M = fmaxf(M, dpp<DPP_ROR4>(M)); M = fmaxf(M, dpp<DPP_ROR8>(M));
        const float f = __builtin_amdgcn_exp2f((mv - M) * LOG2E);
        float L = f * dpp<0xF5>(ml[bb]);
        L += dpp<DPP_XOR2>(L); L += dpp<DPP_ROR4>(L); L += dpp<DPP_ROR8>(L);
        const float* a = av + bb * 8;
        float o = a[0] * dpp<DPP_BC + 0>(f);
        o = fmaf(a[1], dpp<DPP_BC + 2>(f), o); o = fmaf(a[2], dpp<DPP_BC + 4>(f), o); o = fmaf(a[3], dpp<DPP_BC + 6>(f), o);
        o = fmaf(a[4], dpp<DPP_BC + 8>(f), o); o = fmaf(a[5], dpp<DPP_BC + 10>(f), o); o = fmaf(a[6], dpp<DPP_BC + 12>(f), o);
        o = fmaf(a[7], dpp<DPP_BC + 14>(f), o);
        reinterpret_cast<u16*>(lds + L_O)[bb * 256 + tid] = (u16)f2bf(o / L);
      }
      __builtin_amdgcn_fence(__ATOMIC_RELEASE, "wavefront"); __builtin_amdgcn_wave_barrier(); __builtin_amdgcn_fence(__ATOMIC_ACQUIRE, "wavefront");
      float a0[8], a1[8];
#pragma unroll
      for (int k = 0; k < 8; ++k) { a0[k] = 0.f; a1[k] = 0.f; }
      const u32x4 o0 = *reinterpret_cast<const u32x4*>(lds + L_O + 32 * w + 4 * (lane >> 3));
      const u32x4 o1 = *reinterpret_cast<const u32x4*>(lds + L_O + 128 + 32 * w + 4 * (lane >> 3));
#pragma unroll
      for (int k = 0; k < 4; ++k) pair_mac2(wov[2 * k], wov[2 * k + 1], o0[k], o1[k], a0, a1);
      const float s0 = rs8_hi(a0, lane), s1 = rs8_hi(a1, lane);
      const int col = (lane & 7) * 8 + (lane >> 3);
      ldsf[L_RED2 + w * 128 + col] = s0; ldsf[L_RED2 + w * 128 + 64 + col] = s1;
      __syncthreads();
      if (tid < 128) {
        const float a = (ldsf[L_RED2 + tid] + ldsf[L_RED2 + 128 + tid]) + (ldsf[L_RED2 + 256 + tid] + ldsf[L_RED2 + 384 + tid]);
        st_gran((gu64*)(ws + WS_P2) + (size_t)(ii * 16 + kvh) * 128 + tid, tag, __float_as_uint(a));
      }
      if (w == 0) STAMP(8);
    }
    return;
  }

  {
    const u32 tag = __builtin_amdgcn_readfirstlane(lds[L_FLAG]);
    float a0[8], a1[8];
    STAMP(11);

    int task = -1;
    if ((s & 3) == (g & 3)) task = g * 2 + (s >> 2);
    else if ((g >> 2) == s && (g & 3) == ((s + 1) & 3)) task = 64 + s;
    if (task >= 0) {
      const int rr = lane >> 3, cc = lane & 7;
      const int vo = rr * 72 * (FF * 2) + cc * 16;
      const u32* xp = lds + L_XP + rr * 36;
      u32x4 A[8], Bf[8];
      float c0[8], c1[8];
      const __amdgpu_buffer_rsrc_t rs_g = mkrs(wg, ABL_GU), rs_u = mkrs(wu, ABL_GU);
      const __amdgpu_buffer_rsrc_t rs_d = mkrs(wd + (size_t)task * 64 * DM, 64 * DM * 2);
#define FISSUE(BUF, RS, KB) { const int so_ = (KB) < 9 ? task * 128 + (KB) * 8 * (FF * 2) : DM * FF * 2; \
      _Pragma("unroll") for (int j_ = 0; j_ < 8; ++j_) BUF[j_] = bld<AUX_NT>(RS, vo, so_ + j_ * (FF * 2)); } SB
#define MAC4(BUF, X0, X1, C0, C1) { _Pragma("unroll") for (int j_ = 0; j_ < 4; ++j_) { pair_mac2(BUF[2 * j_], BUF[2 * j_ + 1], X0[j_], X1[j_], C0, C1); pin8(C0); pin8(C1); SB; } }
#define FZERO(C0, C1) _Pragma("unroll") for (int k_ = 0; k_ < 8; ++k_) { C0[k_] = 0.f; C1[k_] = 0.f; }
#define DISSUE(BUF, R0) _Pragma("unroll") for (int j_ = 0; j_ < 8; ++j_) BUF[j_] = bld<AUX_NT>(rs_d, lane * 16, ((R0) + j_) * (DM * 2)); SB
      FISSUE(A, rs_g, 0); FISSUE(Bf, rs_u, 0);
      FZERO(a0, a1); FZERO(c0, c1);
#pragma unroll 1
      for (int k = 0; k < 9; ++k) {
        const u32x4 x0 = *reinterpret_cast<const u32x4*>(xp + 4 * k), x1 = *reinterpret_cast<const u32x4*>(xp + 288 + 4 * k);
        MAC4(A, x0, x1, a0, a1); FISSUE(A, rs_g, k + 1);
        MAC4(Bf, x0, x1, c0, c1); FISSUE(Bf, rs_u, k + 1);
      }
      DISSUE(A, 0); DISSUE(Bf, 8);
      {
        const float gs0 = rs8_hi(a0, lane), gs1 = rs8_hi(a1, lane), us0 = rs8_hi(c0, lane), us1 = rs8_hi(c1, lane);
        const int col = cc * 8 + rr;
        const float g0 = rbf(gs0), g1 = rbf(gs1);
        const float e0 = rbf(0.5f * g0 * (1.0f + tanhf(0.7978845608028654f * (g0 + 0.044715f * g0 * g0 * g0))));
        const float e1 = rbf(0.5f * g1 * (1.0f + tanhf(0.7978845608028654f * (g1 + 0.044715f * g1 * g1 * g1))));
        reinterpret_cast<u16*>(lds + L_H)[col] = (u16)f2bf(e0 * rbf(us0));
        reinterpret_cast<u16*>(lds + L_H)[64 + col] = (u16)f2bf(e1 * rbf(us1));
      }
      __builtin_amdgcn_fence(__ATOMIC_RELEASE, "wavefront"); __builtin_amdgcn_wave_barrier(); __builtin_amdgcn_fence(__ATOMIC_ACQUIRE, "wavefront");
      const u32* hp = lds + L_H;
      FZERO(a0, a1);
#pragma unroll 1
      for (int k = 0; k < 4; ++k) {
        const u32x4 h0 = *reinterpret_cast<const u32x4*>(hp + 8 * k), h1 = *reinterpret_cast<const u32x4*>(hp + 32 + 8 * k);
        const u32x4 h2 = *reinterpret_cast<const u32x4*>(hp + 8 * k + 4), h3 = *reinterpret_cast<const u32x4*>(hp + 32 + 8 * k + 4);
        MAC4(A, h0, h1, a0, a1); DISSUE(A, 16 * k + 16);
        MAC4(Bf, h2, h3, a0, a1); DISSUE(Bf, 16 * k + 24);
      }
#pragma unroll
      for (int j = 0; j < 8; ++j) A[j] = bld<AUX_NT>(rs_d, rr * 8 * (DM * 2) + 1024 + cc * 16, j * (DM * 2));
      SB;
      float* fo = ldsf + L_FO;
      *reinterpret_cast<f32x4*>(fo + rr * 128 + cc * 8) = f32x4{a0[0], a0[1], a0[2], a0[3]};
      *reinterpret_cast<f32x4*>(fo + rr * 128 + cc * 8 + 4) = f32x4{a0[4], a0[5], a0[6], a0[7]};
      *reinterpret_cast<f32x4*>(fo + rr * 128 + 64 + cc * 8) = f32x4{a1[0], a1[1], a1[2], a1[3]};
      *reinterpret_cast<f32x4*>(fo + rr * 128 + 64 + cc * 8 + 4) = f32x4{a1[4], a1[5], a1[6], a1[7]};
      FZERO(a0, a1);
      {
        const u32x4 h0 = *reinterpret_cast<const u32x4*>(hp + 4 * rr), h1 = *reinterpret_cast<const u32x4*>(hp + 32 + 4 * rr);
        MAC4(A, h0, h1, a0, a1);
      }
      const float e0 = rs8_hi(a0, lane), e1 = rs8_hi(a1, lane);
      fo[1024 + cc * 8 + rr] = e0; fo[1024 + 64 + cc * 8 + rr] = e1;
      __builtin_amdgcn_fence(__ATOMIC_RELEASE, "wavefront"); __builtin_amdgcn_wave_barrier(); __builtin_amdgcn_fence(__ATOMIC_ACQUIRE, "wavefront");
      const __amdgpu_buffer_rsrc_t rs_pf = mkrs(ws + WS_PF, ABL_PF);
#pragma unroll
      for (int k = 0; k < 9; ++k) {
        const float2 v = *reinterpret_cast<const float2*>(fo + k * 128 + lane * 2);
        __builtin_amdgcn_raw_buffer_store_b128(u32x4{__float_as_uint(v.x), tag, __float_as_uint(v.y), tag}, rs_pf, lane * 16, (k * NFT + task) * 1024, AUX_SC1);
      }
      STAMP(12);
    }

    const bool is_red = (ii == 15 && kvh < 9);
    float f0 = 0.f, f1 = 0.f; u32 xpair = 0;
    if (is_red) {
      xpair = reinterpret_cast<const u32*>(x)[hf * 288 + kvh * 32 + l5];
      const __amdgpu_buffer_rsrc_t rs_f = mkrs(ws + WS_PF + (size_t)kvh * NFT * 1024, NFT * 1024);
      for (int k0 = 0; k0 < NFT; k0 += 12) {
        float p0, p1;
        for (u32 spins = 0;; ++spins) {
          bool ok = true; p0 = 0.f; p1 = 0.f;
          u32x4 v[12];
#pragma unroll
          for (int k = 0; k < 12; ++k) v[k] = bld<AUX_SC1>(rs_f, lane * 16, (k0 + k) * 1024);
#pragma unroll
          for (int k = 0; k < 12; ++k) { p0 += __uint_as_float(v[k][0]); p1 += __uint_as_float(v[k][2]); ok &= v[k][1] == tag && v[k][3] == tag; }
          if (__all(ok) || spins > SPIN_CAP) break;
          __builtin_amdgcn_s_sleep(64);
        }
        f0 += p0; f1 += p1;
      }
      STAMP(13);
    }
    __syncthreads();
    if (is_red) {
      const __amdgpu_buffer_rsrc_t rs_y = mkrs(ws + WS_P2 + (size_t)kvh * 16 * 1024, 16 * 1024);
      float y0, y1;
      for (u32 spins = 0;; ++spins) {
        bool ok = true; y0 = 0.f; y1 = 0.f;
        u32x4 v[16];
#pragma unroll
        for (int k = 0; k < 16; ++k) v[k] = bld<AUX_SC1>(rs_y, lane * 16, k * 1024);
#pragma unroll
        for (int k = 0; k < 16; ++k) { y0 += __uint_as_float(v[k][0]); y1 += __uint_as_float(v[k][2]); ok &= v[k][1] == tag && v[k][3] == tag; }
        if (__all(ok) || spins > SPIN_CAP) break;
        __builtin_amdgcn_s_sleep(2);
      }
      const u32 o0 = f2bf(rbf(bf2f(xpair & 0xffffu) + rbf(y0)) + rbf(f0)), o1 = f2bf(rbf(bf2f(xpair >> 16) + rbf(y1)) + rbf(f1));
      reinterpret_cast<u32*>(out)[hf * 288 + kvh * 32 + l5] = o0 | (o1 << 16);
      if (kvh == 0 && lane == 0) __hip_atomic_store((gu32*)(ws + WS_EPOCH), tag, RLX_AGENT);
      STAMP(14);
    }
  }
}

extern "C" void kernel_launch(void* const* d_in, const int* in_sizes, int n_in, void* d_out, int out_size, void* d_ws, size_t ws_size,
                              hipStream_t stream) {
  (void)in_sizes; (void)n_in; (void)out_size; (void)ws_size;
  hipLaunchKernelGGL(fused_decode_kernel, dim3(256), dim3(NTHR), 0, stream, (const u16*)d_in[0], (const u16*)d_in[1], (const u16*)d_in[2],
                     (const u16*)d_in[3], (const u16*)d_in[4], (const u16*)d_in[6], (const u16*)d_in[7], (const u16*)d_in[8], (u16*)d_out,
                     (unsigned char*)d_ws);
}
```

```cpp
#define FIXT 8800
#include <hip/hip_runtime.h>
#include <hip/hip_bf16.h>
#include <stdint.h>

typedef unsigned u32;
typedef unsigned short u16;
typedef unsigned long long u64;
typedef u32 u32x4 __attribute__((ext_vector_type(4)));
typedef float f32x4 __attribute__((ext_vector_type(4)));
typedef __bf16 bf16x2 __attribute__((ext_vector_type(2)));
#define GAS __attribute__((address_space(1)))
#define LAS __attribute__((address_space(3)))
typedef GAS u64 gu64;
typedef GAS u32 gu32;
#define RLX_AGENT __ATOMIC_RELAXED, __HIP_MEMORY_SCOPE_AGENT
#define SB __builtin_amdgcn_sched_barrier(0)

constexpr int DM = 576, SK = 32768, DH = 128, FF = 4608, NQ = 4096;
constexpr int NSW = 4, NTHR = (NSW + 1) * 64, U = 4;
constexpr int TB = U * 1024;
constexpr int NT = SK / 16;
constexpr int RT = NT / 8;
#define NSTAT 48
#define THR 4
#define WQ_AUX 2
#define ABL_WO 9
#define ABL_WQ (DM * NQ * 2)
#define ABL_PF (9 * NFT * 1024)
#define ABL_GU (DM * FF * 2)
#define NE 4
constexpr int NS = NSTAT;
constexpr int NPOOL = (RT - NS * NSW) * 8;
constexpr int NFT = 72;
constexpr int AUX_NT = 2, AUX_SC1 = 16;
constexpr float LOG2E = 1.4426950408889634f;
constexpr u32 SPIN_CAP = 1u << 16;

constexpr size_t WS_EPOCH = 0;
constexpr size_t WS_CTR = 4096;
constexpr size_t WS_QG = WS_CTR + 32 * 256;
constexpr size_t WS_P1 = WS_QG + 2 * 2048 * 8;
constexpr int P1_STRIDE = 264;
constexpr size_t WS_P2 = WS_P1 + (size_t)256 * P1_STRIDE * 8;
constexpr size_t WS_PF = WS_P2 + (size_t)9 * 16 * 128 * 8;
constexpr size_t WS_END = WS_PF + (size_t)9 * NFT * 128 * 8;

__device__ __forceinline__ float bf2f(u32 lo16) { return __uint_as_float(lo16 << 16); }
__device__ __forceinline__ float rbf(float f) { return (float)(__bf16)f; }
__device__ __forceinline__ u32 f2bf(float f) { const __bf16 h = (__bf16)f; return (u32)__builtin_bit_cast(u16, h); }
__device__ __forceinline__ float dot2(u32 a, u32 b, float c) {
  return __builtin_amdgcn_fdot2_f32_bf16(__builtin_bit_cast(bf16x2, a), __builtin_bit_cast(bf16x2, b), c, false);
}
template <int CTRL> __device__ __forceinline__ float dpp(float x) {
  return __int_as_float(__builtin_amdgcn_update_dpp(0, __float_as_int(x), CTRL, 0xf, 0xf, true));
}
constexpr int DPP_XOR1 = 0xB1, DPP_XOR2 = 0x4E, DPP_HMIR = 0x141, DPP_ROR8 = 0x128, DPP_ROR4 = 0x124, DPP_BC = 0x150;
__device__ __forceinline__ void swap32(float& a, float& b) {
  auto r = __builtin_amdgcn_permlane32_swap(__float_as_uint(a), __float_as_uint(b), false, false);
  a = __uint_as_float(r[0]); b = __uint_as_float(r[1]);
}
__device__ __forceinline__ void swap16(float& a, float& b) {
  auto r = __builtin_amdgcn_permlane16_swap(__float_as_uint(a), __float_as_uint(b), false, false);
  a = __uint_as_float(r[0]); b = __uint_as_float(r[1]);
}
__device__ __forceinline__ float half_sum(float x) {
  x += dpp<DPP_XOR1>(x); x += dpp<DPP_XOR2>(x); x += dpp<DPP_ROR4>(x); x += dpp<DPP_ROR8>(x);
  float a = x, b = x; swap16(a, b); return a + b;
}
__device__ __forceinline__ __amdgpu_buffer_rsrc_t mkrs(const void* p, u32 bytes) {
  return __builtin_amdgcn_make_buffer_rsrc(const_cast<void*>(p), 0, bytes, 0x00020000);
}
template <int AUX> __device__ __forceinline__ u32x4 bld(__amdgpu_buffer_rsrc_t rs, int voff, int soff) {
  return __builtin_amdgcn_raw_buffer_load_b128(rs, voff, soff, AUX);
}
__device__ __forceinline__ void pair_mac2(const u32x4& a, const u32x4& b, u32 xp0, u32 xp1, float* acc0, float* acc1) {
#pragma unroll
  for (int d = 0; d < 4; ++d) {
    const u32 lo = __builtin_amdgcn_perm(b[d], a[d], 0x05040100u), hi = __builtin_amdgcn_perm(b[d], a[d], 0x07060302u);
    acc0[2 * d] = dot2(lo, xp0, acc0[2 * d]); acc0[2 * d + 1] = dot2(hi, xp0, acc0[2 * d + 1]);
    acc1[2 * d] = dot2(lo, xp1, acc1[2 * d]); acc1[2 * d + 1] = dot2(hi, xp1, acc1[2 * d + 1]);
  }
}
__device__ __forceinline__ float rs8_hi(float* v, int lane) {
#pragma unroll
  for (int k = 0; k < 4; ++k) { swap32(v[k], v[k + 4]); v[k] += v[k + 4]; }
#pragma unroll
  for (int k = 0; k < 2; ++k) { swap16(v[k], v[k + 2]); v[k] += v[k + 2]; }
  const bool up = lane & 8;
  const float keep = up ? v[1] : v[0], send = up ? v[0] : v[1];
  return keep + dpp<DPP_ROR8>(send);
}
__device__ __forceinline__ void pin8(float* a) {
  asm volatile("" : "+v"(a[0]), "+v"(a[1]), "+v"(a[2]), "+v"(a[3]), "+v"(a[4]), "+v"(a[5]), "+v"(a[6]), "+v"(a[7]));
}
__device__ __forceinline__ void st_gran(gu64* g, u32 tag, u32 v) { __hip_atomic_store(g, ((u64)tag << 32) | (u64)v, RLX_AGENT); }

#define STAMP(k) do {} while (0)

constexpr int L_XP = 0;
constexpr int L_Q = L_XP + 576;
constexpr int L_FLAG = L_Q + 128;
constexpr int L_H = L_FLAG + 4;
constexpr int L_O = L_H + 64;
constexpr int L_ML = L_O + 256;
constexpr int L_RED = L_ML + 16;
constexpr int L_RED2 = L_RED + 1024;
constexpr int L_FO = L_RED2 + 512;
constexpr int L_STASH = L_FO + 1152;
constexpr int L_TOTAL = L_STASH + NSW * NE * 2048;

__global__ void __launch_bounds__(NTHR, 2)
fused_decode_kernel(const u16* __restrict__ x, const u16* __restrict__ kc, const u16* __restrict__ vc, const u16* __restrict__ wq,
                    const u16* __restrict__ wo, const u16* __restrict__ wg, const u16* __restrict__ wu, const u16* __restrict__ wd,
                    u16* __restrict__ out, unsigned char* __restrict__ ws) {
  const u64 tstart_ = __builtin_amdgcn_s_memrealtime();
  __shared__ __attribute__((aligned(16))) u32 lds[L_TOTAL];
  float* ldsf = reinterpret_cast<float*>(lds);
  const int tid = (int)threadIdx.x - 64, lane = threadIdx.x & 63;
  const int w = __builtin_amdgcn_readfirstlane((int)(threadIdx.x >> 6)) - 1;
  const int bid = blockIdx.x;
  const int g = bid >> 3, s = bid & 7, b = g >> 4, kvh = g & 15;
  const int ii = b * 8 + s;

  const __amdgpu_buffer_rsrc_t rs_k = mkrs(kc + (size_t)g * SK * DH, SK * DH * 2);
  const __amdgpu_buffer_rsrc_t rs_v = mkrs(vc + (size_t)g * SK * DH, SK * DH * 2);
  const int kv_vo = lane * 16;
  const int t0 = s * RT + w;
  const int hf = lane >> 5, l5 = lane & 31;
  const int qs = s * 32 + g;
  u32x4 KA[U], VA[U];
#define ISSUE_K(BUF, T) _Pragma("unroll") for (int i_ = 0; i_ < U; ++i_) BUF[i_] = bld<AUX_NT>(rs_k, kv_vo + i_ * 1024, (T) * TB); SB
#define ISSUE_V(BUF, T) _Pragma("unroll") for (int i_ = 0; i_ < U; ++i_) BUF[i_] = bld<AUX_NT>(rs_v, kv_vo + i_ * 1024, (T) * TB); SB
  if (w == 0) STAMP(0);
  { const int tf = w < 0 ? NT : t0; ISSUE_K(KA, tf); ISSUE_V(VA, tf); }
  volatile LAS u32* lflag = (volatile LAS u32*)(lds + L_FLAG);
  if (threadIdx.x == 0) lflag[1] = 0u;
  asm volatile("; ROLE SPLIT 1" ::: "memory");
  if (w < 0) {
    __builtin_amdgcn_s_setprio(3);
    u32x4 wqv[18];
    u32 xr[9];
    const u32* xw = reinterpret_cast<const u32*>(x) + hf * 288 + l5;
#pragma unroll
    for (int j = 0; j < 9; ++j) xr[j] = xw[j * 32];
    const u32 epoch_v = __hip_atomic_load((gu32*)(ws + WS_EPOCH), RLX_AGENT);
    const __amdgpu_buffer_rsrc_t rs_wq = mkrs(wq, ABL_WQ);
    const int vo = (lane >> 1) * 18 * (NQ * 2) + (lane & 1) * 16;
#pragma unroll
    for (int j = 0; j < 18; ++j) wqv[j] = bld<WQ_AUX>(rs_wq, vo, qs * 32 + j * (NQ * 2));
    SB;
    __syncthreads();
    {
      float v[18]; float sum = 0.f;
#pragma unroll
      for (int j = 0; j < 9; ++j) { v[2 * j] = bf2f(xr[j] & 0xffffu); v[2 * j + 1] = bf2f(xr[j] >> 16); sum += v[2 * j] + v[2 * j + 1]; }
      const float mean = half_sum(sum) * (1.0f / DM);
      float sq = 0.f;
#pragma unroll
      for (int j = 0; j < 18; ++j) { const float d = v[j] - mean; sq = fmaf(d, d, sq); }
      const float var = half_sum(sq) * (1.0f / DM);
      const float mb = rbf(mean), rsd = 1.0f / rbf(sqrtf(rbf(rbf(var) + 1e-5f)));
#pragma unroll
      for (int j = 0; j < 9; ++j)
        lds[L_XP + hf * 288 + j * 32 + l5] = f2bf(rbf(v[2 * j] - mb) * rsd) | (f2bf(rbf(v[2 * j + 1] - mb) * rsd) << 16);
    }
    __builtin_amdgcn_fence(__ATOMIC_RELEASE, "wavefront"); __builtin_amdgcn_wave_barrier(); __builtin_amdgcn_fence(__ATOMIC_ACQUIRE, "wavefront");
    const u32 tag = __builtin_amdgcn_readfirstlane(epoch_v) + 0x9E3779B9u;
    const u32 ctag = tag & 0xfffff000u;
    STAMP(9);
    float a0[8], a1[8];
#pragma unroll
    for (int k = 0; k < 8; ++k) { a0[k] = 0.f; a1[k] = 0.f; }
    {
      const u32* xp = lds + L_XP + (lane >> 1) * 9;
#pragma unroll
      for (int j = 0; j < 9; ++j) pair_mac2(wqv[2 * j], wqv[2 * j + 1], xp[j], xp[288 + j], a0, a1);
      float s0 = rs8_hi(a0, lane), s1 = rs8_hi(a1, lane);
      const bool up = lane & 4;
      const float keep = up ? s1 : s0, send = up ? s0 : s1;
      float qv = keep + dpp<0x1B>(dpp<DPP_HMIR>(send));
      qv += dpp<DPP_XOR2>(qv);
      const u32 qb = f2bf(qv);
      const u32 other = (u32)__builtin_amdgcn_update_dpp(0, (int)qb, DPP_ROR8, 0xf, 0xf, true);
      if ((lane & 0xA) == 0)
        st_gran((gu64*)(ws + WS_QG) + ((lane >> 2) & 1) * 2048 + qs * 8 + (lane & 1) * 4 + (lane >> 4), tag, qb | (other << 16));
    }
    if (s == 0 && lane == 0) __hip_atomic_store((gu32*)(ws + WS_CTR + g * 256), ctag, RLX_AGENT);
    STAMP(10);
    {
      const gu64* qsrc = (const gu64*)(ws + WS_QG) + b * 2048 + kvh * 128 + lane;
      u32 qa, qb;
      for (u32 spins = 0;; ++spins) {
        const u64 v0 = __hip_atomic_load(qsrc, RLX_AGENT), v1 = __hip_atomic_load(qsrc + 64, RLX_AGENT);
        const u32 cw = __hip_atomic_load((const gu32*)(ws + WS_CTR + g * 256), RLX_AGENT);
        qa = (u32)v0; qb = (u32)v1;
        const bool ok = (u32)(v0 >> 32) == tag && (u32)(v1 >> 32) == tag && (cw & 0xfffff000u) == ctag;
        if (__all(ok) || spins > SPIN_CAP) break;
        __builtin_amdgcn_s_sleep(1);
      }
      lds[L_Q + lane] = qa; lds[L_Q + 64 + lane] = qb;
      lds[L_FLAG] = tag;
      __builtin_amdgcn_fence(__ATOMIC_RELEASE, "workgroup");
      lflag[1] = 1u;
    }
  }
  asm volatile("; ROLE SPLIT 2" ::: "memory");

  if (w >= 0) {
    u32x4 KB[U], VB[U], wov[8];
#define WAITV(N) __builtin_amdgcn_s_waitcnt(0x0f70 | ((N) & 15) | (((N) >> 4) << 14)); SB
    __syncthreads();
    u32x4* stash = reinterpret_cast<u32x4*>(lds + L_STASH + w * NE * 2048) + lane;
    int j = 1, ne = 0;
#pragma unroll 1
    while (ne < NE && lflag[1] == 0u) {
      WAITV(4);
#pragma unroll
      for (int i = 0; i < U; ++i) stash[ne * 512 + i * 64] = KA[i];
      SB; ISSUE_K(KA, t0 + j * NSW);
      WAITV(4);
#pragma unroll
      for (int i = 0; i < U; ++i) stash[ne * 512 + 256 + i * 64] = VA[i];
      SB; ISSUE_V(VA, t0 + j * NSW);
      ++ne; ++j;
    }
    if (w == 0) STAMP(1);
    while (lflag[1] == 0u) __builtin_amdgcn_s_sleep(1);
    __builtin_amdgcn_fence(__ATOMIC_ACQUIRE, "workgroup");
    if (w == 0) STAMP(2);
    const u32 tag = __builtin_amdgcn_readfirstlane(lds[L_FLAG]);
    const int r = lane >> 4, c = lane & 15;
    const u32x4 q0 = *reinterpret_cast<const u32x4*>(lds + L_Q + c * 4);
    const u32x4 q1 = *reinterpret_cast<const u32x4*>(lds + L_Q + 64 + c * 4);

    float acc0[8], acc1[8];
#pragma unroll
    for (int k = 0; k < 8; ++k) { acc0[k] = 0.f; acc1[k] = 0.f; }
    float mrun = -INFINITY, lrun = 0.f, p;
    auto qk = [&](const u32x4* Kt) {
      float xv[8];
#pragma unroll
      for (int i = 0; i < U; ++i) {
        float d0 = 0.f, d1 = 0.f;
#pragma unroll
        for (int k = 0; k < 4; ++k) { d0 = dot2(Kt[i][k], q0[k], d0); d1 = dot2(Kt[i][k], q1[k], d1); }
        xv[2 * i] = d0; xv[2 * i + 1] = d1;
      }
#pragma unroll
      for (int k = 0; k < 4; ++k) { const bool up = c & 8; const float keep = up ? xv[k + 4] : xv[k], send = up ? xv[k] : xv[k + 4]; xv[k] = keep + dpp<DPP_ROR8>(send); }
#pragma unroll
      for (int k = 0; k < 2; ++k) { const bool up = c & 4; const float keep = up ? xv[k + 2] : xv[k], send = up ? xv[k] : xv[k + 2]; xv[k] = keep + dpp<DPP_HMIR>(send); }
      { const bool up = c & 2; const float keep = up ? xv[1] : xv[0], send = up ? xv[0] : xv[1]; xv[0] = keep + dpp<DPP_XOR2>(send); }
      float sc = xv[0] + dpp<DPP_XOR1>(xv[0]);
      sc = rbf(rbf(sc) * 0.08838834764831845f);
      float pm = fmaxf(sc, dpp<DPP_ROR4>(sc)); pm = fmaxf(pm, dpp<DPP_ROR8>(pm));
      if (__any(pm > mrun)) {
        const float mn = fmaxf(mrun, pm);
        const float al = __builtin_amdgcn_exp2f((mrun - mn) * LOG2E);
        mrun = mn; lrun *= al;
        const float al0 = dpp<DPP_BC + 0>(al), al1 = dpp<DPP_BC + 2>(al);
#pragma unroll
        for (int k = 0; k < 8; ++k) { acc0[k] *= al0; acc1[k] *= al1; }
      }
      p = __builtin_amdgcn_exp2f((sc - mrun) * LOG2E);
      lrun += p;
    };
    auto pv1 = [&](const u32x4& Vt, float p0, float p1) {
#pragma unroll
      for (int k = 0; k < 4; ++k) {
        const float lo = __uint_as_float(Vt[k] << 16), hi = __uint_as_float(Vt[k] & 0xffff0000u);
        acc0[2 * k] = fmaf(p0, lo, acc0[2 * k]); acc0[2 * k + 1] = fmaf(p0, hi, acc0[2 * k + 1]);
        acc1[2 * k] = fmaf(p1, lo, acc1[2 * k]); acc1[2 * k + 1] = fmaf(p1, hi, acc1[2 * k + 1]);
      }
    };
    auto pv = [&](const u32x4* Vt) {
      pv1(Vt[0], dpp<DPP_BC + 0>(p), dpp<DPP_BC + 2>(p));
      pv1(Vt[1], dpp<DPP_BC + 4>(p), dpp<DPP_BC + 6>(p));
      pv1(Vt[2], dpp<DPP_BC + 8>(p), dpp<DPP_BC + 10>(p));
      pv1(Vt[3], dpp<DPP_BC + 12>(p), dpp<DPP_BC + 14>(p));
      pin8(acc0); pin8(acc1);
    };
    const int cvo = lane * 4;
    int claim = 0, ia = 0, ib;
    {
      const __amdgpu_buffer_rsrc_t rs_wo = mkrs(wo + (size_t)kvh * 256 * DM, ii < ABL_WO ? 256 * DM * 2 : 0);
      const int vo = (lane >> 3) * 8 * (DM * 2) + (lane & 7) * 16, so = 64 * w * (DM * 2) + ii * 128;
#pragma unroll
      for (int k = 0; k < 8; ++k) wov[k] = bld<AUX_NT>(rs_wo, vo, so + k * (DM * 2));
      SB;
    }
#define NEXT_TILE(CUR, NXT) { const int pi_ = __builtin_amdgcn_readfirstlane(claim) & 0xfff; \
      NXT = j < NS ? t0 + j * NSW : ((CUR) < NT && pi_ < NPOOL ? (pi_ & 7) * RT + NS * NSW + (pi_ >> 3) : NT); ++j; \
      claim = __builtin_amdgcn_raw_ptr_buffer_atomic_add_i32(1, mkrs(ws + WS_CTR + g * 256, (j >= NS && NXT < NT) ? 4 : 0), cvo, 0, 0); SB; }
    do {
      WAITV(4); NEXT_TILE(ia, ib); ISSUE_K(KB, ib); qk(KA); SB;
      WAITV(5); ISSUE_V(VB, ib); pv(VA); SB;
      if (ne > 0) {
        --ne;
        u32x4 KE[U], VE[U];
#pragma unroll
        for (int i = 0; i < U; ++i) { KE[i] = stash[ne * 512 + i * 64]; VE[i] = stash[ne * 512 + 256 + i * 64]; }
        qk(KE); pv(VE); SB;
      }
      WAITV(4); NEXT_TILE(ib, ia); ISSUE_K(KA, ia); if (ib < NT) qk(KB); SB;
      WAITV(5); ISSUE_V(VA, ia); if (ib < NT) pv(VB); SB;
    } while (ia < NT);
    if (w == 0) STAMP(5);

    {
      float lr = lrun + dpp<DPP_ROR4>(lrun); lr += dpp<DPP_ROR8>(lr);
      float a = mrun, bb = mrun; swap16(a, bb); float mw = fmaxf(a, bb);
      a = mw; bb = mw; swap32(a, bb); mw = fmaxf(a, bb);
      const float f = __builtin_amdgcn_exp2f((mrun - mw) * LOG2E);
      lr *= f;
      a = lr; bb = lr; swap16(a, bb); lr = a + bb;
      a = lr; bb = lr; swap32(a, bb); lr = a + bb;
      const float f0 = dpp<DPP_BC + 0>(f), f1 = dpp<DPP_BC + 2>(f);
#pragma unroll
      for (int k = 0; k < 8; ++k) { acc0[k] *= f0; acc1[k] *= f1; }
#pragma unroll
      for (int k = 0; k < 8; ++k) { swap32(acc0[k], acc1[k]); acc0[k] += acc1[k]; }
#pragma unroll
      for (int k = 0; k < 4; ++k) { swap16(acc0[k], acc0[k + 4]); acc0[k] += acc0[k + 4]; }
      *reinterpret_cast<f32x4*>(ldsf + L_RED + w * 256 + (r >> 1) * 128 + c * 8 + (r & 1) * 4) = f32x4{acc0[0], acc0[1], acc0[2], acc0[3]};
      if (lane == 0 || lane == 2) { ldsf[L_ML + w * 4 + lane] = mw; ldsf[L_ML + w * 4 + lane + 1] = lr; }
    }
    __syncthreads();
    gu64* p1 = (gu64*)(ws + WS_P1);
    const int h = w >> 1;
    {
      float M = -INFINITY;
#pragma unroll
      for (int k = 0; k < NSW; ++k) M = fmaxf(M, ldsf[L_ML + k * 4 + h * 2]);
      float o = 0.f, L = 0.f;
#pragma unroll
      for (int k = 0; k < NSW; ++k) {
        const float f = __builtin_amdgcn_exp2f((ldsf[L_ML + k * 4 + h * 2] - M) * LOG2E);
        o = fmaf(f, ldsf[L_RED + k * 256 + tid], o); L = fmaf(f, ldsf[L_ML + k * 4 + h * 2 + 1], L);
      }
      gu64* mine = p1 + (size_t)(kvh * 16 + ii) * P1_STRIDE;
      st_gran(mine + tid, tag, __float_as_uint(o));
      if ((tid & 127) < 2) st_gran(mine + 256 + h * 2 + (tid & 1), tag, __float_as_uint((tid & 1) ? L : M));
    }
    if (w == 0) STAMP(6);
    if (ii >= 9) return;

    {
      const gu64* src = p1 + (size_t)(kvh * 16) * P1_STRIDE;
      float av[16], ml[2];
      for (u32 spins = 0;; ++spins) {
        bool ok = true;
#pragma unroll
        for (int k = 0; k < 16; ++k) {
          const u64 v = __hip_atomic_load(src + k * P1_STRIDE + tid, RLX_AGENT);
          av[k] = __uint_as_float((u32)v); ok &= (u32)(v >> 32) == tag;
        }
#pragma unroll
        for (int bb = 0; bb < 2; ++bb) {
          const u64 v = __hip_atomic_load(src + (bb * 8 + (c >> 1)) * P1_STRIDE + 256 + h * 2 + (c & 1), RLX_AGENT);
          ml[bb] = __uint_as_float((u32)v); ok &= (u32)(v >> 32) == tag;
        }
        if (__all(ok) || spins > SPIN_CAP) break;
        __builtin_amdgcn_s_sleep(4);
      }
      if (w == 0) STAMP(7);
#pragma unroll
      for (int bb = 0; bb < 2; ++bb) {
        const float mv = dpp<0xA0>(ml[bb]);
        float M = fmaxf(mv, dpp<DPP_XOR2>(mv)); M = fmaxf(M, dpp<DPP_ROR4>(M)); M = fmaxf(M, dpp<DPP_ROR8>(M));
        const float f = __builtin_amdgcn_exp2f((mv - M) * LOG2E);
        float L = f * dpp<0xF5>(ml[bb]);
        L += dpp<DPP_XOR2>(L); L += dpp<DPP_ROR4>(L); L += dpp<DPP_ROR8>(L);
        const float* a = av + bb * 8;
        float o = a[0] * dpp<DPP_BC + 0>(f);
        o = fmaf(a[1], dpp<DPP_BC + 2>(f), o); o = fmaf(a[2], dpp<DPP_BC + 4>(f), o); o = fmaf(a[3], dpp<DPP_BC + 6>(f), o);
        o = fmaf(a[4], dpp<DPP_BC + 8>(f), o); o = fmaf(a[5], dpp<DPP_BC + 10>(f), o); o = fmaf(a[6], dpp<DPP_BC + 12>(f), o);
        o = fmaf(a[7], dpp<DPP_BC + 14>(f), o);
        reinterpret_cast<u16*>(lds + L_O)[bb * 256 + tid] = (u16)f2bf(o / L);
      }
      __builtin_amdgcn_fence(__ATOMIC_RELEASE, "wavefront"); __builtin_amdgcn_wave_barrier(); __builtin_amdgcn_fence(__ATOMIC_ACQUIRE, "wavefront");
      float a0[8], a1[8];
#pragma unroll
      for (int k = 0; k < 8; ++k) { a0[k] = 0.f; a1[k] = 0.f; }
      const u32x4 o0 = *reinterpret_cast<const u32x4*>(lds + L_O + 32 * w + 4 * (lane >> 3));
      const u32x4 o1 = *reinterpret_cast<const u32x4*>(lds + L_O + 128 + 32 * w + 4 * (lane >> 3));
#pragma unroll
      for (int k = 0; k < 4; ++k) pair_mac2(wov[2 * k], wov[2 * k + 1], o0[k], o1[k], a0, a1);
      const float s0 = rs8_hi(a0, lane), s1 = rs8_hi(a1, lane);
      const int col = (lane & 7) * 8 + (lane >> 3);
      ldsf[L_RED2 + w * 128 + col] = s0; ldsf[L_RED2 + w * 128 + 64 + col] = s1;
      __syncthreads();
      if (tid < 128) {
        const float a = (ldsf[L_RED2 + tid] + ldsf[L_RED2 + 128 + tid]) + (ldsf[L_RED2 + 256 + tid] + ldsf[L_RED2 + 384 + tid]);
        st_gran((gu64*)(ws + WS_P2) + (size_t)(ii * 16 + kvh) * 128 + tid, tag, __float_as_uint(a));
      }
      if (w == 0) STAMP(8);
    }
    return;
  }

  {
    const u32 tag = __builtin_amdgcn_readfirstlane(lds[L_FLAG]);
    float a0[8], a1[8];
    STAMP(11);

    int task = -1;
    if ((s & 3) == (g & 3)) task = g * 2 + (s >> 2);
    else if ((g >> 2) == s && (g & 3) == ((s + 1) & 3)) task = 64 + s;
    if (task >= 0) {
      const int rr = lane >> 3, cc = lane & 7;
      const int vo = rr * 72 * (FF * 2) + cc * 16;
      const u32* xp = lds + L_XP + rr * 36;
      u32x4 A[8], Bf[8];
      float c0[8], c1[8];
      const __amdgpu_buffer_rsrc_t rs_g = mkrs(wg, ABL_GU), rs_u = mkrs(wu, ABL_GU);
      const __amdgpu_buffer_rsrc_t rs_d = mkrs(wd + (size_t)task * 64 * DM, 64 * DM * 2);
#define FISSUE(BUF, RS, KB) { const int so_ = (KB) < 9 ? task * 128 + (KB) * 8 * (FF * 2) : DM * FF * 2; \
      _Pragma("unroll") for (int j_ = 0; j_ < 8; ++j_) BUF[j_] = bld<AUX_NT>(RS, vo, so_ + j_ * (FF * 2)); } SB
#define MAC4(BUF, X0, X1, C0, C1) { _Pragma("unroll") for (int j_ = 0; j_ < 4; ++j_) { pair_mac2(BUF[2 * j_], BUF[2 * j_ + 1], X0[j_], X1[j_], C0, C1); pin8(C0); pin8(C1); SB; } }
#define FZERO(C0, C1) _Pragma("unroll") for (int k_ = 0; k_ < 8; ++k_) { C0[k_] = 0.f; C1[k_] = 0.f; }
#define DISSUE(BUF, R0) _Pragma("unroll") for (int j_ = 0; j_ < 8; ++j_) BUF[j_] = bld<AUX_NT>(rs_d, lane * 16, ((R0) + j_) * (DM * 2)); SB
      FISSUE(A, rs_g, 0); FISSUE(Bf, rs_u, 0);
      FZERO(a0, a1); FZERO(c0, c1);
#pragma unroll 1
      for (int k = 0; k < 9; ++k) {
        const u32x4 x0 = *reinterpret_cast<const u32x4*>(xp + 4 * k), x1 = *reinterpret_cast<const u32x4*>(xp + 288 + 4 * k);
        MAC4(A, x0, x1, a0, a1); FISSUE(A, rs_g, k + 1);
        MAC4(Bf, x0, x1, c0, c1); FISSUE(Bf, rs_u, k + 1);
      }
      DISSUE(A, 0); DISSUE(Bf, 8);
      {
        const float gs0 = rs8_hi(a0, lane), gs1 = rs8_hi(a1, lane), us0 = rs8_hi(c0, lane), us1 = rs8_hi(c1, lane);
        const int col = cc * 8 + rr;
        const float g0 = rbf(gs0), g1 = rbf(gs1);
        const float e0 = rbf(0.5f * g0 * (1.0f + tanhf(0.7978845608028654f * (g0 + 0.044715f * g0 * g0 * g0))));
        const float e1 = rbf(0.5f * g1 * (1.0f + tanhf(0.7978845608028654f * (g1 + 0.044715f * g1 * g1 * g1))));
        reinterpret_cast<u16*>(lds + L_H)[col] = (u16)f2bf(e0 * rbf(us0));
        reinterpret_cast<u16*>(lds + L_H)[64 + col] = (u16)f2bf(e1 * rbf(us1));
      }
      __builtin_amdgcn_fence(__ATOMIC_RELEASE, "wavefront"); __builtin_amdgcn_wave_barrier(); __builtin_amdgcn_fence(__ATOMIC_ACQUIRE, "wavefront");
      const u32* hp = lds + L_H;
      FZERO(a0, a1);
#pragma unroll 1
      for (int k = 0; k < 4; ++k) {
        const u32x4 h0 = *reinterpret_cast<const u32x4*>(hp + 8 * k), h1 = *reinterpret_cast<const u32x4*>(hp + 32 + 8 * k);
        const u32x4 h2 = *reinterpret_cast<const u32x4*>(hp + 8 * k + 4), h3 = *reinterpret_cast<const u32x4*>(hp + 32 + 8 * k + 4);
        MAC4(A, h0, h1, a0, a1); DISSUE(A, 16 * k + 16);
        MAC4(Bf, h2, h3, a0, a1); DISSUE(Bf, 16 * k + 24);
      }
#pragma unroll
      for (int j = 0; j < 8; ++j) A[j] = bld<AUX_NT>(rs_d, rr * 8 * (DM * 2) + 1024 + cc * 16, j * (DM * 2));
      SB;
      float* fo = ldsf + L_FO;
      *reinterpret_cast<f32x4*>(fo + rr * 128 + cc * 8) = f32x4{a0[0], a0[1], a0[2], a0[3]};
      *reinterpret_cast<f32x4*>(fo + rr * 128 + cc * 8 + 4) = f32x4{a0[4], a0[5], a0[6], a0[7]};
      *reinterpret_cast<f32x4*>(fo + rr * 128 + 64 + cc * 8) = f32x4{a1[0], a1[1], a1[2], a1[3]};
      *reinterpret_cast<f32x4*>(fo + rr * 128 + 64 + cc * 8 + 4) = f32x4{a1[4], a1[5], a1[6], a1[7]};
      FZERO(a0, a1);
      {
        const u32x4 h0 = *reinterpret_cast<const u32x4*>(hp + 4 * rr), h1 = *reinterpret_cast<const u32x4*>(hp + 32 + 4 * rr);
        MAC4(A, h0, h1, a0, a1);
      }
      const float e0 = rs8_hi(a0, lane), e1 = rs8_hi(a1, lane);
      fo[1024 + cc * 8 + rr] = e0; fo[1024 + 64 + cc * 8 + rr] = e1;
      __builtin_amdgcn_fence(__ATOMIC_RELEASE, "wavefront"); __builtin_amdgcn_wave_barrier(); __builtin_amdgcn_fence(__ATOMIC_ACQUIRE, "wavefront");
      const __amdgpu_buffer_rsrc_t rs_pf = mkrs(ws + WS_PF, ABL_PF);
#pragma unroll
      for (int k = 0; k < 9; ++k) {
        const float2 v = *reinterpret_cast<const float2*>(fo + k * 128 + lane * 2);
        __builtin_amdgcn_raw_buffer_store_b128(u32x4{__float_as_uint(v.x), tag, __float_as_uint(v.y), tag}, rs_pf, lane * 16, (k * NFT + task) * 1024, AUX_SC1);
      }
      STAMP(12);
    }

    const bool is_red = (ii == 15 && kvh < 9);
    float f0 = 0.f, f1 = 0.f; u32 xpair = 0;
    if (is_red) {
      xpair = reinterpret_cast<const u32*>(x)[hf * 288 + kvh * 32 + l5];
      const __amdgpu_buffer_rsrc_t rs_f = mkrs(ws + WS_PF + (size_t)kvh * NFT * 1024, NFT * 1024);
      for (int k0 = 0; k0 < NFT; k0 += 12) {
        float p0, p1;
        for (u32 spins = 0;; ++spins) {
          bool ok = true; p0 = 0.f; p1 = 0.f;
          u32x4 v[12];
#pragma unroll
          for (int k = 0; k < 12; ++k) v[k] = bld<AUX_SC1>(rs_f, lane * 16, (k0 + k) * 1024);
#pragma unroll
          for (int k = 0; k < 12; ++k) { p0 += __uint_as_float(v[k][0]); p1 += __uint_as_float(v[k][2]); ok &= v[k][1] == tag && v[k][3] == tag; }
          if (__all(ok) || spins > SPIN_CAP) break;
          __builtin_amdgcn_s_sleep(64);
        }
        f0 += p0; f1 += p1;
      }
      STAMP(13);
    }
    __syncthreads();
    if (is_red) {
      const __amdgpu_buffer_rsrc_t rs_y = mkrs(ws + WS_P2 + (size_t)kvh * 16 * 1024, 16 * 1024);
      float y0, y1;
      for (u32 spins = 0;; ++spins) {
        bool ok = true; y0 = 0.f; y1 = 0.f;
        u32x4 v[16];
#pragma unroll
        for (int k = 0; k < 16; ++k) v[k] = bld<AUX_SC1>(rs_y, lane * 16, k * 1024);
#pragma unroll
        for (int k = 0; k < 16; ++k) { y0 += __uint_as_float(v[k][0]); y1 += __uint_as_float(v[k][2]); ok &= v[k][1] == tag && v[k][3] == tag; }
        if (__all(ok) || spins > SPIN_CAP) break;
        __builtin_amdgcn_s_sleep(2);
      }
      const u32 o0 = f2bf(rbf(bf2f(xpair & 0xffffu) + rbf(y0)) + rbf(f0)), o1 = f2bf(rbf(bf2f(xpair >> 16) + rbf(y1)) + rbf(f1));
      reinterpret_cast<u32*>(out)[hf * 288 + kvh * 32 + l5] = o0 | (o1 << 16);
      if (kvh == 0 && lane == 0) __hip_atomic_store((gu32*)(ws + WS_EPOCH), tag, RLX_AGENT);
      STAMP(14);
      while (__builtin_amdgcn_s_memrealtime() - tstart_ < FIXT) __builtin_amdgcn_s_sleep(1);
    }
  }
}

extern "C" void kernel_launch(void* const* d_in, const int* in_sizes, int n_in, void* d_out, int out_size, void* d_ws, size_t ws_size,
                              hipStream_t stream) {
  (void)in_sizes; (void)n_in; (void)out_size; (void)ws_size;
  hipLaunchKernelGGL(fused_decode_kernel, dim3(256), dim3(NTHR), 0, stream, (const u16*)d_in[0], (const u16*)d_in[1], (const u16*)d_in[2],
                     (const u16*)d_in[3], (const u16*)d_in[4], (const u16*)d_in[6], (const u16*)d_in[7], (const u16*)d_in[8], (u16*)d_out,
                     (unsigned char*)d_ws);
}
```

```cpp
#define NCTR 4
#define NSTAT 55
#define DRYRUN 1
#include <hip/hip_runtime.h>
#include <hip/hip_bf16.h>
#include <stdint.h>

typedef unsigned u32;
typedef unsigned short u16;
typedef unsigned long long u64;
typedef u32 u32x4 __attribute__((ext_vector_type(4)));
typedef u32 u32x2 __attribute__((ext_vector_type(2)));
typedef float f32x4 __attribute__((ext_vector_type(4)));
typedef __bf16 bf16x2 __attribute__((ext_vector_type(2)));
#define GAS __attribute__((address_space(1)))
#define LAS __attribute__((address_space(3)))
typedef GAS u64 gu64;
typedef GAS u32 gu32;
#define RLX_AGENT __ATOMIC_RELAXED, __HIP_MEMORY_SCOPE_AGENT
#define SB __builtin_amdgcn_sched_barrier(0)

constexpr int DM = 576, SK = 32768, DH = 128, FF = 4608, NQ = 4096;
constexpr int NSW = 4, NTHR = (NSW + 1) * 64, U = 4;
constexpr int TB = U * 1024;
constexpr int NT = SK / 16;
constexpr int RT = NT / 8;
#define GMIX 0
#define SVC_HEAD 1
#define HEAD_SLEEP 0
#define PREFILTER 0
#define QSLEEP 20
#define NE 8
#define WQ_AUX 2
constexpr int NS = NSTAT;
constexpr int NSF = NS - 8;
constexpr int PEX = (NS - NSF) * NSW;
constexpr int PCOM = (RT - NS * NSW) * 8;
static_assert(PEX == 32 && NE % 2 == 0 && NSF >= 4 * NE + 6 && PCOM + 3 * PEX + 128 < 4096, "");
constexpr int NFT = 72;
constexpr int AUX_NT = 2, AUX_SC1 = 16;
constexpr float LOG2E = 1.4426950408889634f;
constexpr u32 SPIN_CAP = 1u << 16;

constexpr size_t WS_EPOCH = 0;
constexpr size_t WS_CTR = 4096;
constexpr size_t WS_QG = WS_CTR + 128 * 256;
constexpr size_t WS_P1 = WS_QG + 2 * 64 * 4 * 64 * 8;
constexpr int P1_STRIDE = 264;
constexpr size_t WS_P2 = WS_P1 + (size_t)256 * P1_STRIDE * 8;
constexpr size_t WS_PF = WS_P2 + (size_t)9 * 16 * 128 * 8;
constexpr size_t WS_END = WS_PF + (size_t)9 * NFT * 128 * 8;

__device__ __forceinline__ float bf2f(u32 lo16) { return __uint_as_float(lo16 << 16); }
__device__ __forceinline__ float rbf(float f) { return (float)(__bf16)f; }
__device__ __forceinline__ u32 f2bf(float f) { const __bf16 h = (__bf16)f; return (u32)__builtin_bit_cast(u16, h); }
__device__ __forceinline__ float dot2(u32 a, u32 b, float c) {
  return __builtin_amdgcn_fdot2_f32_bf16(__builtin_bit_cast(bf16x2, a), __builtin_bit_cast(bf16x2, b), c, false);
}
template <int CTRL> __device__ __forceinline__ float dpp(float x) {
  return __int_as_float(__builtin_amdgcn_update_dpp(0, __float_as_int(x), CTRL, 0xf, 0xf, true));
}
constexpr int DPP_XOR1 = 0xB1, DPP_XOR2 = 0x4E, DPP_HMIR = 0x141, DPP_ROR8 = 0x128, DPP_ROR4 = 0x124, DPP_BC = 0x150;
__device__ __forceinline__ void swap32(float& a, float& b) {
  auto r = __builtin_amdgcn_permlane32_swap(__float_as_uint(a), __float_as_uint(b), false, false);
  a = __uint_as_float(r[0]); b = __uint_as_float(r[1]);
}
__device__ __forceinline__ void swap16(float& a, float& b) {
  auto r = __builtin_amdgcn_permlane16_swap(__float_as_uint(a), __float_as_uint(b), false, false);
  a = __uint_as_float(r[0]); b = __uint_as_float(r[1]);
}
__device__ __forceinline__ float half_sum(float x) {
  x += dpp<DPP_XOR1>(x); x += dpp<DPP_XOR2>(x); x += dpp<DPP_ROR4>(x); x += dpp<DPP_ROR8>(x);
  float a = x, b = x; swap16(a, b); return a + b;
}
__device__ __forceinline__ __amdgpu_buffer_rsrc_t mkrs(const void* p, u32 bytes) {
  return __builtin_amdgcn_make_buffer_rsrc(const_cast<void*>(p), 0, bytes, 0x00020000);
}
template <int AUX> __device__ __forceinline__ u32x4 bld(__amdgpu_buffer_rsrc_t rs, int voff, int soff) {
  return __builtin_amdgcn_raw_buffer_load_b128(rs, voff, soff, AUX);
}
__device__ __forceinline__ void pair_mac2(const u32x4& a, const u32x4& b, u32 xp0, u32 xp1, float* acc0, float* acc1) {
#pragma unroll
  for (int d = 0; d < 4; ++d) {
    const u32 lo = __builtin_amdgcn_perm(b[d], a[d], 0x05040100u), hi = __builtin_amdgcn_perm(b[d], a[d], 0x07060302u);
    acc0[2 * d] = dot2(lo, xp0, acc0[2 * d]); acc0[2 * d + 1] = dot2(hi, xp0, acc0[2 * d + 1]);
    acc1[2 * d] = dot2(lo, xp1, acc1[2 * d]); acc1[2 * d + 1] = dot2(hi, xp1, acc1[2 * d + 1]);
  }
}
__device__ __forceinline__ float rs8_hi(float* v, int lane) {
#pragma unroll
  for (int k = 0; k < 4; ++k) { swap32(v[k], v[k + 4]); v[k] += v[k + 4]; }
#pragma unroll
  for (int k = 0; k < 2; ++k) { swap16(v[k], v[k + 2]); v[k] += v[k + 2]; }
  const bool up = lane & 8;
  const float keep = up ? v[1] : v[0], send = up ? v[0] : v[1];
  return keep + dpp<DPP_ROR8>(send);
}
__device__ __forceinline__ void pin8(float* a) {
  asm volatile("" : "+v"(a[0]), "+v"(a[1]), "+v"(a[2]), "+v"(a[3]), "+v"(a[4]), "+v"(a[5]), "+v"(a[6]), "+v"(a[7]));
}
__device__ __forceinline__ void st_gran(gu64* g, u32 tag, u32 v) { __hip_atomic_store(g, ((u64)tag << 32) | (u64)v, RLX_AGENT); }

#define STAMP(k) do {} while (0)

constexpr int L_XP = 0;
constexpr int L_Q = L_XP + 576;
constexpr int L_FLAG = L_Q + 128;
constexpr int L_H = L_FLAG + 4;
constexpr int L_FO = L_H + 64;
constexpr int L_SC = L_FO + 1152;
constexpr int L_STASH = L_SC + NSW * 64 * 16;
constexpr int L_O = L_STASH;
constexpr int L_RED = 1024;
constexpr int L_TOTAL = L_STASH + NSW * NE * 1024;

__global__ void __launch_bounds__(NTHR, 2)
fused_decode_kernel(const u16* __restrict__ x, const u16* __restrict__ kc, const u16* __restrict__ vc, const u16* __restrict__ wq,
                    const u16* __restrict__ wo, const u16* __restrict__ wg, const u16* __restrict__ wu, const u16* __restrict__ wd,
                    u16* __restrict__ out, unsigned char* __restrict__ ws) {
  __shared__ __attribute__((aligned(16))) u32 lds[L_TOTAL];
  asm volatile("" :: "s"(x), "s"(kc), "s"(vc), "s"(wq), "s"(ws));
  float* ldsf = reinterpret_cast<float*>(lds);
  const int tid = (int)threadIdx.x - 64, lane = threadIdx.x & 63;
  const int w = __builtin_amdgcn_readfirstlane((int)(threadIdx.x >> 6)) - 1;
  const int bid = blockIdx.x;
  const int s = bid & 7, g = ((bid >> 3) + GMIX * s) & 31, b = g >> 4, kvh = g & 15;
  const int ii = b * 8 + s;

  const u16* kbase = kc + (size_t)g * SK * DH;
  const u16* vbase = vc + (size_t)g * SK * DH;
  const int kv_vo = lane * 16, cvo = lane * 4;
  const int t0 = s * RT + w;
  const int hf = lane >> 5, l5 = lane & 31;
  const int sub = (w + s) & (NCTR - 1);
  const int qcg = g + 32 * (s >> 2), qkq = s & 3;
  u32x4 B0[U], B1[U];
  int c0, c1;
#define ISSUE(BUF, CL, BASE, T, REAL) { CL = __builtin_amdgcn_raw_ptr_buffer_atomic_add_i32(1, mkrs(ws + WS_CTR + g * 1024 + sub * 256, (REAL) ? 4 : 0), cvo, 0, 0); SB; \
    const __amdgpu_buffer_rsrc_t rs_ = mkrs(BASE, SK * DH * 2); const int so_ = (T) * TB; \
    _Pragma("unroll") for (int i_ = 0; i_ < U; ++i_) BUF[i_] = bld<AUX_NT>(rs_, kv_vo + i_ * 1024, so_); SB; }
  if (w == 0) STAMP(0);
  volatile LAS u32* lflag = (volatile LAS u32*)(lds + L_FLAG);
  if (threadIdx.x == 0) lflag[1] = 0u;
  asm volatile("; ROLE SPLIT 1" ::: "memory");
  if (w < 0) {
    __builtin_amdgcn_s_setprio(3);
    u32x4 wqv[18];
    u32 xr[9];
    {
      const __amdgpu_buffer_rsrc_t rs_x = mkrs(x, 2 * DM * 2);
      const u32x4 xa = bld<0>(rs_x, lane * 36, 0), xb = bld<0>(rs_x, lane * 36 + 16, 0);
      xr[8] = __builtin_amdgcn_raw_buffer_load_b32(rs_x, lane * 36 + 32, 0, 0);
#pragma unroll
      for (int j = 0; j < 4; ++j) { xr[j] = xa[j]; xr[4 + j] = xb[j]; }
    }
    const u32 epoch_v = __hip_atomic_load((gu32*)(ws + WS_EPOCH), RLX_AGENT);
    const __amdgpu_buffer_rsrc_t rs_wq = mkrs(wq, DM * NQ * 2);
    const int vo = (lane >> 3) * 18 * (NQ * 2) + (lane & 7) * 16;
#pragma unroll
    for (int j = 0; j < 18; ++j) wqv[j] = bld<AUX_NT>(rs_wq, vo, qcg * 128 + (qkq * 144 + j) * (NQ * 2));
    SB;
    __syncthreads();
    {
      float v[18]; float sum = 0.f;
#pragma unroll
      for (int j = 0; j < 9; ++j) { v[2 * j] = bf2f(xr[j] & 0xffffu); v[2 * j + 1] = bf2f(xr[j] >> 16); sum += v[2 * j] + v[2 * j + 1]; }
      const float mean = half_sum(sum) * (1.0f / DM);
      float sq = 0.f;
#pragma unroll
      for (int j = 0; j < 18; ++j) { const float d = v[j] - mean; sq = fmaf(d, d, sq); }
      const float var = half_sum(sq) * (1.0f / DM);
      const float mb = rbf(mean), rsd = 1.0f / rbf(sqrtf(rbf(rbf(var) + 1e-5f)));
#pragma unroll
      for (int j = 0; j < 9; ++j)
        lds[L_XP + lane * 9 + j] = f2bf(rbf(v[2 * j] - mb) * rsd) | (f2bf(rbf(v[2 * j + 1] - mb) * rsd) << 16);
    }
    __builtin_amdgcn_fence(__ATOMIC_RELEASE, "wavefront"); __builtin_amdgcn_wave_barrier(); __builtin_amdgcn_fence(__ATOMIC_ACQUIRE, "wavefront");
    const u32 tag = __builtin_amdgcn_readfirstlane(epoch_v) + 0x9E3779B9u;
    const u32 ctag = tag & 0xfffff000u;
    STAMP(9);
    float a0[8], a1[8];
#pragma unroll
    for (int k = 0; k < 8; ++k) { a0[k] = 0.f; a1[k] = 0.f; }
    {
      const u32* xp = lds + L_XP + qkq * 72 + (lane >> 3) * 9;
#pragma unroll
      for (int j = 0; j < 9; ++j) pair_mac2(wqv[2 * j], wqv[2 * j + 1], xp[j], xp[288 + j], a0, a1);
      const float s0 = rs8_hi(a0, lane), s1 = rs8_hi(a1, lane);
      gu64* qd = (gu64*)(ws + WS_QG) + (qcg * 4 + qkq) * 64 + (lane & 7) * 8 + (lane >> 3);
      st_gran(qd, tag, __float_as_uint(s0)); st_gran(qd + 16384, tag, __float_as_uint(s1));
    }
    if (s == 0 && lane < NCTR) __hip_atomic_store((gu32*)(ws + WS_CTR + g * 1024 + lane * 256), ctag, RLX_AGENT);
    STAMP(10);
    {
      const unsigned char* qsrc = ws + WS_QG + (size_t)(b * 64 + kvh * 4) * 2048;
      __builtin_amdgcn_s_sleep(QSLEEP);
      const __amdgpu_buffer_rsrc_t rs_q = mkrs(qsrc, 8192);
      u32x4 v[8];
      for (u32 spins = 0;; ++spins) {
        asm volatile("" ::: "memory");
#pragma unroll
        for (int k = 0; k < 8; ++k) v[k] = bld<AUX_SC1>(rs_q, hf * 2048 + l5 * 16, (k >> 2) * 4096 + (k & 3) * 512);
        bool ok = (__hip_atomic_load((const gu32*)(ws + WS_CTR + g * 1024 + (lane & (NCTR - 1)) * 256), RLX_AGENT) & 0xfffff000u) == ctag;
#pragma unroll
        for (int k = 0; k < 8; ++k) ok &= v[k][1] == tag && v[k][3] == tag;
        if (__all(ok) || spins > SPIN_CAP) break;
        __builtin_amdgcn_s_sleep(1);
      }
#pragma unroll
      for (int k = 0; k < 2; ++k) {
        const u32x4* u = v + 4 * k;
        const float qa = (__uint_as_float(u[0][0]) + __uint_as_float(u[1][0])) + (__uint_as_float(u[2][0]) + __uint_as_float(u[3][0]));
        const float qb = (__uint_as_float(u[0][2]) + __uint_as_float(u[1][2])) + (__uint_as_float(u[2][2]) + __uint_as_float(u[3][2]));
        lds[L_Q + k * 64 + lane] = f2bf(qa) | (f2bf(qb) << 16);
      }
      lds[L_FLAG] = tag;
      __builtin_amdgcn_fence(__ATOMIC_RELEASE, "workgroup");
      lflag[1] = 1u;
    }
  }
  asm volatile("; ROLE SPLIT 2" ::: "memory");

  const int task = (s & 3) == (g & 3) ? g * 2 + (s >> 2) : ((g >> 2) == s && (g & 3) == ((s + 1) & 3) ? 64 + s : -1);
  const int r = lane >> 4, c = lane & 15;
  u32x4 wov[8];
  float acc0[8], acc1[8];
#pragma unroll
  for (int k = 0; k < 8; ++k) { acc0[k] = 0.f; acc1[k] = 0.f; wov[k] = u32x4{0u, 0u, 0u, 0u}; }
  float mrun = -INFINITY, lrun = 0.f;
  u32* mylds = lds + (w >= 0 ? L_STASH + w * NE * 1024 : L_FO - L_RED);
  if (w >= 0) {
    u32x4 B2[U];
    int c2;
#define WAITV(N) __builtin_amdgcn_s_waitcnt(0x0f70 | ((N) & 15) | (((N) >> 4) << 14)); SB
    if (HEAD_SLEEP) __builtin_amdgcn_s_sleep(HEAD_SLEEP);
    ISSUE(B0, c0, kbase, t0, false); ISSUE(B1, c1, kbase, t0 + NSW, false);
    __syncthreads();
    u32x4* stash = reinterpret_cast<u32x4*>(mylds) + lane;
    int ne = 0;
#pragma unroll 1
    while (ne < NE && lflag[1] == 0u) {
      WAITV(5);
#pragma unroll
      for (int i = 0; i < U; ++i) stash[ne * 256 + i * 64] = B0[i];
      SB; ISSUE(B0, c0, kbase, t0 + (ne + 2) * NSW, false);
      WAITV(5);
#pragma unroll
      for (int i = 0; i < U; ++i) stash[ne * 256 + 256 + i * 64] = B1[i];
      SB; ISSUE(B1, c1, kbase, t0 + (ne + 3) * NSW, false);
      ne += 2;
    }
    if (w == 0) STAMP(1);
    {
      const __amdgpu_buffer_rsrc_t rs_wo = mkrs(wo + (size_t)kvh * 256 * DM, ii < 9 ? 256 * DM * 2 : 0);
      const int vo = (lane >> 3) * 8 * (DM * 2) + (lane & 7) * 16, so = 64 * w * (DM * 2) + ii * 128;
#pragma unroll
      for (int k = 0; k < 8; ++k) wov[k] = bld<AUX_NT>(rs_wo, vo, so + k * (DM * 2));
      SB;
    }
    while (lflag[1] == 0u) __builtin_amdgcn_s_sleep(1);
    __builtin_amdgcn_fence(__ATOMIC_ACQUIRE, "workgroup");
    if (w == 0) STAMP(2);
    const u32x4 q0 = *reinterpret_cast<const u32x4*>(lds + L_Q + c * 4);
    const u32x4 q1 = *reinterpret_cast<const u32x4*>(lds + L_Q + 64 + c * 4);
    u16* scl = reinterpret_cast<u16*>(lds + L_SC + w * 64 * 16) + (lane >> 1);

    float mseen = -INFINITY;
    auto kstep = [&](const u32x4* Kt, int slot) {
      float xv[8];
#pragma unroll
      for (int i = 0; i < U; ++i) {
        float d0 = 0.f, d1 = 0.f;
#pragma unroll
        for (int k = 0; k < 4; ++k) { d0 = dot2(Kt[i][k], q0[k], d0); d1 = dot2(Kt[i][k], q1[k], d1); }
        xv[2 * i] = d0; xv[2 * i + 1] = d1;
      }
#pragma unroll
      for (int k = 0; k < 4; ++k) { const bool up = c & 8; const float keep = up ? xv[k + 4] : xv[k], send = up ? xv[k] : xv[k + 4]; xv[k] = keep + dpp<DPP_ROR8>(send); }
#pragma unroll
      for (int k = 0; k < 2; ++k) { const bool up = c & 4; const float keep = up ? xv[k + 2] : xv[k], send = up ? xv[k] : xv[k + 2]; xv[k] = keep + dpp<DPP_HMIR>(send); }
      { const bool up = c & 2; const float keep = up ? xv[1] : xv[0], send = up ? xv[0] : xv[1]; xv[0] = keep + dpp<DPP_XOR2>(send); }
      const float sc = xv[0] + dpp<DPP_XOR1>(xv[0]);
      const u32 sb = f2bf(rbf(sc) * 0.08838834764831845f);
      scl[slot * 32] = (u16)sb;
      mseen = fmaxf(mseen, bf2f(sb));
    };
    auto pv1 = [&](const u32x4& Vt, float p0, float p1) {
#pragma unroll
      for (int k = 0; k < 4; ++k) {
        const float lo = __uint_as_float(Vt[k] << 16), hi = __uint_as_float(Vt[k] & 0xffff0000u);
        acc0[2 * k] = fmaf(p0, lo, acc0[2 * k]); acc0[2 * k + 1] = fmaf(p0, hi, acc0[2 * k + 1]);
        acc1[2 * k] = fmaf(p1, lo, acc1[2 * k]); acc1[2 * k + 1] = fmaf(p1, hi, acc1[2 * k + 1]);
      }
    };
    auto vstep = [&](const u32x4* Vt, int slot) {
      const float sc = bf2f(scl[slot * 32]);
      if (__any(mseen > mrun)) {
        float mn = fmaxf(mseen, dpp<DPP_ROR4>(mseen)); mn = fmaxf(mn, dpp<DPP_ROR8>(mn)); mn = fmaxf(mn, mrun);
        const float al = __builtin_amdgcn_exp2f((mrun - mn) * LOG2E);
        mrun = mn; lrun *= al;
        const float al0 = dpp<DPP_BC + 0>(al), al1 = dpp<DPP_BC + 2>(al);
#pragma unroll
        for (int k = 0; k < 8; ++k) { acc0[k] *= al0; acc1[k] *= al1; }
      }
      const float p = __builtin_amdgcn_exp2f((sc - mrun) * LOG2E);
      lrun += p;
      pv1(Vt[0], dpp<DPP_BC + 0>(p), dpp<DPP_BC + 2>(p));
      pv1(Vt[1], dpp<DPP_BC + 4>(p), dpp<DPP_BC + 6>(p));
      pv1(Vt[2], dpp<DPP_BC + 8>(p), dpp<DPP_BC + 10>(p));
      pv1(Vt[3], dpp<DPP_BC + 12>(p), dpp<DPP_BC + 14>(p));
      pin8(acc0); pin8(acc1);
    };
    const int n = task >= 0 ? NSF : NS;
    const int g3 = g & 3, nx = (g3 == (((g >> 2) + 1) & 3) ? 3 : 2) * PEX;
    int it = ne, iend = 0x7fffffff, ptile = 0;
#define STEP(BUF, CL, NBUF, NCL) { \
      const int i2_ = it + 2 - 2 * n;                        \
      if (it < iend) { WAITV(5); \
        if (i2_ >= 0 && !(i2_ & 1) && ptile < NT) { const int pi_ = (__builtin_amdgcn_readfirstlane(CL) & 0xfff) * NCTR + sub, pc_ = pi_ - nx; \
          const int e_ = pi_ >> 5, reg_ = e_ == 2 ? (g >> 2) : g3 + 4 * e_; \
          ptile = pc_ < 0 ? reg_ * RT + NSF * NSW + (pi_ & 31) : (pc_ < PCOM ? (pc_ & 7) * RT + NS * NSW + (pc_ >> 3) : NT); \
          if (ptile >= NT) iend = it + 2; } } \
      const bool isv_ = i2_ >= 0 ? (i2_ & 1) : (i2_ >= -n); \
      const int tl_ = i2_ >= 0 ? ptile : t0 + (isv_ ? i2_ + n : i2_ + 2 * n) * NSW; \
      ISSUE(NBUF, NCL, isv_ ? vbase : kbase, tl_, i2_ >= -2 && !(i2_ & 1) && ptile < NT); \
      if (it < iend) { const int i0_ = i2_ - 2; \
        if (i0_ >= 0 ? (i0_ & 1) : (i0_ >= -n)) vstep(BUF, i0_ >= 0 ? 63 : i0_ + n); else kstep(BUF, i0_ >= 0 ? 63 : it); } \
      SB; ++it; }
    do {
      STEP(B0, c0, B2, c2);
      if (ne > 0) {
        --ne;
        u32x4 KE[U];
#pragma unroll
        for (int i = 0; i < U; ++i) KE[i] = stash[ne * 256 + i * 64];
        kstep(KE, ne); SB;
      }
      STEP(B1, c1, B0, c0);
      STEP(B2, c2, B1, c1);
    } while (it < iend);
    if (w == 0) STAMP(5);
  }
  asm volatile("; ROLE SPLIT 3" ::: "memory");

  const u32 tag = __builtin_amdgcn_readfirstlane(lds[L_FLAG]);
  const bool dry = w < 0;
  if (DRYRUN || !dry) {
    const int wt = dry ? 0 : w, tt = wt * 64 + lane, h = wt >> 1;
    {
      float lr = lrun + dpp<DPP_ROR4>(lrun); lr += dpp<DPP_ROR8>(lr);
      float a = mrun, bb = mrun; swap16(a, bb); float mw = fmaxf(a, bb);
      a = mw; bb = mw; swap32(a, bb); mw = fmaxf(a, bb);
      const float f = __builtin_amdgcn_exp2f((mrun - mw) * LOG2E);
      lr *= f;
      a = lr; bb = lr; swap16(a, bb); lr = a + bb;
      a = lr; bb = lr; swap32(a, bb); lr = a + bb;
      const float f0 = dpp<DPP_BC + 0>(f), f1 = dpp<DPP_BC + 2>(f);
#pragma unroll
      for (int k = 0; k < 8; ++k) { acc0[k] *= f0; acc1[k] *= f1; }
#pragma unroll
      for (int k = 0; k < 8; ++k) { swap32(acc0[k], acc1[k]); acc0[k] += acc1[k]; }
#pragma unroll
      for (int k = 0; k < 4; ++k) { swap16(acc0[k], acc0[k + 4]); acc0[k] += acc0[k + 4]; }
      *reinterpret_cast<f32x4*>(mylds + L_RED + (r >> 1) * 128 + c * 8 + (r & 1) * 4) = f32x4{acc0[0], acc0[1], acc0[2], acc0[3]};
      if (lane == 0 || lane == 2) { mylds[L_RED + 256 + lane] = __float_as_uint(mw); mylds[L_RED + 256 + lane + 1] = __float_as_uint(lr); }
    }
    if (!dry) __syncthreads();
    const __amdgpu_buffer_rsrc_t rs_p1 = mkrs(ws + WS_P1 + (size_t)kvh * 16 * P1_STRIDE * 8, dry ? 0 : 16 * P1_STRIDE * 8);
    {
      const float* red = ldsf + L_STASH + L_RED;
      float M = -INFINITY;
#pragma unroll
      for (int k = 0; k < NSW; ++k) M = fmaxf(M, red[k * NE * 1024 + 256 + h * 2]);
      float o = 0.f, L = 0.f;
#pragma unroll
      for (int k = 0; k < NSW; ++k) {
        const float f = __builtin_amdgcn_exp2f((red[k * NE * 1024 + 256 + h * 2] - M) * LOG2E);
        o = fmaf(f, red[k * NE * 1024 + tt], o); L = fmaf(f, red[k * NE * 1024 + 256 + h * 2 + 1], L);
      }
      __builtin_amdgcn_raw_buffer_store_b64(u32x2{__float_as_uint(o), tag}, rs_p1, tt * 8, ii * (P1_STRIDE * 8), AUX_SC1);
      __builtin_amdgcn_raw_buffer_store_b64(u32x2{__float_as_uint((lane & 1) ? L : M), tag}, rs_p1, (tt & 127) < 2 ? (256 + h * 2 + lane) * 8 : 0x7fff0000, ii * (P1_STRIDE * 8), AUX_SC1);
    }
    if (w == 0) STAMP(6);
    if (ii < 9) {
      u32x4 av[8]; u32x2 ml;
      for (u32 spins = 0;; ++spins) {
        asm volatile("" ::: "memory");
#pragma unroll
        for (int k = 0; k < 8; ++k) av[k] = bld<AUX_SC1>(rs_p1, hf * (8 * P1_STRIDE * 8) + wt * 512 + l5 * 16, k * (P1_STRIDE * 8));
        ml = __builtin_amdgcn_raw_buffer_load_b64(rs_p1, ((hf * 8 + (c >> 1)) * P1_STRIDE + 256 + h * 2 + (c & 1)) * 8, 0, AUX_SC1);
        bool ok = ml[1] == tag;
#pragma unroll
        for (int k = 0; k < 8; ++k) ok &= av[k][1] == tag && av[k][3] == tag;
        if (__all(ok) || dry || spins > SPIN_CAP) break;
        __builtin_amdgcn_s_sleep(1);
      }
      if (w == 0) STAMP(7);
      u32* lo = lds + (dry ? L_FO : L_O);
      {
        const float mlv = __uint_as_float(ml[0]);
        const float mv = dpp<0xA0>(mlv);
        float M = fmaxf(mv, dpp<DPP_XOR2>(mv)); M = fmaxf(M, dpp<DPP_ROR4>(M)); M = fmaxf(M, dpp<DPP_ROR8>(M));
        const float f = __builtin_amdgcn_exp2f((mv - M) * LOG2E);
        float L = f * dpp<0xF5>(mlv);
        L += dpp<DPP_XOR2>(L); L += dpp<DPP_ROR4>(L); L += dpp<DPP_ROR8>(L);
        float oa = 0.f, ob = 0.f;
#define HOP1(K) { const float f_ = dpp<DPP_BC + 2 * (K)>(f); oa = fmaf(__uint_as_float(av[K][0]), f_, oa); ob = fmaf(__uint_as_float(av[K][2]), f_, ob); }
        HOP1(0) HOP1(1) HOP1(2) HOP1(3) HOP1(4) HOP1(5) HOP1(6) HOP1(7)
        const float rl = 1.0f / L;
        lo[wt * 64 + lane] = f2bf(oa * rl) | (f2bf(ob * rl) << 16);
      }
      __builtin_amdgcn_fence(__ATOMIC_RELEASE, "wavefront"); __builtin_amdgcn_wave_barrier(); __builtin_amdgcn_fence(__ATOMIC_ACQUIRE, "wavefront");
      float a0[8], a1[8];
#pragma unroll
      for (int k = 0; k < 8; ++k) { a0[k] = 0.f; a1[k] = 0.f; }
      const u32x4 o0 = *reinterpret_cast<const u32x4*>(lo + 64 * wt + 4 * (lane >> 3));
      const u32x4 o1 = *reinterpret_cast<const u32x4*>(lo + 64 * wt + 32 + 4 * (lane >> 3));
#pragma unroll
      for (int k = 0; k < 4; ++k) pair_mac2(wov[2 * k], wov[2 * k + 1], o0[k], o1[k], a0, a1);
      const float s0 = rs8_hi(a0, lane), s1 = rs8_hi(a1, lane);
      const int col = (lane & 7) * 8 + (lane >> 3);
      float* r2 = reinterpret_cast<float*>(lo) + 256;
      r2[wt * 128 + col] = s0; r2[wt * 128 + 64 + col] = s1;
      if (!dry) __syncthreads();
      if (wt == 0) {
        const float2 u0 = *reinterpret_cast<const float2*>(r2 + lane * 2), u1 = *reinterpret_cast<const float2*>(r2 + 128 + lane * 2);
        const float2 u2 = *reinterpret_cast<const float2*>(r2 + 256 + lane * 2), u3 = *reinterpret_cast<const float2*>(r2 + 384 + lane * 2);
        __builtin_amdgcn_raw_buffer_store_b128(u32x4{__float_as_uint((u0.x + u1.x) + (u2.x + u3.x)), tag, __float_as_uint((u0.y + u1.y) + (u2.y + u3.y)), tag},
                                               mkrs(ws + WS_P2, dry ? 0 : 9 * 16 * 1024), lane * 16, (ii * 16 + kvh) * 1024, AUX_SC1);
      }
      if (w == 0) STAMP(8);
    }
  }
  if (!dry) return;

  {
    float a0[8], a1[8];
    STAMP(11);

    if (task >= 0) {
      const int rr = lane >> 3, cc = lane & 7;
      const int vo = rr * 72 * (FF * 2) + cc * 16;
      const u32* xp = lds + L_XP + rr * 36;
      u32x4 A[8], Bf[8];
      float c0[8], c1[8];
      const __amdgpu_buffer_rsrc_t rs_g = mkrs(wg, DM * FF * 2), rs_u = mkrs(wu, DM * FF * 2);
      const __amdgpu_buffer_rsrc_t rs_d = mkrs(wd + (size_t)task * 64 * DM, 64 * DM * 2);
#define FISSUE(BUF, RS, KB) { const int so_ = (KB) < 9 ? task * 128 + (KB) * 8 * (FF * 2) : DM * FF * 2; \
      _Pragma("unroll") for (int j_ = 0; j_ < 8; ++j_) BUF[j_] = bld<AUX_NT>(RS, vo, so_ + j_ * (FF * 2)); } SB
#define MAC4(BUF, X0, X1, C0, C1) { _Pragma("unroll") for (int j_ = 0; j_ < 4; ++j_) { pair_mac2(BUF[2 * j_], BUF[2 * j_ + 1], X0[j_], X1[j_], C0, C1); pin8(C0); pin8(C1); SB; } }
#define FZERO(C0, C1) _Pragma("unroll") for (int k_ = 0; k_ < 8; ++k_) { C0[k_] = 0.f; C1[k_] = 0.f; }
#define DISSUE(BUF, R0) _Pragma("unroll") for (int j_ = 0; j_ < 8; ++j_) BUF[j_] = bld<AUX_NT>(rs_d, lane * 16, ((R0) + j_) * (DM * 2)); SB
      FISSUE(A, rs_g, 0); FISSUE(Bf, rs_u, 0);
      FZERO(a0, a1); FZERO(c0, c1);
#pragma unroll 1
      for (int k = 0; k < 9; ++k) {
        const u32x4 x0 = *reinterpret_cast<const u32x4*>(xp + 4 * k), x1 = *reinterpret_cast<const u32x4*>(xp + 288 + 4 * k);
        MAC4(A, x0, x1, a0, a1); FISSUE(A, rs_g, k + 1);
        MAC4(Bf, x0, x1, c0, c1); FISSUE(Bf, rs_u, k + 1);
      }
      DISSUE(A, 0); DISSUE(Bf, 8);
      {
        const float gs0 = rs8_hi(a0, lane), gs1 = rs8_hi(a1, lane), us0 = rs8_hi(c0, lane), us1 = rs8_hi(c1, lane);
        const int col = cc * 8 + rr;
        const float g0 = rbf(gs0), g1 = rbf(gs1);
        const float e0 = rbf(0.5f * g0 * (1.0f + tanhf(0.7978845608028654f * (g0 + 0.044715f * g0 * g0 * g0))));
        const float e1 = rbf(0.5f * g1 * (1.0f + tanhf(0.7978845608028654f * (g1 + 0.044715f * g1 * g1 * g1))));
        reinterpret_cast<u16*>(lds + L_H)[col] = (u16)f2bf(e0 * rbf(us0));
        reinterpret_cast<u16*>(lds + L_H)[64 + col] = (u16)f2bf(e1 * rbf(us1));
      }
      __builtin_amdgcn_fence(__ATOMIC_RELEASE, "wavefront"); __builtin_amdgcn_wave_barrier(); __builtin_amdgcn_fence(__ATOMIC_ACQUIRE, "wavefront");
      const u32* hp = lds + L_H;
      FZERO(a0, a1);
#pragma unroll 1
      for (int k = 0; k < 4; ++k) {
        const u32x4 h0 = *reinterpret_cast<const u32x4*>(hp + 8 * k), h1 = *reinterpret_cast<const u32x4*>(hp + 32 + 8 * k);
        const u32x4 h2 = *reinterpret_cast<const u32x4*>(hp + 8 * k + 4), h3 = *reinterpret_cast<const u32x4*>(hp + 32 + 8 * k + 4);
        MAC4(A, h0, h1, a0, a1); DISSUE(A, 16 * k + 16);
        MAC4(Bf, h2, h3, a0, a1); DISSUE(Bf, 16 * k + 24);
      }
#pragma unroll
      for (int j = 0; j < 8; ++j) A[j] = bld<AUX_NT>(rs_d, rr * 8 * (DM * 2) + 1024 + cc * 16, j * (DM * 2));
      SB;
      float* fo = ldsf + L_FO;
      *reinterpret_cast<f32x4*>(fo + rr * 128 + cc * 8) = f32x4{a0[0], a0[1], a0[2], a0[3]};
      *reinterpret_cast<f32x4*>(fo + rr * 128 + cc * 8 + 4) = f32x4{a0[4], a0[5], a0[6], a0[7]};
      *reinterpret_cast<f32x4*>(fo + rr * 128 + 64 + cc * 8) = f32x4{a1[0], a1[1], a1[2], a1[3]};
      *reinterpret_cast<f32x4*>(fo + rr * 128 + 64 + cc * 8 + 4) = f32x4{a1[4], a1[5], a1[6], a1[7]};
      FZERO(a0, a1);
      {
        const u32x4 h0 = *reinterpret_cast<const u32x4*>(hp + 4 * rr), h1 = *reinterpret_cast<const u32x4*>(hp + 32 + 4 * rr);
        MAC4(A, h0, h1, a0, a1);
      }
      const float e0 = rs8_hi(a0, lane), e1 = rs8_hi(a1, lane);
      fo[1024 + cc * 8 + rr] = e0; fo[1024 + 64 + cc * 8 + rr] = e1;
      __builtin_amdgcn_fence(__ATOMIC_RELEASE, "wavefront"); __builtin_amdgcn_wave_barrier(); __builtin_amdgcn_fence(__ATOMIC_ACQUIRE, "wavefront");
      const __amdgpu_buffer_rsrc_t rs_pf = mkrs(ws + WS_PF, 9 * NFT * 1024);
#pragma unroll
      for (int k = 0; k < 9; ++k) {
        const float2 v = *reinterpret_cast<const float2*>(fo + k * 128 + lane * 2);
        __builtin_amdgcn_raw_buffer_store_b128(u32x4{__float_as_uint(v.x), tag, __float_as_uint(v.y), tag}, rs_pf, lane * 16, (k * NFT + task) * 1024, AUX_SC1);
      }
      STAMP(12);
    }

    const bool is_red = (ii == 15 && kvh < 9);
    float f0 = 0.f, f1 = 0.f; u32 xpair = 0;
    if (is_red) {
      xpair = reinterpret_cast<const u32*>(x)[hf * 288 + kvh * 32 + l5];
      const __amdgpu_buffer_rsrc_t rs_f = mkrs(ws + WS_PF + (size_t)kvh * NFT * 1024, NFT * 1024);
      for (int k0 = 0; k0 < NFT; k0 += 12) {
        float p0, p1;
        for (u32 spins = 0;; ++spins) {
          asm volatile("" ::: "memory");
          bool ok = true; p0 = 0.f; p1 = 0.f;
          u32x4 v[12];
#pragma unroll
          for (int k = 0; k < 12; ++k) v[k] = bld<AUX_SC1>(rs_f, lane * 16, (k0 + k) * 1024);
#pragma unroll
          for (int k = 0; k < 12; ++k) { p0 += __uint_as_float(v[k][0]); p1 += __uint_as_float(v[k][2]); ok &= v[k][1] == tag && v[k][3] == tag; }
          if (__all(ok) || spins > SPIN_CAP) break;
          __builtin_amdgcn_s_sleep(64);
        }
        f0 += p0; f1 += p1;
      }
      STAMP(13);
    }
#pragma unroll 1
    for (int pass = DRYRUN && is_red ? 0 : 1; pass < 2; ++pass) {
      if (pass) __syncthreads();
      if (!is_red) break;
      const __amdgpu_buffer_rsrc_t rs_y = mkrs(ws + WS_P2 + (size_t)kvh * 16 * 1024, pass ? 16 * 1024 : 0);
      float y0, y1;
      for (u32 spins = 0;; ++spins) {
        asm volatile("" ::: "memory");
        bool ok = true; y0 = 0.f; y1 = 0.f;
        u32x4 v[16];
#pragma unroll
        for (int k = 0; k < 16; ++k) v[k] = bld<AUX_SC1>(rs_y, lane * 16, k * 1024);
#pragma unroll
        for (int k = 0; k < 16; ++k) { y0 += __uint_as_float(v[k][0]); y1 += __uint_as_float(v[k][2]); ok &= v[k][1] == tag && v[k][3] == tag; }
        if (__all(ok) || !pass || spins > SPIN_CAP) break;
        __builtin_amdgcn_s_sleep(1);
      }
      const u32 o0 = f2bf(rbf(bf2f(xpair & 0xffffu) + rbf(y0)) + rbf(f0)), o1 = f2bf(rbf(bf2f(xpair >> 16) + rbf(y1)) + rbf(f1));
      __builtin_amdgcn_raw_buffer_store_b32(o0 | (o1 << 16), mkrs(out, pass ? 2 * DM * 2 : 0), (hf * 288 + l5) * 4, kvh * 128, 0);
      __builtin_amdgcn_raw_buffer_store_b32(tag, mkrs(ws + WS_EPOCH, pass && kvh == 0 ? 4 : 0), lane * 4, 0, AUX_SC1);
      if (pass) STAMP(14);
    }
  }
}

extern "C" void kernel_launch(void* const* d_in, const int* in_sizes, int n_in, void* d_out, int out_size, void* d_ws, size_t ws_size,
                              hipStream_t stream) {
  (void)in_sizes; (void)n_in; (void)out_size; (void)ws_size;
  hipLaunchKernelGGL(fused_decode_kernel, dim3(256), dim3(NTHR), 0, stream, (const u16*)d_in[0], (const u16*)d_in[1], (const u16*)d_in[2],
                     (const u16*)d_in[3], (const u16*)d_in[4], (const u16*)d_in[6], (const u16*)d_in[7], (const u16*)d_in[8], (u16*)d_out,
                     (unsigned char*)d_ws);
}
```

```cpp
#define FIXT 12000
#define OUT_AUX 0
#define XALL 1
#define XFLUSH 1
#define NCTR 4
#define NSTAT 57
#include <hip/hip_runtime.h>
#include <hip/hip_bf16.h>
#include <stdint.h>

typedef unsigned u32;
typedef unsigned short u16;
typedef unsigned long long u64;
typedef u32 u32x4 __attribute__((ext_vector_type(4)));
typedef u32 u32x2 __attribute__((ext_vector_type(2)));
typedef float f32x4 __attribute__((ext_vector_type(4)));
typedef __bf16 bf16x2 __attribute__((ext_vector_type(2)));
#define GAS __attribute__((address_space(1)))
#define LAS __attribute__((address_space(3)))
typedef GAS u64 gu64;
typedef GAS u32 gu32;
#define RLX_AGENT __ATOMIC_RELAXED, __HIP_MEMORY_SCOPE_AGENT
#define SB __builtin_amdgcn_sched_barrier(0)

constexpr int DM = 576, SK = 32768, DH = 128, FF = 4608, NQ = 4096;
constexpr int NSW = 4, NTHR = (NSW + 1) * 64, U = 4;
constexpr int TB = U * 1024;
constexpr int NT = SK / 16;
constexpr int RT = NT / 8;
#define GMIX 0
#define SVC_HEAD 1
#define HEAD_SLEEP 0
#define PREFILTER 0
#define QSLEEP 20
#define DRYRUN 0
#define NE 8
#define WQ_AUX 2
constexpr int NS = NSTAT;
constexpr int NSF = NS - 8;
constexpr int PEX = (NS - NSF) * NSW;
constexpr int PCOM = (RT - NS * NSW) * 8;
static_assert(PEX == 32 && NE % 2 == 0 && NSF >= 4 * NE + 6 && PCOM + 3 * PEX + 128 < 4096, "");
constexpr int NFT = 72;
constexpr int AUX_NT = 2, AUX_SC1 = 16;
constexpr float LOG2E = 1.4426950408889634f;
constexpr u32 SPIN_CAP = 1u << 16;

constexpr size_t WS_EPOCH = 0;
constexpr size_t WS_CTR = 4096;
constexpr size_t WS_QG = WS_CTR + 128 * 256;
constexpr size_t WS_P1 = WS_QG + 2 * 64 * 4 * 64 * 8;
constexpr int P1_STRIDE = 264;
constexpr size_t WS_P2 = WS_P1 + (size_t)256 * P1_STRIDE * 8;
constexpr size_t WS_PF = WS_P2 + (size_t)9 * 16 * 128 * 8;
constexpr size_t WS_END = WS_PF + (size_t)9 * NFT * 128 * 8;

__device__ __forceinline__ float bf2f(u32 lo16) { return __uint_as_float(lo16 << 16); }
__device__ __forceinline__ float rbf(float f) { return (float)(__bf16)f; }
__device__ __forceinline__ u32 f2bf(float f) { const __bf16 h = (__bf16)f; return (u32)__builtin_bit_cast(u16, h); }
__device__ __forceinline__ float dot2(u32 a, u32 b, float c) {
  return __builtin_amdgcn_fdot2_f32_bf16(__builtin_bit_cast(bf16x2, a), __builtin_bit_cast(bf16x2, b), c, false);
}
template <int CTRL> __device__ __forceinline__ float dpp(float x) {
  return __int_as_float(__builtin_amdgcn_update_dpp(0, __float_as_int(x), CTRL, 0xf, 0xf, true));
}
constexpr int DPP_XOR1 = 0xB1, DPP_XOR2 = 0x4E, DPP_HMIR = 0x141, DPP_ROR8 = 0x128, DPP_ROR4 = 0x124, DPP_BC = 0x150;
__device__ __forceinline__ void swap32(float& a, float& b) {
  auto r = __builtin_amdgcn_permlane32_swap(__float_as_uint(a), __float_as_uint(b), false, false);
  a = __uint_as_float(r[0]); b = __uint_as_float(r[1]);
}
__device__ __forceinline__ void swap16(float& a, float& b) {
  auto r = __builtin_amdgcn_permlane16_swap(__float_as_uint(a), __float_as_uint(b), false, false);
  a = __uint_as_float(r[0]); b = __uint_as_float(r[1]);
}
__device__ __forceinline__ float half_sum(float x) {
  x += dpp<DPP_XOR1>(x); x += dpp<DPP_XOR2>(x); x += dpp<DPP_ROR4>(x); x += dpp<DPP_ROR8>(x);
  float a = x, b = x; swap16(a, b); return a + b;
}
__device__ __forceinline__ __amdgpu_buffer_rsrc_t mkrs(const void* p, u32 bytes) {
  return __builtin_amdgcn_make_buffer_rsrc(const_cast<void*>(p), 0, bytes, 0x00020000);
}
template <int AUX> __device__ __forceinline__ u32x4 bld(__amdgpu_buffer_rsrc_t rs, int voff, int soff) {
  return __builtin_amdgcn_raw_buffer_load_b128(rs, voff, soff, AUX);
}
__device__ __forceinline__ void pair_mac2(const u32x4& a, const u32x4& b, u32 xp0, u32 xp1, float* acc0, float* acc1) {
#pragma unroll
  for (int d = 0; d < 4; ++d) {
    const u32 lo = __builtin_amdgcn_perm(b[d], a[d], 0x05040100u), hi = __builtin_amdgcn_perm(b[d], a[d], 0x07060302u);
    acc0[2 * d] = dot2(lo, xp0, acc0[2 * d]); acc0[2 * d + 1] = dot2(hi, xp0, acc0[2 * d + 1]);
    acc1[2 * d] = dot2(lo, xp1, acc1[2 * d]); acc1[2 * d + 1] = dot2(hi, xp1, acc1[2 * d + 1]);
  }
}
__device__ __forceinline__ float rs8_hi(float* v, int lane) {
#pragma unroll
  for (int k = 0; k < 4; ++k) { swap32(v[k], v[k + 4]); v[k] += v[k + 4]; }
#pragma unroll
  for (int k = 0; k < 2; ++k) { swap16(v[k], v[k + 2]); v[k] += v[k + 2]; }
  const bool up = lane & 8;
  const float keep = up ? v[1] : v[0], send = up ? v[0] : v[1];
  return keep + dpp<DPP_ROR8>(send);
}
__device__ __forceinline__ void pin8(float* a) {
  asm volatile("" : "+v"(a[0]), "+v"(a[1]), "+v"(a[2]), "+v"(a[3]), "+v"(a[4]), "+v"(a[5]), "+v"(a[6]), "+v"(a[7]));
}
__device__ __forceinline__ void st_gran(gu64* g, u32 tag, u32 v) { __hip_atomic_store(g, ((u64)tag << 32) | (u64)v, RLX_AGENT); }

#define STAMP(k) do {} while (0)

constexpr int L_XP = 0;
constexpr int L_Q = L_XP + 576;
constexpr int L_FLAG = L_Q + 128;
constexpr int L_H = L_FLAG + 4;
constexpr int L_FO = L_H + 64;
constexpr int L_SC = L_FO + 1152;
constexpr int L_STASH = L_SC + NSW * 64 * 16;
constexpr int L_O = L_STASH;
constexpr int L_RED = 1024;
constexpr int L_TOTAL = L_STASH + NSW * NE * 1024;

__global__ void __launch_bounds__(NTHR, 2)
fused_decode_kernel(const u16* __restrict__ x, const u16* __restrict__ kc, const u16* __restrict__ vc, const u16* __restrict__ wq,
                    const u16* __restrict__ wo, const u16* __restrict__ wg, const u16* __restrict__ wu, const u16* __restrict__ wd,
                    u16* __restrict__ out, unsigned char* __restrict__ ws) {
  __shared__ __attribute__((aligned(16))) u32 lds[L_TOTAL];
  u64 tstart_; asm volatile("s_memrealtime %0\n\ts_waitcnt lgkmcnt(0)" : "=s"(tstart_) :: "memory"); __builtin_amdgcn_sched_barrier(0);
  asm volatile("" :: "s"(x), "s"(kc), "s"(vc), "s"(wq), "s"(ws));
  float* ldsf = reinterpret_cast<float*>(lds);
  const int tid = (int)threadIdx.x - 64, lane = threadIdx.x & 63;
  const int w = __builtin_amdgcn_readfirstlane((int)(threadIdx.x >> 6)) - 1;
  const int bid = blockIdx.x;
  const int s = bid & 7, g = ((bid >> 3) + GMIX * s) & 31, b = g >> 4, kvh = g & 15;
  const int ii = b * 8 + s;

  const u16* kbase = kc + (size_t)g * SK * DH;
  const u16* vbase = vc + (size_t)g * SK * DH;
  const int kv_vo = lane * 16, cvo = lane * 4;
  const int t0 = s * RT + w;
  const int hf = lane >> 5, l5 = lane & 31;
  const int sub = (w + s) & (NCTR - 1);
  const int qcg = g + 32 * (s >> 2), qkq = s & 3;
  u32x4 B0[U], B1[U];
  int c0, c1;
#define ISSUE(BUF, CL, BASE, T, REAL) { CL = __builtin_amdgcn_raw_ptr_buffer_atomic_add_i32(1, mkrs(ws + WS_CTR + g * 1024 + sub * 256, (REAL) ? 4 : 0), cvo, 0, 0); SB; \
    const __amdgpu_buffer_rsrc_t rs_ = mkrs(BASE, SK * DH * 2); const int so_ = (T) * TB; \
    _Pragma("unroll") for (int i_ = 0; i_ < U; ++i_) BUF[i_] = bld<AUX_NT>(rs_, kv_vo + i_ * 1024, so_); SB; }
  if (w == 0) STAMP(0);
  volatile LAS u32* lflag = (volatile LAS u32*)(lds + L_FLAG);
  if (threadIdx.x == 0) lflag[1] = 0u;
  asm volatile("; ROLE SPLIT 1" ::: "memory");
  if (w < 0) {
    __builtin_amdgcn_s_setprio(3);
    u32x4 wqv[18];
    u32 xr[9];
    {
      const __amdgpu_buffer_rsrc_t rs_x = mkrs(x, 2 * DM * 2);
      const u32x4 xa = bld<0>(rs_x, lane * 36, 0), xb = bld<0>(rs_x, lane * 36 + 16, 0);
      xr[8] = __builtin_amdgcn_raw_buffer_load_b32(rs_x, lane * 36 + 32, 0, 0);
#pragma unroll
      for (int j = 0; j < 4; ++j) { xr[j] = xa[j]; xr[4 + j] = xb[j]; }
    }
    const u32 epoch_v = __hip_atomic_load((gu32*)(ws + WS_EPOCH), RLX_AGENT);
    const __amdgpu_buffer_rsrc_t rs_wq = mkrs(wq, DM * NQ * 2);
    const int vo = (lane >> 3) * 18 * (NQ * 2) + (lane & 7) * 16;
#pragma unroll
    for (int j = 0; j < 18; ++j) wqv[j] = bld<AUX_NT>(rs_wq, vo, qcg * 128 + (qkq * 144 + j) * (NQ * 2));
    SB;
    __syncthreads();
    {
      float v[18]; float sum = 0.f;
#pragma unroll
      for (int j = 0; j < 9; ++j) { v[2 * j] = bf2f(xr[j] & 0xffffu); v[2 * j + 1] = bf2f(xr[j] >> 16); sum += v[2 * j] + v[2 * j + 1]; }
      const float mean = half_sum(sum) * (1.0f / DM);
      float sq = 0.f;
#pragma unroll
      for (int j = 0; j < 18; ++j) { const float d = v[j] - mean; sq = fmaf(d, d, sq); }
      const float var = half_sum(sq) * (1.0f / DM);
      const float mb = rbf(mean), rsd = 1.0f / rbf(sqrtf(rbf(rbf(var) + 1e-5f)));
#pragma unroll
      for (int j = 0; j < 9; ++j)
        lds[L_XP + lane * 9 + j] = f2bf(rbf(v[2 * j] - mb) * rsd) | (f2bf(rbf(v[2 * j + 1] - mb) * rsd) << 16);
    }
    __builtin_amdgcn_fence(__ATOMIC_RELEASE, "wavefront"); __builtin_amdgcn_wave_barrier(); __builtin_amdgcn_fence(__ATOMIC_ACQUIRE, "wavefront");
    const u32 tag = __builtin_amdgcn_readfirstlane(epoch_v) + 0x9E3779B9u;
    const u32 ctag = tag & 0xfffff000u;
    STAMP(9);
    float a0[8], a1[8];
#pragma unroll
    for (int k = 0; k < 8; ++k) { a0[k] = 0.f; a1[k] = 0.f; }
    {
      const u32* xp = lds + L_XP + qkq * 72 + (lane >> 3) * 9;
#pragma unroll
      for (int j = 0; j < 9; ++j) pair_mac2(wqv[2 * j], wqv[2 * j + 1], xp[j], xp[288 + j], a0, a1);
      const float s0 = rs8_hi(a0, lane), s1 = rs8_hi(a1, lane);
      gu64* qd = (gu64*)(ws + WS_QG) + (qcg * 4 + qkq) * 64 + (lane & 7) * 8 + (lane >> 3);
      st_gran(qd, tag, __float_as_uint(s0)); st_gran(qd + 16384, tag, __float_as_uint(s1));
    }
    if (s == 0 && lane < NCTR) __hip_atomic_store((gu32*)(ws + WS_CTR + g * 1024 + lane * 256), ctag, RLX_AGENT);
    STAMP(10);
    {
      const unsigned char* qsrc = ws + WS_QG + (size_t)(b * 64 + kvh * 4) * 2048;
      __builtin_amdgcn_s_sleep(QSLEEP);
      const __amdgpu_buffer_rsrc_t rs_q = mkrs(qsrc, 8192);
      u32x4 v[8];
      for (u32 spins = 0;; ++spins) {
        asm volatile("" ::: "memory");
#pragma unroll
        for (int k = 0; k < 8; ++k) v[k] = bld<AUX_SC1>(rs_q, hf * 2048 + l5 * 16, (k >> 2) * 4096 + (k & 3) * 512);
        bool ok = (__hip_atomic_load((const gu32*)(ws + WS_CTR + g * 1024 + (lane & (NCTR - 1)) * 256), RLX_AGENT) & 0xfffff000u) == ctag;
#pragma unroll
        for (int k = 0; k < 8; ++k) ok &= v[k][1] == tag && v[k][3] == tag;
        if (__all(ok) || spins > SPIN_CAP) break;
        __builtin_amdgcn_s_sleep(1);
      }
#pragma unroll
      for (int k = 0; k < 2; ++k) {
        const u32x4* u = v + 4 * k;
        const float qa = (__uint_as_float(u[0][0]) + __uint_as_float(u[1][0])) + (__uint_as_float(u[2][0]) + __uint_as_float(u[3][0]));
        const float qb = (__uint_as_float(u[0][2]) + __uint_as_float(u[1][2])) + (__uint_as_float(u[2][2]) + __uint_as_float(u[3][2]));
        lds[L_Q + k * 64 + lane] = f2bf(qa) | (f2bf(qb) << 16);
      }
      lds[L_FLAG] = tag;
      __builtin_amdgcn_fence(__ATOMIC_RELEASE, "workgroup");
      lflag[1] = 1u;
    }
  }
  asm volatile("; ROLE SPLIT 2" ::: "memory");

  const int task = (s & 3) == (g & 3) ? g * 2 + (s >> 2) : ((g >> 2) == s && (g & 3) == ((s + 1) & 3) ? 64 + s : -1);
  const int r = lane >> 4, c = lane & 15;
  u32x4 wov[8];
  float acc0[8], acc1[8];
#pragma unroll
  for (int k = 0; k < 8; ++k) { acc0[k] = 0.f; acc1[k] = 0.f; wov[k] = u32x4{0u, 0u, 0u, 0u}; }
  float mrun = -INFINITY, lrun = 0.f;
  u32* mylds = lds + (w >= 0 ? L_STASH + w * NE * 1024 : L_FO - L_RED);
  if (w >= 0) {
    u32x4 B2[U];
    int c2;
#define WAITV(N) __builtin_amdgcn_s_waitcnt(0x0f70 | ((N) & 15) | (((N) >> 4) << 14)); SB
    if (HEAD_SLEEP) __builtin_amdgcn_s_sleep(HEAD_SLEEP);
    ISSUE(B0, c0, kbase, t0, false); ISSUE(B1, c1, kbase, t0 + NSW, false);
    __syncthreads();
    u32x4* stash = reinterpret_cast<u32x4*>(mylds) + lane;
    int ne = 0;
#pragma unroll 1
    while (ne < NE && lflag[1] == 0u) {
      WAITV(5);
#pragma unroll
      for (int i = 0; i < U; ++i) stash[ne * 256 + i * 64] = B0[i];
      SB; ISSUE(B0, c0, kbase, t0 + (ne + 2) * NSW, false);
      WAITV(5);
#pragma unroll
      for (int i = 0; i < U; ++i) stash[ne * 256 + 256 + i * 64] = B1[i];
      SB; ISSUE(B1, c1, kbase, t0 + (ne + 3) * NSW, false);
      ne += 2;
    }
    if (w == 0) STAMP(1);
    {
      const __amdgpu_buffer_rsrc_t rs_wo = mkrs(wo + (size_t)kvh * 256 * DM, ii < 9 ? 256 * DM * 2 : 0);
      const int vo = (lane >> 3) * 8 * (DM * 2) + (lane & 7) * 16, so = 64 * w * (DM * 2) + ii * 128;
#pragma unroll
      for (int k = 0; k < 8; ++k) wov[k] = bld<AUX_NT>(rs_wo, vo, so + k * (DM * 2));
      SB;
    }
    while (lflag[1] == 0u) __builtin_amdgcn_s_sleep(1);
    __builtin_amdgcn_fence(__ATOMIC_ACQUIRE, "workgroup");
    if (w == 0) STAMP(2);
    const u32x4 q0 = *reinterpret_cast<const u32x4*>(lds + L_Q + c * 4);
    const u32x4 q1 = *reinterpret_cast<const u32x4*>(lds + L_Q + 64 + c * 4);
    u16* scl = reinterpret_cast<u16*>(lds + L_SC + w * 64 * 16) + (lane >> 1);

    float mseen = -INFINITY;
    auto kstep = [&](const u32x4* Kt, int slot) {
      float xv[8];
#pragma unroll
      for (int i = 0; i < U; ++i) {
        float d0 = 0.f, d1 = 0.f;
#pragma unroll
        for (int k = 0; k < 4; ++k) { d0 = dot2(Kt[i][k], q0[k], d0); d1 = dot2(Kt[i][k], q1[k], d1); }
        xv[2 * i] = d0; xv[2 * i + 1] = d1;
      }
#pragma unroll
      for (int k = 0; k < 4; ++k) { const bool up = c & 8; const float keep = up ? xv[k + 4] : xv[k], send = up ? xv[k] : xv[k + 4]; xv[k] = keep + dpp<DPP_ROR8>(send); }
#pragma unroll
      for (int k = 0; k < 2; ++k) { const bool up = c & 4; const float keep = up ? xv[k + 2] : xv[k], send = up ? xv[k] : xv[k + 2]; xv[k] = keep + dpp<DPP_HMIR>(send); }
      { const bool up = c & 2; const float keep = up ? xv[1] : xv[0], send = up ? xv[0] : xv[1]; xv[0] = keep + dpp<DPP_XOR2>(send); }
      const float sc = xv[0] + dpp<DPP_XOR1>(xv[0]);
      const u32 sb = f2bf(rbf(sc) * 0.08838834764831845f);
      scl[slot * 32] = (u16)sb;
      mseen = fmaxf(mseen, bf2f(sb));
    };
    auto pv1 = [&](const u32x4& Vt, float p0, float p1) {
#pragma unroll
      for (int k = 0; k < 4; ++k) {
        const float lo = __uint_as_float(Vt[k] << 16), hi = __uint_as_float(Vt[k] & 0xffff0000u);
        acc0[2 * k] = fmaf(p0, lo, acc0[2 * k]); acc0[2 * k + 1] = fmaf(p0, hi, acc0[2 * k + 1]);
        acc1[2 * k] = fmaf(p1, lo, acc1[2 * k]); acc1[2 * k + 1] = fmaf(p1, hi, acc1[2 * k + 1]);
      }
    };
    auto vstep = [&](const u32x4* Vt, int slot) {
      const float sc = bf2f(scl[slot * 32]);
      if (__any(mseen > mrun)) {
        float mn = fmaxf(mseen, dpp<DPP_ROR4>(mseen)); mn = fmaxf(mn, dpp<DPP_ROR8>(mn)); mn = fmaxf(mn, mrun);
        const float al = __builtin_amdgcn_exp2f((mrun - mn) * LOG2E);
        mrun = mn; lrun *= al;
        const float al0 = dpp<DPP_BC + 0>(al), al1 = dpp<DPP_BC + 2>(al);
#pragma unroll
        for (int k = 0; k < 8; ++k) { acc0[k] *= al0; acc1[k] *= al1; }
      }
      const float p = __builtin_amdgcn_exp2f((sc - mrun) * LOG2E);
      lrun += p;
      pv1(Vt[0], dpp<DPP_BC + 0>(p), dpp<DPP_BC + 2>(p));
      pv1(Vt[1], dpp<DPP_BC + 4>(p), dpp<DPP_BC + 6>(p));
      pv1(Vt[2], dpp<DPP_BC + 8>(p), dpp<DPP_BC + 10>(p));
      pv1(Vt[3], dpp<DPP_BC + 12>(p), dpp<DPP_BC + 14>(p));
      pin8(acc0); pin8(acc1);
    };
    const int n = task >= 0 ? NSF : NS;
    const int g3 = g & 3, nx = (g3 == (((g >> 2) + 1) & 3) ? 3 : 2) * PEX;
    int it = ne, iend = 0x7fffffff, ptile = 0;
#define STEP(BUF, CL, NBUF, NCL) { \
      const int i2_ = it + 2 - 2 * n;                        \
      if (it < iend) { WAITV(5); \
        if (i2_ >= 0 && !(i2_ & 1) && ptile < NT) { const int pi_ = (__builtin_amdgcn_readfirstlane(CL) & 0xfff) * NCTR + sub, pc_ = pi_ - nx; \
          const int e_ = pi_ >> 5, reg_ = e_ == 2 ? (g >> 2) : g3 + 4 * e_; \
          ptile = pc_ < 0 ? reg_ * RT + NSF * NSW + (pi_ & 31) : (pc_ < PCOM ? (pc_ & 7) * RT + NS * NSW + (pc_ >> 3) : NT); \
          if (ptile >= NT) iend = it + 2; } } \
      const bool isv_ = i2_ >= 0 ? (i2_ & 1) : (i2_ >= -n); \
      const int tl_ = i2_ >= 0 ? ptile : t0 + (isv_ ? i2_ + n : i2_ + 2 * n) * NSW; \
      ISSUE(NBUF, NCL, isv_ ? vbase : kbase, tl_, i2_ >= -2 && !(i2_ & 1) && ptile < NT); \
      if (it < iend) { const int i0_ = i2_ - 2; \
        if (i0_ >= 0 ? (i0_ & 1) : (i0_ >= -n)) vstep(BUF, i0_ >= 0 ? 63 : i0_ + n); else kstep(BUF, i0_ >= 0 ? 63 : it); } \
      SB; ++it; }
    do {
      STEP(B0, c0, B2, c2);
      if (ne > 0) {
        --ne;
        u32x4 KE[U];
#pragma unroll
        for (int i = 0; i < U; ++i) KE[i] = stash[ne * 256 + i * 64];
        kstep(KE, ne); SB;
      }
      STEP(B1, c1, B0, c0);
      STEP(B2, c2, B1, c1);
    } while (it < iend);
    if (w == 0) STAMP(5);
  }
  asm volatile("; ROLE SPLIT 3" ::: "memory");

  const u32 tag = __builtin_amdgcn_readfirstlane(lds[L_FLAG]);
  const bool dry = w < 0;
  if (DRYRUN || !dry) {
    const int wt = dry ? 0 : w, tt = wt * 64 + lane, h = wt >> 1;
    {
      float lr = lrun + dpp<DPP_ROR4>(lrun); lr += dpp<DPP_ROR8>(lr);
      float a = mrun, bb = mrun; swap16(a, bb); float mw = fmaxf(a, bb);
      a = mw; bb = mw; swap32(a, bb); mw = fmaxf(a, bb);
      const float f = __builtin_amdgcn_exp2f((mrun - mw) * LOG2E);
      lr *= f;
      a = lr; bb = lr; swap16(a, bb); lr = a + bb;
      a = lr; bb = lr; swap32(a, bb); lr = a + bb;
      const float f0 = dpp<DPP_BC + 0>(f), f1 = dpp<DPP_BC + 2>(f);
#pragma unroll
      for (int k = 0; k < 8; ++k) { acc0[k] *= f0; acc1[k] *= f1; }
#pragma unroll
      for (int k = 0; k < 8; ++k) { swap32(acc0[k], acc1[k]); acc0[k] += acc1[k]; }
#pragma unroll
      for (int k = 0; k < 4; ++k) { swap16(acc0[k], acc0[k + 4]); acc0[k] += acc0[k + 4]; }
      *reinterpret_cast<f32x4*>(mylds + L_RED + (r >> 1) * 128 + c * 8 + (r & 1) * 4) = f32x4{acc0[0], acc0[1], acc0[2], acc0[3]};
      if (lane == 0 || lane == 2) { mylds[L_RED + 256 + lane] = __float_as_uint(mw); mylds[L_RED + 256 + lane + 1] = __float_as_uint(lr); }
    }
    if (!dry) __syncthreads();
    const __amdgpu_buffer_rsrc_t rs_p1 = mkrs(ws + WS_P1 + (size_t)kvh * 16 * P1_STRIDE * 8, dry ? 0 : 16 * P1_STRIDE * 8);
    {
      const float* red = ldsf + L_STASH + L_RED;
      float M = -INFINITY;
#pragma unroll
      for (int k = 0; k < NSW; ++k) M = fmaxf(M, red[k * NE * 1024 + 256 + h * 2]);
      float o = 0.f, L = 0.f;
#pragma unroll
      for (int k = 0; k < NSW; ++k) {
        const float f = __builtin_amdgcn_exp2f((red[k * NE * 1024 + 256 + h * 2] - M) * LOG2E);
        o = fmaf(f, red[k * NE * 1024 + tt], o); L = fmaf(f, red[k * NE * 1024 + 256 + h * 2 + 1], L);
      }
      __builtin_amdgcn_raw_buffer_store_b64(u32x2{__float_as_uint(o), tag}, rs_p1, tt * 8, ii * (P1_STRIDE * 8), AUX_SC1);
      __builtin_amdgcn_raw_buffer_store_b64(u32x2{__float_as_uint((lane & 1) ? L : M), tag}, rs_p1, (tt & 127) < 2 ? (256 + h * 2 + lane) * 8 : 0x7fff0000, ii * (P1_STRIDE * 8), AUX_SC1);
    }
    if (w == 0) STAMP(6);
    if (ii < 9) {
      u32x4 av[8]; u32x2 ml;
      for (u32 spins = 0;; ++spins) {
        asm volatile("" ::: "memory");
#pragma unroll
        for (int k = 0; k < 8; ++k) av[k] = bld<AUX_SC1>(rs_p1, hf * (8 * P1_STRIDE * 8) + wt * 512 + l5 * 16, k * (P1_STRIDE * 8));
        ml = __builtin_amdgcn_raw_buffer_load_b64(rs_p1, ((hf * 8 + (c >> 1)) * P1_STRIDE + 256 + h * 2 + (c & 1)) * 8, 0, AUX_SC1);
        bool ok = ml[1] == tag;
#pragma unroll
        for (int k = 0; k < 8; ++k) ok &= av[k][1] == tag && av[k][3] == tag;
        if (__all(ok) || dry || spins > SPIN_CAP) break;
        __builtin_amdgcn_s_sleep(1);
      }
      if (w == 0) STAMP(7);
      u32* lo = lds + (dry ? L_FO : L_O);
      {
        const float mlv = __uint_as_float(ml[0]);
        const float mv = dpp<0xA0>(mlv);
        float M = fmaxf(mv, dpp<DPP_XOR2>(mv)); M = fmaxf(M, dpp<DPP_ROR4>(M)); M = fmaxf(M, dpp<DPP_ROR8>(M));
        const float f = __builtin_amdgcn_exp2f((mv - M) * LOG2E);
        float L = f * dpp<0xF5>(mlv);
        L += dpp<DPP_XOR2>(L); L += dpp<DPP_ROR4>(L); L += dpp<DPP_ROR8>(L);
        float oa = 0.f, ob = 0.f;
#define HOP1(K) { const float f_ = dpp<DPP_BC + 2 * (K)>(f); oa = fmaf(__uint_as_float(av[K][0]), f_, oa); ob = fmaf(__uint_as_float(av[K][2]), f_, ob); }
        HOP1(0) HOP1(1) HOP1(2) HOP1(3) HOP1(4) HOP1(5) HOP1(6) HOP1(7)
        const float rl = 1.0f / L;
        lo[wt * 64 + lane] = f2bf(oa * rl) | (f2bf(ob * rl) << 16);
      }
      __builtin_amdgcn_fence(__ATOMIC_RELEASE, "wavefront"); __builtin_amdgcn_wave_barrier(); __builtin_amdgcn_fence(__ATOMIC_ACQUIRE, "wavefront");
      float a0[8], a1[8];
#pragma unroll
      for (int k = 0; k < 8; ++k) { a0[k] = 0.f; a1[k] = 0.f; }
      const u32x4 o0 = *reinterpret_cast<const u32x4*>(lo + 64 * wt + 4 * (lane >> 3));
      const u32x4 o1 = *reinterpret_cast<const u32x4*>(lo + 64 * wt + 32 + 4 * (lane >> 3));
#pragma unroll
      for (int k = 0; k < 4; ++k) pair_mac2(wov[2 * k], wov[2 * k + 1], o0[k], o1[k], a0, a1);
      const float s0 = rs8_hi(a0, lane), s1 = rs8_hi(a1, lane);
      const int col = (lane & 7) * 8 + (lane >> 3);
      float* r2 = reinterpret_cast<float*>(lo) + 256;
      r2[wt * 128 + col] = s0; r2[wt * 128 + 64 + col] = s1;
      if (!dry) __syncthreads();
      if (wt == 0) {
        const float2 u0 = *reinterpret_cast<const float2*>(r2 + lane * 2), u1 = *reinterpret_cast<const float2*>(r2 + 128 + lane * 2);
        const float2 u2 = *reinterpret_cast<const float2*>(r2 + 256 + lane * 2), u3 = *reinterpret_cast<const float2*>(r2 + 384 + lane * 2);
        __builtin_amdgcn_raw_buffer_store_b128(u32x4{__float_as_uint((u0.x + u1.x) + (u2.x + u3.x)), tag, __float_as_uint((u0.y + u1.y) + (u2.y + u3.y)), tag},
                                               mkrs(ws + WS_P2, dry ? 0 : 9 * 16 * 1024), lane * 16, (ii * 16 + kvh) * 1024, AUX_SC1);
      }
      if (w == 0) STAMP(8);
    }
  }
  if (!dry) return;

  {
    float a0[8], a1[8];
    STAMP(11);

    if (task >= 0) {
      const int rr = lane >> 3, cc = lane & 7;
      const int vo = rr * 72 * (FF * 2) + cc * 16;
      const u32* xp = lds + L_XP + rr * 36;
      u32x4 A[8], Bf[8];
      float c0[8], c1[8];
      const __amdgpu_buffer_rsrc_t rs_g = mkrs(wg, DM * FF * 2), rs_u = mkrs(wu, DM * FF * 2);
      const __amdgpu_buffer_rsrc_t rs_d = mkrs(wd + (size_t)task * 64 * DM, 64 * DM * 2);
#define FISSUE(BUF, RS, KB) { const int so_ = (KB) < 9 ? task * 128 + (KB) * 8 * (FF * 2) : DM * FF * 2; \
      _Pragma("unroll") for (int j_ = 0; j_ < 8; ++j_) BUF[j_] = bld<AUX_NT>(RS, vo, so_ + j_ * (FF * 2)); } SB
#define MAC4(BUF, X0, X1, C0, C1) { _Pragma("unroll") for (int j_ = 0; j_ < 4; ++j_) { pair_mac2(BUF[2 * j_], BUF[2 * j_ + 1], X0[j_], X1[j_], C0, C1); pin8(C0); pin8(C1); SB; } }
#define FZERO(C0, C1) _Pragma("unroll") for (int k_ = 0; k_ < 8; ++k_) { C0[k_] = 0.f; C1[k_] = 0.f; }
#define DISSUE(BUF, R0) _Pragma("unroll") for (int j_ = 0; j_ < 8; ++j_) BUF[j_] = bld<AUX_NT>(rs_d, lane * 16, ((R0) + j_) * (DM * 2)); SB
      FISSUE(A, rs_g, 0); FISSUE(Bf, rs_u, 0);
      FZERO(a0, a1); FZERO(c0, c1);
#pragma unroll 1
      for (int k = 0; k < 9; ++k) {
        const u32x4 x0 = *reinterpret_cast<const u32x4*>(xp + 4 * k), x1 = *reinterpret_cast<const u32x4*>(xp + 288 + 4 * k);
        MAC4(A, x0, x1, a0, a1); FISSUE(A, rs_g, k + 1);
        MAC4(Bf, x0, x1, c0, c1); FISSUE(Bf, rs_u, k + 1);
      }
      DISSUE(A, 0); DISSUE(Bf, 8);
      {
        const float gs0 = rs8_hi(a0, lane), gs1 = rs8_hi(a1, lane), us0 = rs8_hi(c0, lane), us1 = rs8_hi(c1, lane);
        const int col = cc * 8 + rr;
        const float g0 = rbf(gs0), g1 = rbf(gs1);
        const float e0 = rbf(0.5f * g0 * (1.0f + tanhf(0.7978845608028654f * (g0 + 0.044715f * g0 * g0 * g0))));
        const float e1 = rbf(0.5f * g1 * (1.0f + tanhf(0.7978845608028654f * (g1 + 0.044715f * g1 * g1 * g1))));
        reinterpret_cast<u16*>(lds + L_H)[col] = (u16)f2bf(e0 * rbf(us0));
        reinterpret_cast<u16*>(lds + L_H)[64 + col] = (u16)f2bf(e1 * rbf(us1));
      }
      __builtin_amdgcn_fence(__ATOMIC_RELEASE, "wavefront"); __builtin_amdgcn_wave_barrier(); __builtin_amdgcn_fence(__ATOMIC_ACQUIRE, "wavefront");
      const u32* hp = lds + L_H;
      FZERO(a0, a1);
#pragma unroll 1
      for (int k = 0; k < 4; ++k) {
        const u32x4 h0 = *reinterpret_cast<const u32x4*>(hp + 8 * k), h1 = *reinterpret_cast<const u32x4*>(hp + 32 + 8 * k);
        const u32x4 h2 = *reinterpret_cast<const u32x4*>(hp + 8 * k + 4), h3 = *reinterpret_cast<const u32x4*>(hp + 32 + 8 * k + 4);
        MAC4(A, h0, h1, a0, a1); DISSUE(A, 16 * k + 16);
        MAC4(Bf, h2, h3, a0, a1); DISSUE(Bf, 16 * k + 24);
      }
#pragma unroll
      for (int j = 0; j < 8; ++j) A[j] = bld<AUX_NT>(rs_d, rr * 8 * (DM * 2) + 1024 + cc * 16, j * (DM * 2));
      SB;
      float* fo = ldsf + L_FO;
      *reinterpret_cast<f32x4*>(fo + rr * 128 + cc * 8) = f32x4{a0[0], a0[1], a0[2], a0[3]};
      *reinterpret_cast<f32x4*>(fo + rr * 128 + cc * 8 + 4) = f32x4{a0[4], a0[5], a0[6], a0[7]};
      *reinterpret_cast<f32x4*>(fo + rr * 128 + 64 + cc * 8) = f32x4{a1[0], a1[1], a1[2], a1[3]};
      *reinterpret_cast<f32x4*>(fo + rr * 128 + 64 + cc * 8 + 4) = f32x4{a1[4], a1[5], a1[6], a1[7]};
      FZERO(a0, a1);
      {
        const u32x4 h0 = *reinterpret_cast<const u32x4*>(hp + 4 * rr), h1 = *reinterpret_cast<const u32x4*>(hp + 32 + 4 * rr);
        MAC4(A, h0, h1, a0, a1);
      }
      const float e0 = rs8_hi(a0, lane), e1 = rs8_hi(a1, lane);
      fo[1024 + cc * 8 + rr] = e0; fo[1024 + 64 + cc * 8 + rr] = e1;
      __builtin_amdgcn_fence(__ATOMIC_RELEASE, "wavefront"); __builtin_amdgcn_wave_barrier(); __builtin_amdgcn_fence(__ATOMIC_ACQUIRE, "wavefront");
      const __amdgpu_buffer_rsrc_t rs_pf = mkrs(ws + WS_PF, 9 * NFT * 1024);
#pragma unroll
      for (int k = 0; k < 9; ++k) {
        const float2 v = *reinterpret_cast<const float2*>(fo + k * 128 + lane * 2);
        __builtin_amdgcn_raw_buffer_store_b128(u32x4{__float_as_uint(v.x), tag, __float_as_uint(v.y), tag}, rs_pf, lane * 16, (k * NFT + task) * 1024, AUX_SC1);
      }
      STAMP(12);
    }

    const bool is_red = (ii == 15 && kvh < 9);
    float f0 = 0.f, f1 = 0.f; u32 xpair = 0;
    if (is_red) {
      xpair = reinterpret_cast<const u32*>(x)[hf * 288 + kvh * 32 + l5];
      const __amdgpu_buffer_rsrc_t rs_f = mkrs(ws + WS_PF + (size_t)kvh * NFT * 1024, NFT * 1024);
      for (int k0 = 0; k0 < NFT; k0 += 12) {
        float p0, p1;
        for (u32 spins = 0;; ++spins) {
          asm volatile("" ::: "memory");
          bool ok = true; p0 = 0.f; p1 = 0.f;
          u32x4 v[12];
#pragma unroll
          for (int k = 0; k < 12; ++k) v[k] = bld<AUX_SC1>(rs_f, lane * 16, (k0 + k) * 1024);
#pragma unroll
          for (int k = 0; k < 12; ++k) { p0 += __uint_as_float(v[k][0]); p1 += __uint_as_float(v[k][2]); ok &= v[k][1] == tag && v[k][3] == tag; }
          if (__all(ok) || spins > SPIN_CAP) break;
          __builtin_amdgcn_s_sleep(64);
        }
        f0 += p0; f1 += p1;
      }
      STAMP(13);
    }
#pragma unroll 1
    for (int pass = DRYRUN && is_red ? 0 : 1; pass < 2; ++pass) {
      if (pass) __syncthreads();
      if (!is_red && ii >= 9) { while (__builtin_amdgcn_s_memrealtime() - tstart_ < FIXT - 1000) __builtin_amdgcn_s_sleep(8);
        if (XFLUSH && g == 31) { __builtin_amdgcn_fence(__ATOMIC_RELEASE, "agent"); asm volatile("s_waitcnt vmcnt(0)" ::: "memory"); __builtin_amdgcn_fence(__ATOMIC_ACQUIRE, "agent"); asm volatile("s_waitcnt vmcnt(0)" ::: "memory"); } }
      if (!is_red) break;
      const __amdgpu_buffer_rsrc_t rs_y = mkrs(ws + WS_P2 + (size_t)kvh * 16 * 1024, pass ? 16 * 1024 : 0);
      float y0, y1;
      for (u32 spins = 0;; ++spins) {
        asm volatile("" ::: "memory");
        bool ok = true; y0 = 0.f; y1 = 0.f;
        u32x4 v[16];
#pragma unroll
        for (int k = 0; k < 16; ++k) v[k] = bld<AUX_SC1>(rs_y, lane * 16, k * 1024);
#pragma unroll
        for (int k = 0; k < 16; ++k) { y0 += __uint_as_float(v[k][0]); y1 += __uint_as_float(v[k][2]); ok &= v[k][1] == tag && v[k][3] == tag; }
        if (__all(ok) || !pass || spins > SPIN_CAP) break;
        __builtin_amdgcn_s_sleep(1);
      }
      const u32 o0 = f2bf(rbf(bf2f(xpair & 0xffffu) + rbf(y0)) + rbf(f0)), o1 = f2bf(rbf(bf2f(xpair >> 16) + rbf(y1)) + rbf(f1));
      __builtin_amdgcn_raw_buffer_store_b32(o0 | (o1 << 16), mkrs(out, pass ? 2 * DM * 2 : 0), (hf * 288 + l5) * 4, kvh * 128, OUT_AUX);
      __builtin_amdgcn_raw_buffer_store_b32(tag, mkrs(ws + WS_EPOCH, pass && kvh == 0 ? 4 : 0), lane * 4, 0, AUX_SC1);
      if (pass) { while (__builtin_amdgcn_s_memrealtime() - tstart_ < FIXT) __builtin_amdgcn_s_sleep(1); }
    }
  }
}

extern "C" void kernel_launch(void* const* d_in, const int* in_sizes, int n_in, void* d_out, int out_size, void* d_ws, size_t ws_size,
                              hipStream_t stream) {
  (void)in_sizes; (void)n_in; (void)out_size; (void)ws_size;
  hipLaunchKernelGGL(fused_decode_kernel, dim3(256), dim3(NTHR), 0, stream, (const u16*)d_in[0], (const u16*)d_in[1], (const u16*)d_in[2],
                     (const u16*)d_in[3], (const u16*)d_in[4], (const u16*)d_in[6], (const u16*)d_in[7], (const u16*)d_in[8], (u16*)d_out,
                     (unsigned char*)d_ws);
}
```

```cpp
#define NSTAT 61
#include <hip/hip_runtime.h>
#include <hip/hip_bf16.h>
#include <stdint.h>

typedef unsigned u32;
typedef unsigned short u16;
typedef unsigned long long u64;
typedef u32 u32x4 __attribute__((ext_vector_type(4)));
typedef u32 u32x2 __attribute__((ext_vector_type(2)));
typedef float f32x2 __attribute__((ext_vector_type(2)));
typedef float f32x4 __attribute__((ext_vector_type(4)));
typedef __bf16 bf16x2 __attribute__((ext_vector_type(2)));
#define GAS __attribute__((address_space(1)))
#define LAS __attribute__((address_space(3)))
typedef GAS u64 gu64;
typedef GAS u32 gu32;
#define RLX_AGENT __ATOMIC_RELAXED, __HIP_MEMORY_SCOPE_AGENT
#define SB __builtin_amdgcn_sched_barrier(0)

constexpr int DM = 576, SK = 32768, DH = 128, FF = 4608, NQ = 4096;
constexpr int NSW = 4, NTHR = (NSW + 1) * 64, U = 4;
constexpr int TB = U * 1024;
constexpr int NT = SK / 16;
constexpr int RT = NT / 8;
#define TAU 554
#define EQ 1000
#define FEND 58000
#define QSLEEP 20
#define NCTR 4
#define NE 8
constexpr int NS = NSTAT;
constexpr int NSF = NS - 4;
constexpr int PEX = (NS - NSF) * NSW;
constexpr int PCOM = (RT - NS * NSW) * 8;
static_assert(PEX == 16 && NE % 2 == 0 && NSF >= 4 * NE + 6 && PCOM + 3 * PEX + 128 < 4096, "");
constexpr int TK = TAU * 256 / 10, TKF = TK * (2 * NS - 12) / (2 * NSF - 12);
constexpr int TF0 = 8000 * 256 / 10, TFS = (FEND - 8000) * 256 / 10 / 17;
constexpr int NFT = 72;
constexpr int AUX_NT = 2, AUX_SC1 = 16;
constexpr float LOG2E = 1.4426950408889634f;
constexpr u32 SPIN_CAP = 1u << 16;

constexpr size_t WS_EPOCH = 0;
constexpr size_t WS_CTR = 4096;
constexpr size_t WS_QG = WS_CTR + 128 * 256;
constexpr size_t WS_P1 = WS_QG + 2 * 64 * 4 * 64 * 8;
constexpr int P1_STRIDE = 264;
constexpr size_t WS_P2 = WS_P1 + (size_t)256 * P1_STRIDE * 8;
constexpr size_t WS_HG = WS_P2 + (size_t)9 * 25 * 128 * 8;
constexpr size_t WS_END = WS_HG + (size_t)NFT * 64 * 8;

__device__ __forceinline__ float bf2f(u32 lo16) { return __uint_as_float(lo16 << 16); }
__device__ __forceinline__ float rbf(float f) { return (float)(__bf16)f; }
__device__ __forceinline__ u32 f2bf(float f) { const __bf16 h = (__bf16)f; return (u32)__builtin_bit_cast(u16, h); }
__device__ __forceinline__ float dot2(u32 a, u32 b, float c) {
  return __builtin_amdgcn_fdot2_f32_bf16(__builtin_bit_cast(bf16x2, a), __builtin_bit_cast(bf16x2, b), c, false);
}
template <int CTRL> __device__ __forceinline__ float dpp(float x) {
  return __int_as_float(__builtin_amdgcn_update_dpp(0, __float_as_int(x), CTRL, 0xf, 0xf, true));
}
constexpr int DPP_XOR1 = 0xB1, DPP_XOR2 = 0x4E, DPP_HMIR = 0x141, DPP_ROR8 = 0x128, DPP_ROR4 = 0x124, DPP_BC = 0x150;
__device__ __forceinline__ void swap32(float& a, float& b) {
  auto r = __builtin_amdgcn_permlane32_swap(__float_as_uint(a), __float_as_uint(b), false, false);
  a = __uint_as_float(r[0]); b = __uint_as_float(r[1]);
}
__device__ __forceinline__ void swap16(float& a, float& b) {
  auto r = __builtin_amdgcn_permlane16_swap(__float_as_uint(a), __float_as_uint(b), false, false);
  a = __uint_as_float(r[0]); b = __uint_as_float(r[1]);
}
__device__ __forceinline__ float half_sum(float x) {
  x += dpp<DPP_XOR1>(x); x += dpp<DPP_XOR2>(x); x += dpp<DPP_ROR4>(x); x += dpp<DPP_ROR8>(x);
  float a = x, b = x; swap16(a, b); return a + b;
}
__device__ __forceinline__ __amdgpu_buffer_rsrc_t mkrs(const void* p, u32 bytes) {
  return __builtin_amdgcn_make_buffer_rsrc(const_cast<void*>(p), 0, bytes, 0x00020000);
}
template <int AUX> __device__ __forceinline__ u32x4 bld(__amdgpu_buffer_rsrc_t rs, int voff, int soff) {
  return __builtin_amdgcn_raw_buffer_load_b128(rs, voff, soff, AUX);
}
__device__ __forceinline__ void pair_mac2(const u32x4& a, const u32x4& b, u32 xp0, u32 xp1, float* acc0, float* acc1) {
#pragma unroll
  for (int d = 0; d < 4; ++d) {
    const u32 lo = __builtin_amdgcn_perm(b[d], a[d], 0x05040100u), hi = __builtin_amdgcn_perm(b[d], a[d], 0x07060302u);
    acc0[2 * d] = dot2(lo, xp0, acc0[2 * d]); acc0[2 * d + 1] = dot2(hi, xp0, acc0[2 * d + 1]);
    acc1[2 * d] = dot2(lo, xp1, acc1[2 * d]); acc1[2 * d + 1] = dot2(hi, xp1, acc1[2 * d + 1]);
  }
}
__device__ __forceinline__ float rs8_hi(float* v, int lane) {
#pragma unroll
  for (int k = 0; k < 4; ++k) { swap32(v[k], v[k + 4]); v[k] += v[k + 4]; }
#pragma unroll
  for (int k = 0; k < 2; ++k) { swap16(v[k], v[k + 2]); v[k] += v[k + 2]; }
  const bool up = lane & 8;
  const float keep = up ? v[1] : v[0], send = up ? v[0] : v[1];
  return keep + dpp<DPP_ROR8>(send);
}
__device__ __forceinline__ void pin8(float* a) {
  asm volatile("" : "+v"(a[0]), "+v"(a[1]), "+v"(a[2]), "+v"(a[3]), "+v"(a[4]), "+v"(a[5]), "+v"(a[6]), "+v"(a[7]));
}
__device__ __forceinline__ void st_gran(gu64* g, u32 tag, u32 v) { __hip_atomic_store(g, ((u64)tag << 32) | (u64)v, RLX_AGENT); }

#define STAMP(k) do {} while (0)

constexpr int L_XP = 0;
constexpr int L_Q = L_XP + 576;
constexpr int L_FLAG = L_Q + 128;
constexpr int L_H = L_FLAG + 4;
constexpr int L_VEC = L_H + 64;
constexpr int L_SC = L_VEC + 512;
constexpr int L_STASH = L_SC + NSW * 64 * 16;
constexpr int L_R2 = L_STASH;
constexpr int L_RED = 1024;
constexpr int L_TOTAL = L_STASH + NSW * NE * 1024;

__global__ void __launch_bounds__(NTHR, 2)
fused_decode_kernel(const u16* __restrict__ x, const u16* __restrict__ kc, const u16* __restrict__ vc, const u16* __restrict__ wq,
                    const u16* __restrict__ wo, const u16* __restrict__ wg, const u16* __restrict__ wu, const u16* __restrict__ wd,
                    u16* __restrict__ out, unsigned char* __restrict__ ws) {
  __shared__ __attribute__((aligned(16))) u32 lds[L_TOTAL];
  const u32 tstart = (u32)__builtin_amdgcn_s_memrealtime();
  asm volatile("" :: "s"(x), "s"(kc), "s"(vc), "s"(wq), "s"(ws), "s"(tstart));
#define PACE(T) { const u32 ta_ = tstart + ((u32)(T) >> 8); while ((int)((u32)__builtin_amdgcn_s_memrealtime() - ta_) < 0) __builtin_amdgcn_s_sleep(1); }
  float* ldsf = reinterpret_cast<float*>(lds);
  const int tid = (int)threadIdx.x - 64, lane = threadIdx.x & 63;
  const int w = __builtin_amdgcn_readfirstlane((int)(threadIdx.x >> 6)) - 1;
  const int bid = blockIdx.x;
  const int s = bid & 7, g = bid >> 3, b = g >> 4, kvh = g & 15;
  const int ii = b * 8 + s;
  const int ee = kvh * 7 + ii - 9;
  const bool is_wo = ii < 9, is_dn = !is_wo && ee < 81, is_red = !is_wo && ee >= 81 && ee < 90;
  const int drb = ee / 9, slice = is_wo ? ii : ee - drb * 9;

  const u16* kbase = kc + (size_t)g * SK * DH;
  const u16* vbase = vc + (size_t)g * SK * DH;
  const int kv_vo = lane * 16, cvo = lane * 4;
  const int t0 = s * RT + w;
  const int hf = lane >> 5, l5 = lane & 31;
  const int sub = (w + s) & (NCTR - 1);
  const int qcg = g + 32 * (s >> 2), qkq = s & 3;
  u32x4 B0[U], B1[U];
  int c0, c1;
#define ISSUE(BUF, CL, BASE, T, REAL) { CL = __builtin_amdgcn_raw_ptr_buffer_atomic_add_i32(1, mkrs(ws + WS_CTR + g * 1024 + sub * 256, (REAL) ? 4 : 0), cvo, 0, 0); SB; \
    const __amdgpu_buffer_rsrc_t rs_ = mkrs(BASE, SK * DH * 2); const int so_ = (T) * TB; \
    _Pragma("unroll") for (int i_ = 0; i_ < U; ++i_) BUF[i_] = bld<AUX_NT>(rs_, kv_vo + i_ * 1024, so_); SB; }
  if (w == 0) STAMP(0);
  volatile LAS u32* lflag = (volatile LAS u32*)(lds + L_FLAG);
  if (threadIdx.x == 0) lflag[1] = 0u;
  asm volatile("; ROLE SPLIT 1" ::: "memory");
  if (w < 0) {
    __builtin_amdgcn_s_setprio(3);
    u32x4 wqv[18];
    u32 xr[9];
    {
      const __amdgpu_buffer_rsrc_t rs_x = mkrs(x, 2 * DM * 2);
      const u32x4 xa = bld<0>(rs_x, lane * 36, 0), xb = bld<0>(rs_x, lane * 36 + 16, 0);
      xr[8] = __builtin_amdgcn_raw_buffer_load_b32(rs_x, lane * 36 + 32, 0, 0);
#pragma unroll
      for (int j = 0; j < 4; ++j) { xr[j] = xa[j]; xr[4 + j] = xb[j]; }
    }
    const u32 epoch_v = __hip_atomic_load((gu32*)(ws + WS_EPOCH), RLX_AGENT);
    const __amdgpu_buffer_rsrc_t rs_wq = mkrs(wq, DM * NQ * 2);
    const int vo = (lane >> 3) * 18 * (NQ * 2) + (lane & 7) * 16;
#pragma unroll
    for (int j = 0; j < 18; ++j) wqv[j] = bld<AUX_NT>(rs_wq, vo, qcg * 128 + (qkq * 144 + j) * (NQ * 2));
    SB;
    __syncthreads();
    {
      float v[18]; float sum = 0.f;
#pragma unroll
      for (int j = 0; j < 9; ++j) { v[2 * j] = bf2f(xr[j] & 0xffffu); v[2 * j + 1] = bf2f(xr[j] >> 16); sum += v[2 * j] + v[2 * j + 1]; }
      const float mean = half_sum(sum) * (1.0f / DM);
      float sq = 0.f;
#pragma unroll
      for (int j = 0; j < 18; ++j) { const float d = v[j] - mean; sq = fmaf(d, d, sq); }
      const float var = half_sum(sq) * (1.0f / DM);
      const float mb = rbf(mean), rsd = 1.0f / rbf(sqrtf(rbf(rbf(var) + 1e-5f)));
#pragma unroll
      for (int j = 0; j < 9; ++j)
        lds[L_XP + lane * 9 + j] = f2bf(rbf(v[2 * j] - mb) * rsd) | (f2bf(rbf(v[2 * j + 1] - mb) * rsd) << 16);
    }
    __builtin_amdgcn_fence(__ATOMIC_RELEASE, "wavefront"); __builtin_amdgcn_wave_barrier(); __builtin_amdgcn_fence(__ATOMIC_ACQUIRE, "wavefront");
    const u32 tag = __builtin_amdgcn_readfirstlane(epoch_v) + 0x9E3779B9u;
    const u32 ctag = tag & 0xfffff000u;
    STAMP(9);
    float a0[8], a1[8];
#pragma unroll
    for (int k = 0; k < 8; ++k) { a0[k] = 0.f; a1[k] = 0.f; }
    {
      const u32* xp = lds + L_XP + qkq * 72 + (lane >> 3) * 9;
#pragma unroll
      for (int j = 0; j < 9; ++j) pair_mac2(wqv[2 * j], wqv[2 * j + 1], xp[j], xp[288 + j], a0, a1);
      const float s0 = rs8_hi(a0, lane), s1 = rs8_hi(a1, lane);
      gu64* qd = (gu64*)(ws + WS_QG) + (qcg * 4 + qkq) * 64 + (lane & 7) * 8 + (lane >> 3);
      st_gran(qd, tag, __float_as_uint(s0)); st_gran(qd + 16384, tag, __float_as_uint(s1));
    }
    if (s == 0 && lane < NCTR) __hip_atomic_store((gu32*)(ws + WS_CTR + g * 1024 + lane * 256), ctag, RLX_AGENT);
    STAMP(10);
    {
      const unsigned char* qsrc = ws + WS_QG + (size_t)(b * 64 + kvh * 4) * 2048;
      __builtin_amdgcn_s_sleep(QSLEEP);
      const __amdgpu_buffer_rsrc_t rs_q = mkrs(qsrc, 8192);
      u32x4 v[8];
      for (u32 spins = 0;; ++spins) {
        asm volatile("" ::: "memory");
#pragma unroll
        for (int k = 0; k < 8; ++k) v[k] = bld<AUX_SC1>(rs_q, hf * 2048 + l5 * 16, (k >> 2) * 4096 + (k & 3) * 512);
        bool ok = (__hip_atomic_load((const gu32*)(ws + WS_CTR + g * 1024 + (lane & (NCTR - 1)) * 256), RLX_AGENT) & 0xfffff000u) == ctag;
#pragma unroll
        for (int k = 0; k < 8; ++k) ok &= v[k][1] == tag && v[k][3] == tag;
        if (__all(ok) || spins > SPIN_CAP) break;
        __builtin_amdgcn_s_sleep(1);
      }
#pragma unroll
      for (int k = 0; k < 2; ++k) {
        const u32x4* u = v + 4 * k;
        const float qa = (__uint_as_float(u[0][0]) + __uint_as_float(u[1][0])) + (__uint_as_float(u[2][0]) + __uint_as_float(u[3][0]));
        const float qb = (__uint_as_float(u[0][2]) + __uint_as_float(u[1][2])) + (__uint_as_float(u[2][2]) + __uint_as_float(u[3][2]));
        lds[L_Q + k * 64 + lane] = f2bf(qa) | (f2bf(qb) << 16);
      }
      lds[L_FLAG] = tag;
      __builtin_amdgcn_fence(__ATOMIC_RELEASE, "workgroup");
      lflag[1] = 1u;
    }
  }
  asm volatile("; ROLE SPLIT 2" ::: "memory");

  const int task = (s & 3) == (g & 3) ? g * 2 + (s >> 2) : ((g >> 2) == s && (g & 3) == ((s + 1) & 3) ? 64 + s : -1);
  const int r = lane >> 4, c = lane & 15;
  if (w >= 0) {
    u32x4 B2[U];
    int c2;
    float acc0[8], acc1[8];
#pragma unroll
    for (int k = 0; k < 8; ++k) { acc0[k] = 0.f; acc1[k] = 0.f; }
    float mrun = -INFINITY, lrun = 0.f;
    u32* mylds = lds + L_STASH + w * NE * 1024;
#define WAITV(N) __builtin_amdgcn_s_waitcnt(0x0f70 | ((N) & 15) | (((N) >> 4) << 14)); SB
    ISSUE(B0, c0, kbase, t0, false); ISSUE(B1, c1, kbase, t0 + NSW, false);
    __syncthreads();
    u32x4* stash = reinterpret_cast<u32x4*>(mylds) + lane;
    int ne = 0, tnext = TK + EQ * 256 / 10;
#pragma unroll 1
    while (ne < NE && lflag[1] == 0u) {
      WAITV(5);
#pragma unroll
      for (int i = 0; i < U; ++i) stash[ne * 256 + i * 64] = B0[i];
      SB; PACE(tnext); tnext += TK; ISSUE(B0, c0, kbase, t0 + (ne + 2) * NSW, false);
      WAITV(5);
#pragma unroll
      for (int i = 0; i < U; ++i) stash[ne * 256 + 256 + i * 64] = B1[i];
      SB; PACE(tnext); tnext += TK; ISSUE(B1, c1, kbase, t0 + (ne + 3) * NSW, false);
      ne += 2;
    }
    if (w == 0) STAMP(1);
    while (lflag[1] == 0u) __builtin_amdgcn_s_sleep(1);
    __builtin_amdgcn_fence(__ATOMIC_ACQUIRE, "workgroup");
    if (w == 0) STAMP(2);
    const u32x4 q0 = *reinterpret_cast<const u32x4*>(lds + L_Q + c * 4);
    const u32x4 q1 = *reinterpret_cast<const u32x4*>(lds + L_Q + 64 + c * 4);
    u16* scl = reinterpret_cast<u16*>(lds + L_SC + w * 64 * 16) + (lane >> 1);

    float mseen = -INFINITY;
    auto kstep = [&](const u32x4* Kt, int slot) {
      float xv[8];
#pragma unroll
      for (int i = 0; i < U; ++i) {
        float d0 = 0.f, d1 = 0.f;
#pragma unroll
        for (int k = 0; k < 4; ++k) { d0 = dot2(Kt[i][k], q0[k], d0); d1 = dot2(Kt[i][k], q1[k], d1); }
        xv[2 * i] = d0; xv[2 * i + 1] = d1;
      }
#pragma unroll
      for (int k = 0; k < 4; ++k) { const bool up = c & 8; const float keep = up ? xv[k + 4] : xv[k], send = up ? xv[k] : xv[k + 4]; xv[k] = keep + dpp<DPP_ROR8>(send); }
#pragma unroll
      for (int k = 0; k < 2; ++k) { const bool up = c & 4; const float keep = up ? xv[k + 2] : xv[k], send = up ? xv[k] : xv[k + 2]; xv[k] = keep + dpp<DPP_HMIR>(send); }
      { const bool up = c & 2; const float keep = up ? xv[1] : xv[0], send = up ? xv[0] : xv[1]; xv[0] = keep + dpp<DPP_XOR2>(send); }
      const float sc = xv[0] + dpp<DPP_XOR1>(xv[0]);
      const u32 sb = f2bf(rbf(sc) * 0.08838834764831845f);
      scl[slot * 32] = (u16)sb;
      mseen = fmaxf(mseen, bf2f(sb));
    };
    auto pv1 = [&](const u32x4& Vt, float p0, float p1) {
#pragma unroll
      for (int k = 0; k < 4; ++k) {
        const f32x2 v = {__uint_as_float(Vt[k] << 16), __uint_as_float(Vt[k] & 0xffff0000u)};
        const f32x2 r0 = __builtin_elementwise_fma(v, f32x2{p0, p0}, f32x2{acc0[2 * k], acc0[2 * k + 1]});
        const f32x2 r1 = __builtin_elementwise_fma(v, f32x2{p1, p1}, f32x2{acc1[2 * k], acc1[2 * k + 1]});
        acc0[2 * k] = r0[0]; acc0[2 * k + 1] = r0[1]; acc1[2 * k] = r1[0]; acc1[2 * k + 1] = r1[1];
      }
    };
    auto vstep = [&](const u32x4* Vt, int slot) {
      const float sc = bf2f(scl[slot * 32]);
      if (__any(mseen > mrun)) {
        float mn = fmaxf(mseen, dpp<DPP_ROR4>(mseen)); mn = fmaxf(mn, dpp<DPP_ROR8>(mn)); mn = fmaxf(mn, mrun);
        const float al = __builtin_amdgcn_exp2f((mrun - mn) * LOG2E);
        mrun = mn; lrun *= al;
        const float al0 = dpp<DPP_BC + 0>(al), al1 = dpp<DPP_BC + 2>(al);
#pragma unroll
        for (int k = 0; k < 8; ++k) { acc0[k] *= al0; acc1[k] *= al1; }
      }
      const float p = __builtin_amdgcn_exp2f((sc - mrun) * LOG2E);
      lrun += p;
      pv1(Vt[0], dpp<DPP_BC + 0>(p), dpp<DPP_BC + 2>(p));
      pv1(Vt[1], dpp<DPP_BC + 4>(p), dpp<DPP_BC + 6>(p));
      pv1(Vt[2], dpp<DPP_BC + 8>(p), dpp<DPP_BC + 10>(p));
      pv1(Vt[3], dpp<DPP_BC + 12>(p), dpp<DPP_BC + 14>(p));
      pin8(acc0); pin8(acc1);
    };
    const int n = task >= 0 ? NSF : NS, tkb = task >= 0 ? TKF : TK;
    const int g3 = g & 3, nx = (g3 == (((g >> 2) + 1) & 3) ? 3 : 2) * PEX;
    int it = ne, iend = 0x7fffffff, ptile = 0;
#define STEP(BUF, CL, NBUF, NCL) { \
      const int i2_ = it + 2 - 2 * n;                        \
      if (it + 2 < iend) { PACE(tnext); } tnext += it >= 10 && i2_ < 0 ? tkb : TK; \
      if (it < iend) { WAITV(5); \
        if (i2_ >= 0 && !(i2_ & 1) && ptile < NT) { const int pi_ = (__builtin_amdgcn_readfirstlane(CL) & 0xfff) * NCTR + sub, pc_ = pi_ - nx; \
          const int e_ = pi_ >> 4, reg_ = e_ == 2 ? (g >> 2) : g3 + 4 * e_; \
          ptile = pc_ < 0 ? reg_ * RT + NSF * NSW + (pi_ & 15) : (pc_ < PCOM ? (pc_ & 7) * RT + NS * NSW + (pc_ >> 3) : NT); \
          if (ptile >= NT) iend = it + 2; } } \
      const bool isv_ = i2_ >= 0 ? (i2_ & 1) : (i2_ >= -n); \
      const int tl_ = i2_ >= 0 ? ptile : t0 + (isv_ ? i2_ + n : i2_ + 2 * n) * NSW; \
      ISSUE(NBUF, NCL, isv_ ? vbase : kbase, tl_, i2_ >= -2 && !(i2_ & 1) && ptile < NT); \
      if (it < iend) { const int i0_ = i2_ - 2; \
        if (i0_ >= 0 ? (i0_ & 1) : (i0_ >= -n)) vstep(BUF, i0_ >= 0 ? 63 : i0_ + n); else kstep(BUF, i0_ >= 0 ? 63 : it); } \
      SB; ++it; }
    do {
      STEP(B0, c0, B2, c2);
      if (ne > 0) {
        --ne;
        u32x4 KE[U];
#pragma unroll
        for (int i = 0; i < U; ++i) KE[i] = stash[ne * 256 + i * 64];
        kstep(KE, ne); SB;
      }
      STEP(B1, c1, B0, c0);
      STEP(B2, c2, B1, c1);
    } while (it < iend);
    if (w == 0) STAMP(5);
    const u32 tag = __builtin_amdgcn_readfirstlane(lds[L_FLAG]);
    const int h = w >> 1;
    {
      float lr = lrun + dpp<DPP_ROR4>(lrun); lr += dpp<DPP_ROR8>(lr);
      float a = mrun, bb = mrun; swap16(a, bb); float mw = fmaxf(a, bb);
      a = mw; bb = mw; swap32(a, bb); mw = fmaxf(a, bb);
      const float f = __builtin_amdgcn_exp2f((mrun - mw) * LOG2E);
      lr *= f;
      a = lr; bb = lr; swap16(a, bb); lr = a + bb;
      a = lr; bb = lr; swap32(a, bb); lr = a + bb;
      const float f0 = dpp<DPP_BC + 0>(f), f1 = dpp<DPP_BC + 2>(f);
#pragma unroll
      for (int k = 0; k < 8; ++k) { acc0[k] *= f0; acc1[k] *= f1; }
#pragma unroll
      for (int k = 0; k < 8; ++k) { swap32(acc0[k], acc1[k]); acc0[k] += acc1[k]; }
#pragma unroll
      for (int k = 0; k < 4; ++k) { swap16(acc0[k], acc0[k + 4]); acc0[k] += acc0[k + 4]; }
      *reinterpret_cast<f32x4*>(mylds + L_RED + (r >> 1) * 128 + c * 8 + (r & 1) * 4) = f32x4{acc0[0], acc0[1], acc0[2], acc0[3]};
      if (lane == 0 || lane == 2) { mylds[L_RED + 256 + lane] = __float_as_uint(mw); mylds[L_RED + 256 + lane + 1] = __float_as_uint(lr); }
    }
    __syncthreads();
    const __amdgpu_buffer_rsrc_t rs_p1 = mkrs(ws + WS_P1 + (size_t)kvh * 16 * P1_STRIDE * 8, 16 * P1_STRIDE * 8);
    {
      const float* red = ldsf + L_STASH + L_RED;
      float M = -INFINITY;
#pragma unroll
      for (int k = 0; k < NSW; ++k) M = fmaxf(M, red[k * NE * 1024 + 256 + h * 2]);
      float o = 0.f, L = 0.f;
#pragma unroll
      for (int k = 0; k < NSW; ++k) {
        const float f = __builtin_amdgcn_exp2f((red[k * NE * 1024 + 256 + h * 2] - M) * LOG2E);
        o = fmaf(f, red[k * NE * 1024 + tid], o); L = fmaf(f, red[k * NE * 1024 + 256 + h * 2 + 1], L);
      }
      __builtin_amdgcn_raw_buffer_store_b64(u32x2{__float_as_uint(o), tag}, rs_p1, tid * 8, ii * (P1_STRIDE * 8), AUX_SC1);
      __builtin_amdgcn_raw_buffer_store_b64(u32x2{__float_as_uint((lane & 1) ? L : M), tag}, rs_p1, (tid & 127) < 2 ? (256 + h * 2 + lane) * 8 : 0x7fff0000, ii * (P1_STRIDE * 8), AUX_SC1);
    }
    if (w == 0) STAMP(6);
    if (!is_wo && !is_dn) return;
    SB;
    u32x4 wv[16];
    {
      const int rw = is_wo ? 8 : 16;
      const u16* wb = is_wo ? wo + (size_t)kvh * 256 * DM : wd + (size_t)drb * 512 * DM;
      const __amdgpu_buffer_rsrc_t rs_a = mkrs(wb, is_wo ? 256 * DM * 2 : (is_dn ? 512 * DM * 2 : 0)), rs_b = mkrs(wb, is_dn ? 512 * DM * 2 : 0);
      const int vo = (lane >> 3) * rw * (DM * 2) + (lane & 7) * 16, so = 8 * w * rw * (DM * 2) + slice * 128;
#pragma unroll
      for (int k = 0; k < 16; ++k) wv[k] = bld<AUX_NT>(k < 8 ? rs_a : rs_b, vo, so + k * (DM * 2));
      SB;
    }
    u32* vec = lds + L_VEC;
    if (is_wo) {
      u32x4 av[8]; u32x2 ml;
      for (u32 spins = 0;; ++spins) {
        asm volatile("" ::: "memory");
#pragma unroll
        for (int k = 0; k < 8; ++k) av[k] = bld<AUX_SC1>(rs_p1, hf * (8 * P1_STRIDE * 8) + w * 512 + l5 * 16, k * (P1_STRIDE * 8));
        ml = __builtin_amdgcn_raw_buffer_load_b64(rs_p1, ((hf * 8 + (c >> 1)) * P1_STRIDE + 256 + h * 2 + (c & 1)) * 8, 0, AUX_SC1);
        bool ok = ml[1] == tag;
#pragma unroll
        for (int k = 0; k < 8; ++k) ok &= av[k][1] == tag && av[k][3] == tag;
        if (__all(ok) || spins > SPIN_CAP) break;
        __builtin_amdgcn_s_sleep(1);
      }
      if (w == 0) STAMP(7);
      const float mlv = __uint_as_float(ml[0]);
      const float mv = dpp<0xA0>(mlv);
      float M = fmaxf(mv, dpp<DPP_XOR2>(mv)); M = fmaxf(M, dpp<DPP_ROR4>(M)); M = fmaxf(M, dpp<DPP_ROR8>(M));
      const float f = __builtin_amdgcn_exp2f((mv - M) * LOG2E);
      float L = f * dpp<0xF5>(mlv);
      L += dpp<DPP_XOR2>(L); L += dpp<DPP_ROR4>(L); L += dpp<DPP_ROR8>(L);
      float oa = 0.f, ob = 0.f;
#define HOP1(K) { const float f_ = dpp<DPP_BC + 2 * (K)>(f); oa = fmaf(__uint_as_float(av[K][0]), f_, oa); ob = fmaf(__uint_as_float(av[K][2]), f_, ob); }
      HOP1(0) HOP1(1) HOP1(2) HOP1(3) HOP1(4) HOP1(5) HOP1(6) HOP1(7)
      const float rl = 1.0f / L;
      vec[w * 128 + hf * 64 + (l5 >> 2) * 8 + (l5 & 3)] = f2bf(oa * rl) | (f2bf(ob * rl) << 16);
      __builtin_amdgcn_fence(__ATOMIC_RELEASE, "wavefront"); __builtin_amdgcn_wave_barrier(); __builtin_amdgcn_fence(__ATOMIC_ACQUIRE, "wavefront");
    }
    {
      float a0[8], a1[8];
#pragma unroll
      for (int k = 0; k < 8; ++k) { a0[k] = 0.f; a1[k] = 0.f; }
      const u32* vp = vec + w * 128 + (lane >> 3) * 8;
      const u32x4 o0 = *reinterpret_cast<const u32x4*>(vp), o1 = *reinterpret_cast<const u32x4*>(vp + 64);
#pragma unroll
      for (int k = 0; k < 4; ++k) pair_mac2(wv[2 * k], wv[2 * k + 1], o0[k], o1[k], a0, a1);
      if (is_dn) {
        const u32x4 o2 = *reinterpret_cast<const u32x4*>(vp + 4), o3 = *reinterpret_cast<const u32x4*>(vp + 68);
#pragma unroll
        for (int k = 0; k < 4; ++k) pair_mac2(wv[8 + 2 * k], wv[9 + 2 * k], o2[k], o3[k], a0, a1);
      }
      const float s0 = rs8_hi(a0, lane), s1 = rs8_hi(a1, lane);
      const int col = (lane & 7) * 8 + (lane >> 3);
      float* r2 = ldsf + L_R2;
      r2[w * 128 + col] = s0; r2[w * 128 + 64 + col] = s1;
      __syncthreads();
      if (w == 0) {
        const float2 u0 = *reinterpret_cast<const float2*>(r2 + lane * 2), u1 = *reinterpret_cast<const float2*>(r2 + 128 + lane * 2);
        const float2 u2 = *reinterpret_cast<const float2*>(r2 + 256 + lane * 2), u3 = *reinterpret_cast<const float2*>(r2 + 384 + lane * 2);
        __builtin_amdgcn_raw_buffer_store_b128(u32x4{__float_as_uint((u0.x + u1.x) + (u2.x + u3.x)), tag, __float_as_uint((u0.y + u1.y) + (u2.y + u3.y)), tag},
                                               mkrs(ws + WS_P2, 9 * 25 * 1024), lane * 16, (slice * 25 + (is_wo ? kvh : 16 + drb)) * 1024, AUX_SC1);
      }
    }
    return;
  }

  {
    const u32 tag = __builtin_amdgcn_readfirstlane(lds[L_FLAG]);
    float a0[8], a1[8];
    STAMP(11);

    if (task >= 0) {
      const int rr = lane >> 3, cc = lane & 7;
      const int vo = rr * 72 * (FF * 2) + cc * 16;
      const u32* xp = lds + L_XP + rr * 36;
      u32x4 A[8], Bf[8];
      float c0[8], c1[8];
      const __amdgpu_buffer_rsrc_t rs_g = mkrs(wg, DM * FF * 2), rs_u = mkrs(wu, DM * FF * 2);
#define FISSUE(BUF, RS, KB) { const int so_ = (KB) < 9 ? task * 128 + (KB) * 8 * (FF * 2) : DM * FF * 2; \
      _Pragma("unroll") for (int j_ = 0; j_ < 8; ++j_) BUF[j_] = bld<AUX_NT>(RS, vo, so_ + j_ * (FF * 2)); } SB
#define MAC4(BUF, X0, X1, C0, C1) { _Pragma("unroll") for (int j_ = 0; j_ < 4; ++j_) { pair_mac2(BUF[2 * j_], BUF[2 * j_ + 1], X0[j_], X1[j_], C0, C1); pin8(C0); pin8(C1); SB; } }
#define FZERO(C0, C1) _Pragma("unroll") for (int k_ = 0; k_ < 8; ++k_) { C0[k_] = 0.f; C1[k_] = 0.f; }
      int tf = TF0;
#define FPACE() { const u32 ta_ = tstart + ((u32)tf >> 8); while ((int)((u32)__builtin_amdgcn_s_memrealtime() - ta_) < 0) __builtin_amdgcn_s_sleep(8); tf += TFS; }
      FPACE(); FISSUE(A, rs_g, 0); FPACE(); FISSUE(Bf, rs_u, 0);
      FZERO(a0, a1); FZERO(c0, c1);
#pragma unroll 1
      for (int k = 0; k < 9; ++k) {
        const u32x4 x0 = *reinterpret_cast<const u32x4*>(xp + 4 * k), x1 = *reinterpret_cast<const u32x4*>(xp + 288 + 4 * k);
        MAC4(A, x0, x1, a0, a1); if (k < 8) { FPACE(); } FISSUE(A, rs_g, k + 1);
        MAC4(Bf, x0, x1, c0, c1); if (k < 8) { FPACE(); } FISSUE(Bf, rs_u, k + 1);
      }
      {
        const float gs0 = rs8_hi(a0, lane), gs1 = rs8_hi(a1, lane), us0 = rs8_hi(c0, lane), us1 = rs8_hi(c1, lane);
        const int col = cc * 8 + rr;
        const float g0 = rbf(gs0), g1 = rbf(gs1);
        const float e0 = rbf(0.5f * g0 * (1.0f + tanhf(0.7978845608028654f * (g0 + 0.044715f * g0 * g0 * g0))));
        const float e1 = rbf(0.5f * g1 * (1.0f + tanhf(0.7978845608028654f * (g1 + 0.044715f * g1 * g1 * g1))));
        reinterpret_cast<u16*>(lds + L_H)[col] = (u16)f2bf(e0 * rbf(us0));
        reinterpret_cast<u16*>(lds + L_H)[64 + col] = (u16)f2bf(e1 * rbf(us1));
      }
      __builtin_amdgcn_fence(__ATOMIC_RELEASE, "wavefront"); __builtin_amdgcn_wave_barrier(); __builtin_amdgcn_fence(__ATOMIC_ACQUIRE, "wavefront");
      st_gran((gu64*)(ws + WS_HG) + task * 64 + lane, tag, lds[L_H + lane]);
      STAMP(12);
    }
    __builtin_amdgcn_s_setprio(0);
    if (is_dn) {
      { const u32 ta_ = tstart + (FEND + 3000) / 10; while ((int)((u32)__builtin_amdgcn_s_memrealtime() - ta_) < 0) __builtin_amdgcn_s_sleep(32); }
      const __amdgpu_buffer_rsrc_t rs_h = mkrs(ws + WS_HG + (size_t)drb * 4096, 4096);
      u32x4 v[4];
      for (u32 spins = 0;; ++spins) {
        asm volatile("" ::: "memory");
        bool ok = true;
#pragma unroll
        for (int k = 0; k < 4; ++k) v[k] = bld<AUX_SC1>(rs_h, lane * 16, k * 1024);
#pragma unroll
        for (int k = 0; k < 4; ++k) ok &= v[k][1] == tag && v[k][3] == tag;
        if (__all(ok) || spins > SPIN_CAP) break;
        __builtin_amdgcn_s_sleep(16);
      }
#pragma unroll
      for (int k = 0; k < 4; ++k) *reinterpret_cast<u32x2*>(lds + L_VEC + k * 128 + (l5 >> 4) * 64 + hf * 32 + (l5 & 15) * 2) = u32x2{v[k][0], v[k][2]};
      STAMP(13);
    }
    u32 xpair = 0;
    if (is_red) xpair = reinterpret_cast<const u32*>(x)[hf * 288 + slice * 32 + l5];
    __syncthreads();
    if (is_red) {
      const __amdgpu_buffer_rsrc_t rs_y = mkrs(ws + WS_P2 + (size_t)slice * 25 * 1024, 25 * 1024);
      float y0, y1, f0, f1;
      for (u32 spins = 0;; ++spins) {
        asm volatile("" ::: "memory");
        bool ok = true; y0 = 0.f; y1 = 0.f; f0 = 0.f; f1 = 0.f;
        u32x4 v[25];
#pragma unroll
        for (int k = 0; k < 25; ++k) v[k] = bld<AUX_SC1>(rs_y, lane * 16, k * 1024);
#pragma unroll
        for (int k = 0; k < 25; ++k) {
          if (k < 16) { y0 += __uint_as_float(v[k][0]); y1 += __uint_as_float(v[k][2]); } else { f0 += __uint_as_float(v[k][0]); f1 += __uint_as_float(v[k][2]); }
          ok &= v[k][1] == tag && v[k][3] == tag;
        }
        if (__all(ok) || spins > SPIN_CAP) break;
        __builtin_amdgcn_s_sleep(1);
      }
      const u32 o0 = f2bf(rbf(bf2f(xpair & 0xffffu) + rbf(y0)) + rbf(f0)), o1 = f2bf(rbf(bf2f(xpair >> 16) + rbf(y1)) + rbf(f1));
      reinterpret_cast<u32*>(out)[hf * 288 + slice * 32 + l5] = o0 | (o1 << 16);
      if (slice == 0 && lane == 0) __hip_atomic_store((gu32*)(ws + WS_EPOCH), tag, RLX_AGENT);
      STAMP(14);
    }
  }
}

extern "C" void kernel_launch(void* const* d_in, const int* in_sizes, int n_in, void* d_out, int out_size, void* d_ws, size_t ws_size,
                              hipStream_t stream) {
  (void)in_sizes; (void)n_in; (void)out_size; (void)ws_size;
  hipLaunchKernelGGL(fused_decode_kernel, dim3(256), dim3(NTHR), 0, stream, (const u16*)d_in[0], (const u16*)d_in[1], (const u16*)d_in[2],
                     (const u16*)d_in[3], (const u16*)d_in[4], (const u16*)d_in[6], (const u16*)d_in[7], (const u16*)d_in[8], (u16*)d_out,
                     (unsigned char*)d_ws);
}
```

```cpp
#define EQ 1500
#include <hip/hip_runtime.h>
#include <hip/hip_bf16.h>
#include <stdint.h>

typedef unsigned u32;
typedef unsigned short u16;
typedef unsigned long long u64;
typedef u32 u32x4 __attribute__((ext_vector_type(4)));
typedef u32 u32x2 __attribute__((ext_vector_type(2)));
typedef float f32x2 __attribute__((ext_vector_type(2)));
typedef float f32x4 __attribute__((ext_vector_type(4)));
typedef __bf16 bf16x2 __attribute__((ext_vector_type(2)));
#define GAS __attribute__((address_space(1)))
#define LAS __attribute__((address_space(3)))
typedef GAS u64 gu64;
typedef GAS u32 gu32;
#define RLX_AGENT __ATOMIC_RELAXED, __HIP_MEMORY_SCOPE_AGENT
#define SB __builtin_amdgcn_sched_barrier(0)

constexpr int DM = 576, SK = 32768, DH = 128, FF = 4608, NQ = 4096;
constexpr int NSW = 4, NTHR = (NSW + 1) * 64, U = 4;
constexpr int TB = U * 1024;
constexpr int NT = SK / 16;
constexpr int RT = NT / 8;
#define NSTAT 61
#define TAU 554
#define FEND 58000
#define PACE_END 1000
#define NX 4
#define QSLEEP 20
#define NCTR 4
#define NE 8
constexpr int NS = NSTAT;
constexpr int NSF = NS - 4;
constexpr int PEX = (NS - NSF) * NSW;
constexpr int PCOM = (RT - NS * NSW) * 8;
static_assert(PEX == 16 && NE % 2 == 0 && NSF >= 4 * (NE + NX) + 6 && NX <= NE && PCOM + 3 * PEX + 128 < 4096, "");
constexpr int TK = TAU * 256 / 10, TKF = TK * (2 * NS - 12) / (2 * NSF - 12);
constexpr int TF0 = 8000 * 256 / 10, TFS = (FEND - 8000) * 256 / 10 / 17;
constexpr int NFT = 72;
constexpr int AUX_NT = 2, AUX_SC1 = 16;
constexpr float LOG2E = 1.4426950408889634f;
constexpr u32 SPIN_CAP = 1u << 16;

constexpr size_t WS_EPOCH = 0;
constexpr size_t WS_CTR = 4096;
constexpr size_t WS_QG = WS_CTR + 128 * 256;
constexpr size_t WS_P1 = WS_QG + 2 * 64 * 4 * 64 * 8;
constexpr int P1_STRIDE = 264;
constexpr size_t WS_P2 = WS_P1 + (size_t)256 * P1_STRIDE * 8;
constexpr size_t WS_HG = WS_P2 + (size_t)9 * 25 * 128 * 8;
constexpr size_t WS_END = WS_HG + (size_t)NFT * 64 * 8;

__device__ __forceinline__ float bf2f(u32 lo16) { return __uint_as_float(lo16 << 16); }
__device__ __forceinline__ float rbf(float f) { return (float)(__bf16)f; }
__device__ __forceinline__ u32 f2bf(float f) { const __bf16 h = (__bf16)f; return (u32)__builtin_bit_cast(u16, h); }
__device__ __forceinline__ float dot2(u32 a, u32 b, float c) {
  return __builtin_amdgcn_fdot2_f32_bf16(__builtin_bit_cast(bf16x2, a), __builtin_bit_cast(bf16x2, b), c, false);
}
template <int CTRL> __device__ __forceinline__ float dpp(float x) {
  return __int_as_float(__builtin_amdgcn_update_dpp(0, __float_as_int(x), CTRL, 0xf, 0xf, true));
}
constexpr int DPP_XOR1 = 0xB1, DPP_XOR2 = 0x4E, DPP_HMIR = 0x141, DPP_ROR8 = 0x128, DPP_ROR4 = 0x124, DPP_BC = 0x150;
__device__ __forceinline__ void swap32(float& a, float& b) {
  auto r = __builtin_amdgcn_permlane32_swap(__float_as_uint(a), __float_as_uint(b), false, false);
  a = __uint_as_float(r[0]); b = __uint_as_float(r[1]);
}
__device__ __forceinline__ void swap16(float& a, float& b) {
  auto r = __builtin_amdgcn_permlane16_swap(__float_as_uint(a), __float_as_uint(b), false, false);
  a = __uint_as_float(r[0]); b = __uint_as_float(r[1]);
}
__device__ __forceinline__ float half_sum(float x) {
  x += dpp<DPP_XOR1>(x); x += dpp<DPP_XOR2>(x); x += dpp<DPP_ROR4>(x); x += dpp<DPP_ROR8>(x);
  float a = x, b = x; swap16(a, b); return a + b;
}
__device__ __forceinline__ __amdgpu_buffer_rsrc_t mkrs(const void* p, u32 bytes) {
  return __builtin_amdgcn_make_buffer_rsrc(const_cast<void*>(p), 0, bytes, 0x00020000);
}
template <int AUX> __device__ __forceinline__ u32x4 bld(__amdgpu_buffer_rsrc_t rs, int voff, int soff) {
  return __builtin_amdgcn_raw_buffer_load_b128(rs, voff, soff, AUX);
}
__device__ __forceinline__ void pair_mac2(const u32x4& a, const u32x4& b, u32 xp0, u32 xp1, float* acc0, float* acc1) {
#pragma unroll
  for (int d = 0; d < 4; ++d) {
    const u32 lo = __builtin_amdgcn_perm(b[d], a[d], 0x05040100u), hi = __builtin_amdgcn_perm(b[d], a[d], 0x07060302u);
    acc0[2 * d] = dot2(lo, xp0, acc0[2 * d]); acc0[2 * d + 1] = dot2(hi, xp0, acc0[2 * d + 1]);
    acc1[2 * d] = dot2(lo, xp1, acc1[2 * d]); acc1[2 * d + 1] = dot2(hi, xp1, acc1[2 * d + 1]);
  }
}
__device__ __forceinline__ float rs8_hi(float* v, int lane) {
#pragma unroll
  for (int k = 0; k < 4; ++k) { swap32(v[k], v[k + 4]); v[k] += v[k + 4]; }
#pragma unroll
  for (int k = 0; k < 2; ++k) { swap16(v[k], v[k + 2]); v[k] += v[k + 2]; }
  const bool up = lane & 8;
  const float keep = up ? v[1] : v[0], send = up ? v[0] : v[1];
  return keep + dpp<DPP_ROR8>(send);
}
__device__ __forceinline__ void pin8(float* a) {
  asm volatile("" : "+v"(a[0]), "+v"(a[1]), "+v"(a[2]), "+v"(a[3]), "+v"(a[4]), "+v"(a[5]), "+v"(a[6]), "+v"(a[7]));
}
__device__ __forceinline__ void st_gran(gu64* g, u32 tag, u32 v) { __hip_atomic_store(g, ((u64)tag << 32) | (u64)v, RLX_AGENT); }

#define STAMP(k) do {} while (0)

constexpr int L_XP = 0;
constexpr int L_Q = L_XP + 576;
constexpr int L_FLAG = L_Q + 128;
constexpr int L_H = L_FLAG + 4;
constexpr int L_VEC = L_H + 64;
constexpr int L_SC = L_VEC + 512;
constexpr int L_STASH = L_SC + NSW * 64 * 16;
constexpr int L_R2 = L_STASH;
constexpr int L_RED = 1024;
constexpr int L_TOTAL = L_STASH + NSW * NE * 1024;

__global__ void __launch_bounds__(NTHR, 2)
fused_decode_kernel(const u16* __restrict__ x, const u16* __restrict__ kc, const u16* __restrict__ vc, const u16* __restrict__ wq,
                    const u16* __restrict__ wo, const u16* __restrict__ wg, const u16* __restrict__ wu, const u16* __restrict__ wd,
                    u16* __restrict__ out, unsigned char* __restrict__ ws) {
  __shared__ __attribute__((aligned(16))) u32 lds[L_TOTAL];
  const u64 tstart64 = __builtin_amdgcn_s_memrealtime();
  const u32 tstart = (u32)tstart64;
  asm volatile("" :: "s"(x), "s"(kc), "s"(vc), "s"(wq), "s"(ws), "s"(tstart));
#define PACE(T) { const u32 ta_ = tstart + ((u32)(T) >> 8); while ((int)((u32)__builtin_amdgcn_s_memrealtime() - ta_) < 0) __builtin_amdgcn_s_sleep(1); }
  float* ldsf = reinterpret_cast<float*>(lds);
  const int tid = (int)threadIdx.x - 64, lane = threadIdx.x & 63;
  const int w = __builtin_amdgcn_readfirstlane((int)(threadIdx.x >> 6)) - 1;
  const int bid = blockIdx.x;
  const int s = bid & 7, g = bid >> 3, b = g >> 4, kvh = g & 15;
  const int ii = b * 8 + s;
  const int ee = kvh * 7 + ii - 9;
  const bool is_wo = ii < 9, is_dn = !is_wo && ee < 81, is_red = !is_wo && ee >= 81 && ee < 90;
  const int drb = ee / 9, slice = is_wo ? ii : ee - drb * 9;

  const u16* kbase = kc + (size_t)g * SK * DH;
  const u16* vbase = vc + (size_t)g * SK * DH;
  const int kv_vo = lane * 16, cvo = lane * 4;
  const int t0 = s * RT + w;
  const int hf = lane >> 5, l5 = lane & 31;
  const int sub = (w + s) & (NCTR - 1);
  const int qcg = g + 32 * (s >> 2), qkq = s & 3;
  u32x4 EX[NX][U];
#define ISSUE0(BUF, T) { const __amdgpu_buffer_rsrc_t rs_ = mkrs(kbase, SK * DH * 2); const int so_ = (T) * TB; \
    _Pragma("unroll") for (int i_ = 0; i_ < U; ++i_) BUF[i_] = bld<AUX_NT>(rs_, kv_vo + i_ * 1024, so_); SB; }
#define ISSUE(BUF, CL, BASE, T, REAL) { CL = __builtin_amdgcn_raw_ptr_buffer_atomic_add_i32(1, mkrs(ws + WS_CTR + g * 1024 + sub * 256, (REAL) ? 4 : 0), cvo, 0, 0); SB; \
    const __amdgpu_buffer_rsrc_t rs_ = mkrs(BASE, SK * DH * 2); const int so_ = (T) * TB; \
    _Pragma("unroll") for (int i_ = 0; i_ < U; ++i_) BUF[i_] = bld<AUX_NT>(rs_, kv_vo + i_ * 1024, so_); SB; }
  if (w >= 0) { ISSUE0(EX[0], t0); ISSUE0(EX[1], t0 + NSW); }
  asm volatile("; ROLE SPLIT 0" ::: "memory");
  volatile LAS u32* lflag = (volatile LAS u32*)(lds + L_FLAG);
  if (threadIdx.x == 0) lflag[1] = 0u;
  asm volatile("; ROLE SPLIT 1" ::: "memory");
  if (w < 0) {
    __builtin_amdgcn_s_setprio(3);
    u32x4 wqv[18];
    u32 xr[9];
    {
      const __amdgpu_buffer_rsrc_t rs_x = mkrs(x, 2 * DM * 2);
      const u32x4 xa = bld<0>(rs_x, lane * 36, 0), xb = bld<0>(rs_x, lane * 36 + 16, 0);
      xr[8] = __builtin_amdgcn_raw_buffer_load_b32(rs_x, lane * 36 + 32, 0, 0);
#pragma unroll
      for (int j = 0; j < 4; ++j) { xr[j] = xa[j]; xr[4 + j] = xb[j]; }
    }
    const u32 epoch_v = __hip_atomic_load((gu32*)(ws + WS_EPOCH), RLX_AGENT);
    const __amdgpu_buffer_rsrc_t rs_wq = mkrs(wq, DM * NQ * 2);
    const int vo = (lane >> 3) * 18 * (NQ * 2) + (lane & 7) * 16;
#pragma unroll
    for (int j = 0; j < 18; ++j) wqv[j] = bld<AUX_NT>(rs_wq, vo, qcg * 128 + (qkq * 144 + j) * (NQ * 2));
    SB;
    {
      float v[18]; float sum = 0.f;
#pragma unroll
      for (int j = 0; j < 9; ++j) { v[2 * j] = bf2f(xr[j] & 0xffffu); v[2 * j + 1] = bf2f(xr[j] >> 16); sum += v[2 * j] + v[2 * j + 1]; }
      const float mean = half_sum(sum) * (1.0f / DM);
      float sq = 0.f;
#pragma unroll
      for (int j = 0; j < 18; ++j) { const float d = v[j] - mean; sq = fmaf(d, d, sq); }
      const float var = half_sum(sq) * (1.0f / DM);
      const float mb = rbf(mean), rsd = 1.0f / rbf(sqrtf(rbf(rbf(var) + 1e-5f)));
#pragma unroll
      for (int j = 0; j < 9; ++j)
        lds[L_XP + lane * 9 + j] = f2bf(rbf(v[2 * j] - mb) * rsd) | (f2bf(rbf(v[2 * j + 1] - mb) * rsd) << 16);
    }
    __builtin_amdgcn_fence(__ATOMIC_RELEASE, "wavefront"); __builtin_amdgcn_wave_barrier(); __builtin_amdgcn_fence(__ATOMIC_ACQUIRE, "wavefront");
    const u32 tag = __builtin_amdgcn_readfirstlane(epoch_v) + 0x9E3779B9u;
    const u32 ctag = tag & 0xfffff000u;
    STAMP(9);
    float a0[8], a1[8];
#pragma unroll
    for (int k = 0; k < 8; ++k) { a0[k] = 0.f; a1[k] = 0.f; }
    {
      const u32* xp = lds + L_XP + qkq * 72 + (lane >> 3) * 9;
#pragma unroll
      for (int j = 0; j < 9; ++j) pair_mac2(wqv[2 * j], wqv[2 * j + 1], xp[j], xp[288 + j], a0, a1);
      const float s0 = rs8_hi(a0, lane), s1 = rs8_hi(a1, lane);
      gu64* qd = (gu64*)(ws + WS_QG) + (qcg * 4 + qkq) * 64 + (lane & 7) * 8 + (lane >> 3);
      st_gran(qd, tag, __float_as_uint(s0)); st_gran(qd + 16384, tag, __float_as_uint(s1));
    }
    if (s == 0 && lane < NCTR) __hip_atomic_store((gu32*)(ws + WS_CTR + g * 1024 + lane * 256), ctag, RLX_AGENT);
    STAMP(10);
    __syncthreads();
    {
      const unsigned char* qsrc = ws + WS_QG + (size_t)(b * 64 + kvh * 4) * 2048;
      __builtin_amdgcn_s_sleep(QSLEEP);
      const __amdgpu_buffer_rsrc_t rs_q = mkrs(qsrc, 8192);
      u32x4 v[8];
      for (u32 spins = 0;; ++spins) {
        asm volatile("" ::: "memory");
#pragma unroll
        for (int k = 0; k < 8; ++k) v[k] = bld<AUX_SC1>(rs_q, hf * 2048 + l5 * 16, (k >> 2) * 4096 + (k & 3) * 512);
        bool ok = (__hip_atomic_load((const gu32*)(ws + WS_CTR + g * 1024 + (lane & (NCTR - 1)) * 256), RLX_AGENT) & 0xfffff000u) == ctag;
#pragma unroll
        for (int k = 0; k < 8; ++k) ok &= v[k][1] == tag && v[k][3] == tag;
        if (__all(ok) || spins > SPIN_CAP) break;
        __builtin_amdgcn_s_sleep(1);
      }
#pragma unroll
      for (int k = 0; k < 2; ++k) {
        const u32x4* u = v + 4 * k;
        const float qa = (__uint_as_float(u[0][0]) + __uint_as_float(u[1][0])) + (__uint_as_float(u[2][0]) + __uint_as_float(u[3][0]));
        const float qb = (__uint_as_float(u[0][2]) + __uint_as_float(u[1][2])) + (__uint_as_float(u[2][2]) + __uint_as_float(u[3][2]));
        lds[L_Q + k * 64 + lane] = f2bf(qa) | (f2bf(qb) << 16);
      }
      lds[L_FLAG] = tag;
      __builtin_amdgcn_fence(__ATOMIC_RELEASE, "workgroup");
      lflag[1] = 1u;
    }
  }
  asm volatile("; ROLE SPLIT 2" ::: "memory");

  const int task = (s & 3) == (g & 3) ? g * 2 + (s >> 2) : ((g >> 2) == s && (g & 3) == ((s + 1) & 3) ? 64 + s : -1);
  const int r = lane >> 4, c = lane & 15;
  auto final_rows = [&](int j) {
    const u32 tag = __builtin_amdgcn_readfirstlane(lds[L_FLAG]);
    const __amdgpu_buffer_rsrc_t rs_y = mkrs(ws + WS_P2 + (size_t)(slice * 25 + j * 5) * 1024, 5 * 1024);
    u32x4 v[5];
    for (u32 spins = 0;; ++spins) {
      asm volatile("" ::: "memory");
      bool ok = true;
#pragma unroll
      for (int k = 0; k < 5; ++k) v[k] = bld<AUX_SC1>(rs_y, lane * 16, k * 1024);
#pragma unroll
      for (int k = 0; k < 5; ++k) ok &= v[k][1] == tag && v[k][3] == tag;
      if (__all(ok) || spins > SPIN_CAP) break;
      __builtin_amdgcn_s_sleep(1);
    }
    const float a0 = __uint_as_float(v[0][0]), a1 = __uint_as_float(v[0][2]);
    const float b0 = (__uint_as_float(v[1][0]) + __uint_as_float(v[2][0])) + (__uint_as_float(v[3][0]) + __uint_as_float(v[4][0]));
    const float b1 = (__uint_as_float(v[1][2]) + __uint_as_float(v[2][2])) + (__uint_as_float(v[3][2]) + __uint_as_float(v[4][2]));
    *reinterpret_cast<f32x4*>(ldsf + L_SC + (j * 64 + lane) * 4) = j < 3 ? f32x4{a0 + b0, a1 + b1, 0.f, 0.f} : (j == 3 ? f32x4{a0, a1, b0, b1} : f32x4{0.f, 0.f, a0 + b0, a1 + b1});
  };
  if (w >= 0) {
    u32x4 B0[U], B1[U], B2[U];
    int c0, c1, c2;
    float acc0[8], acc1[8];
#pragma unroll
    for (int k = 0; k < 8; ++k) { acc0[k] = 0.f; acc1[k] = 0.f; }
    float mrun = -INFINITY, lrun = 0.f;
    u32* mylds = lds + L_STASH + w * NE * 1024;
#define WAITV(N) __builtin_amdgcn_s_waitcnt(0x0f70 | ((N) & 15) | (((N) >> 4) << 14)); SB
    int ne = 0, tnext = TK + EQ * 256 / 10;
#pragma unroll
    for (int e = 2; e < NX; ++e) { WAITV(4); PACE(tnext); tnext += TK; ISSUE0(EX[e], t0 + e * NSW); }
    WAITV(4); PACE(tnext); tnext += TK; ISSUE(B0, c0, kbase, t0 + NX * NSW, false);
    WAITV(5); PACE(tnext); tnext += TK; ISSUE(B1, c1, kbase, t0 + (NX + 1) * NSW, false);
    __syncthreads();
    u32x4* stash = reinterpret_cast<u32x4*>(mylds) + lane;
#pragma unroll 1
    while (ne < NE && lflag[1] == 0u) {
      WAITV(5);
#pragma unroll
      for (int i = 0; i < U; ++i) stash[ne * 256 + i * 64] = B0[i];
      SB; PACE(tnext); tnext += TK; ISSUE(B0, c0, kbase, t0 + (NX + ne + 2) * NSW, false);
      WAITV(5);
#pragma unroll
      for (int i = 0; i < U; ++i) stash[ne * 256 + 256 + i * 64] = B1[i];
      SB; PACE(tnext); tnext += TK; ISSUE(B1, c1, kbase, t0 + (NX + ne + 3) * NSW, false);
      ne += 2;
    }
    if (w == 0) STAMP(1);
    while (lflag[1] == 0u) __builtin_amdgcn_s_sleep(1);
    __builtin_amdgcn_fence(__ATOMIC_ACQUIRE, "workgroup");
    if (w == 0) STAMP(2);
    const u32x4 q0 = *reinterpret_cast<const u32x4*>(lds + L_Q + c * 4);
    const u32x4 q1 = *reinterpret_cast<const u32x4*>(lds + L_Q + 64 + c * 4);
    u16* scl = reinterpret_cast<u16*>(lds + L_SC + w * 64 * 16) + (lane >> 1);

    float mseen = -INFINITY;
    auto kstep = [&](const u32x4* Kt, int slot) {
      float xv[8];
#pragma unroll
      for (int i = 0; i < U; ++i) {
        float d0 = 0.f, d1 = 0.f;
#pragma unroll
        for (int k = 0; k < 4; ++k) { d0 = dot2(Kt[i][k], q0[k], d0); d1 = dot2(Kt[i][k], q1[k], d1); }
        xv[2 * i] = d0; xv[2 * i + 1] = d1;
      }
#pragma unroll
      for (int k = 0; k < 4; ++k) { const bool up = c & 8; const float keep = up ? xv[k + 4] : xv[k], send = up ? xv[k] : xv[k + 4]; xv[k] = keep + dpp<DPP_ROR8>(send); }
#pragma unroll
      for (int k = 0; k < 2; ++k) { const bool up = c & 4; const float keep = up ? xv[k + 2] : xv[k], send = up ? xv[k] : xv[k + 2]; xv[k] = keep + dpp<DPP_HMIR>(send); }
      { const bool up = c & 2; const float keep = up ? xv[1] : xv[0], send = up ? xv[0] : xv[1]; xv[0] = keep + dpp<DPP_XOR2>(send); }
      const float sc = xv[0] + dpp<DPP_XOR1>(xv[0]);
      const u32 sb = f2bf(rbf(sc) * 0.08838834764831845f);
      scl[slot * 32] = (u16)sb;
      mseen = fmaxf(mseen, bf2f(sb));
    };
    auto pv1 = [&](const u32x4& Vt, float p0, float p1) {
#pragma unroll
      for (int k = 0; k < 4; ++k) {
        const f32x2 v = {__uint_as_float(Vt[k] << 16), __uint_as_float(Vt[k] & 0xffff0000u)};
        const f32x2 r0 = __builtin_elementwise_fma(v, f32x2{p0, p0}, f32x2{acc0[2 * k], acc0[2 * k + 1]});
        const f32x2 r1 = __builtin_elementwise_fma(v, f32x2{p1, p1}, f32x2{acc1[2 * k], acc1[2 * k + 1]});
        acc0[2 * k] = r0[0]; acc0[2 * k + 1] = r0[1]; acc1[2 * k] = r1[0]; acc1[2 * k + 1] = r1[1];
      }
    };
    auto vstep = [&](const u32x4* Vt, int slot) {
      const float sc = bf2f(scl[slot * 32]);
      if (__any(mseen > mrun)) {
        float mn = fmaxf(mseen, dpp<DPP_ROR4>(mseen)); mn = fmaxf(mn, dpp<DPP_ROR8>(mn)); mn = fmaxf(mn, mrun);
        const float al = __builtin_amdgcn_exp2f((mrun - mn) * LOG2E);
        mrun = mn; lrun *= al;
        const float al0 = dpp<DPP_BC + 0>(al), al1 = dpp<DPP_BC + 2>(al);
#pragma unroll
        for (int k = 0; k < 8; ++k) { acc0[k] *= al0; acc1[k] *= al1; }
      }
      const float p = __builtin_amdgcn_exp2f((sc - mrun) * LOG2E);
      lrun += p;
      pv1(Vt[0], dpp<DPP_BC + 0>(p), dpp<DPP_BC + 2>(p));
      pv1(Vt[1], dpp<DPP_BC + 4>(p), dpp<DPP_BC + 6>(p));
      pv1(Vt[2], dpp<DPP_BC + 8>(p), dpp<DPP_BC + 10>(p));
      pv1(Vt[3], dpp<DPP_BC + 12>(p), dpp<DPP_BC + 14>(p));
      pin8(acc0); pin8(acc1);
    };
    const int n = task >= 0 ? NSF : NS, tkb = task >= 0 ? TKF : TK;
    const int g3 = g & 3, nx = (g3 == (((g >> 2) + 1) & 3) ? 3 : 2) * PEX;
    int it = NX + ne, iend = 0x7fffffff, ptile = 0, sbase = NX;
#define STEP(BUF, CL, NBUF, NCL) { \
      const int i2_ = it + 2 - 2 * n;                        \
      if (it + 2 < iend && i2_ < PACE_END) { PACE(tnext); } tnext += it >= 10 && i2_ < 0 ? tkb : TK; \
      if (it < iend) { WAITV(5); \
        if (i2_ >= 0 && !(i2_ & 1) && ptile < NT) { const int pi_ = (__builtin_amdgcn_readfirstlane(CL) & 0xfff) * NCTR + sub, pc_ = pi_ - nx; \
          const int e_ = pi_ >> 4, reg_ = e_ == 2 ? (g >> 2) : g3 + 4 * e_; \
          ptile = pc_ < 0 ? reg_ * RT + NSF * NSW + (pi_ & 15) : (pc_ < PCOM ? (pc_ & 7) * RT + NS * NSW + (pc_ >> 3) : NT); \
          if (ptile >= NT) iend = it + 2; } } \
      const bool isv_ = i2_ >= 0 ? (i2_ & 1) : (i2_ >= -n); \
      const int tl_ = i2_ >= 0 ? ptile : t0 + (isv_ ? i2_ + n : i2_ + 2 * n) * NSW; \
      ISSUE(NBUF, NCL, isv_ ? vbase : kbase, tl_, i2_ >= -2 && !(i2_ & 1) && ptile < NT); \
      if (it < iend) { const int i0_ = i2_ - 2; \
        if (i0_ >= 0 ? (i0_ & 1) : (i0_ >= -n)) vstep(BUF, i0_ >= 0 ? 63 : i0_ + n); else kstep(BUF, i0_ >= 0 ? 63 : it); } \
      SB; ++it; }
    do {
      STEP(B0, c0, B2, c2);
      if (ne == 0 && sbase != 0) {
#pragma unroll
        for (int e = 0; e < NX; ++e)
#pragma unroll
          for (int i = 0; i < U; ++i) stash[e * 256 + i * 64] = EX[e][i];
        ne = NX; sbase = 0;
      }
      if (ne > 0) {
        --ne;
        u32x4 KE[U];
#pragma unroll
        for (int i = 0; i < U; ++i) KE[i] = stash[ne * 256 + i * 64];
        kstep(KE, sbase + ne); SB;
      }
      STEP(B1, c1, B0, c0);
      STEP(B2, c2, B1, c1);
    } while (it < iend);
    if (w == 0) STAMP(5);
    const u32 tag = __builtin_amdgcn_readfirstlane(lds[L_FLAG]);
    const int h = w >> 1;
    {
      float lr = lrun + dpp<DPP_ROR4>(lrun); lr += dpp<DPP_ROR8>(lr);
      float a = mrun, bb = mrun; swap16(a, bb); float mw = fmaxf(a, bb);
      a = mw; bb = mw; swap32(a, bb); mw = fmaxf(a, bb);
      const float f = __builtin_amdgcn_exp2f((mrun - mw) * LOG2E);
      lr *= f;
      a = lr; bb = lr; swap16(a, bb); lr = a + bb;
      a = lr; bb = lr; swap32(a, bb); lr = a + bb;
      const float f0 = dpp<DPP_BC + 0>(f), f1 = dpp<DPP_BC + 2>(f);
#pragma unroll
      for (int k = 0; k < 8; ++k) { acc0[k] *= f0; acc1[k] *= f1; }
#pragma unroll
      for (int k = 0; k < 8; ++k) { swap32(acc0[k], acc1[k]); acc0[k] += acc1[k]; }
#pragma unroll
      for (int k = 0; k < 4; ++k) { swap16(acc0[k], acc0[k + 4]); acc0[k] += acc0[k + 4]; }
      *reinterpret_cast<f32x4*>(mylds + L_RED + (r >> 1) * 128 + c * 8 + (r & 1) * 4) = f32x4{acc0[0], acc0[1], acc0[2], acc0[3]};
      if (lane == 0 || lane == 2) { mylds[L_RED + 256 + lane] = __float_as_uint(mw); mylds[L_RED + 256 + lane + 1] = __float_as_uint(lr); }
    }
    __syncthreads();
    const __amdgpu_buffer_rsrc_t rs_p1 = mkrs(ws + WS_P1 + (size_t)kvh * 16 * P1_STRIDE * 8, 16 * P1_STRIDE * 8);
    {
      const float* red = ldsf + L_STASH + L_RED;
      float M = -INFINITY;
#pragma unroll
      for (int k = 0; k < NSW; ++k) M = fmaxf(M, red[k * NE * 1024 + 256 + h * 2]);
      float o = 0.f, L = 0.f;
#pragma unroll
      for (int k = 0; k < NSW; ++k) {
        const float f = __builtin_amdgcn_exp2f((red[k * NE * 1024 + 256 + h * 2] - M) * LOG2E);
        o = fmaf(f, red[k * NE * 1024 + tid], o); L = fmaf(f, red[k * NE * 1024 + 256 + h * 2 + 1], L);
      }
      __builtin_amdgcn_raw_buffer_store_b64(u32x2{__float_as_uint(o), tag}, rs_p1, tid * 8, ii * (P1_STRIDE * 8), AUX_SC1);
      __builtin_amdgcn_raw_buffer_store_b64(u32x2{__float_as_uint((lane & 1) ? L : M), tag}, rs_p1, (tid & 127) < 2 ? (256 + h * 2 + lane) * 8 : 0x7fff0000, ii * (P1_STRIDE * 8), AUX_SC1);
    }
    if (w == 0) STAMP(6);
    if (!is_wo && !is_dn) {
      if (is_red) { final_rows(w + 1); __syncthreads(); }
      return;
    }
    SB;
    u32x4 wv[16];
    {
      const int rw = is_wo ? 8 : 16;
      const u16* wb = is_wo ? wo + (size_t)kvh * 256 * DM : wd + (size_t)drb * 512 * DM;
      const __amdgpu_buffer_rsrc_t rs_a = mkrs(wb, is_wo ? 256 * DM * 2 : (is_dn ? 512 * DM * 2 : 0)), rs_b = mkrs(wb, is_dn ? 512 * DM * 2 : 0);
      const int vo = (lane >> 3) * rw * (DM * 2) + (lane & 7) * 16, so = 8 * w * rw * (DM * 2) + slice * 128;
#pragma unroll
      for (int k = 0; k < 16; ++k) wv[k] = bld<AUX_NT>(k < 8 ? rs_a : rs_b, vo, so + k * (DM * 2));
      SB;
    }
    u32* vec = lds + L_VEC;
    if (is_wo) {
      u32x4 av[8]; u32x2 ml;
      for (u32 spins = 0;; ++spins) {
        asm volatile("" ::: "memory");
#pragma unroll
        for (int k = 0; k < 8; ++k) av[k] = bld<AUX_SC1>(rs_p1, hf * (8 * P1_STRIDE * 8) + w * 512 + l5 * 16, k * (P1_STRIDE * 8));
        ml = __builtin_amdgcn_raw_buffer_load_b64(rs_p1, ((hf * 8 + (c >> 1)) * P1_STRIDE + 256 + h * 2 + (c & 1)) * 8, 0, AUX_SC1);
        bool ok = ml[1] == tag;
#pragma unroll
        for (int k = 0; k < 8; ++k) ok &= av[k][1] == tag && av[k][3] == tag;
        if (__all(ok) || spins > SPIN_CAP) break;
        __builtin_amdgcn_s_sleep(1);
      }
      if (w == 0) STAMP(7);
      const float mlv = __uint_as_float(ml[0]);
      const float mv = dpp<0xA0>(mlv);
      float M = fmaxf(mv, dpp<DPP_XOR2>(mv)); M = fmaxf(M, dpp<DPP_ROR4>(M)); M = fmaxf(M, dpp<DPP_ROR8>(M));
      const float f = __builtin_amdgcn_exp2f((mv - M) * LOG2E);
      float L = f * dpp<0xF5>(mlv);
      L += dpp<DPP_XOR2>(L); L += dpp<DPP_ROR4>(L); L += dpp<DPP_ROR8>(L);
      float oa = 0.f, ob = 0.f;
#define HOP1(K) { const float f_ = dpp<DPP_BC + 2 * (K)>(f); oa = fmaf(__uint_as_float(av[K][0]), f_, oa); ob = fmaf(__uint_as_float(av[K][2]), f_, ob); }
      HOP1(0) HOP1(1) HOP1(2) HOP1(3) HOP1(4) HOP1(5) HOP1(6) HOP1(7)
      const float rl = 1.0f / L;
      vec[w * 128 + hf * 64 + (l5 >> 2) * 8 + (l5 & 3)] = f2bf(oa * rl) | (f2bf(ob * rl) << 16);
      __builtin_amdgcn_fence(__ATOMIC_RELEASE, "wavefront"); __builtin_amdgcn_wave_barrier(); __builtin_amdgcn_fence(__ATOMIC_ACQUIRE, "wavefront");
    }
    {
      float a0[8], a1[8];
#pragma unroll
      for (int k = 0; k < 8; ++k) { a0[k] = 0.f; a1[k] = 0.f; }
      const u32* vp = vec + w * 128 + (lane >> 3) * 8;
      const u32x4 o0 = *reinterpret_cast<const u32x4*>(vp), o1 = *reinterpret_cast<const u32x4*>(vp + 64);
#pragma unroll
      for (int k = 0; k < 4; ++k) pair_mac2(wv[2 * k], wv[2 * k + 1], o0[k], o1[k], a0, a1);
      if (is_dn) {
        const u32x4 o2 = *reinterpret_cast<const u32x4*>(vp + 4), o3 = *reinterpret_cast<const u32x4*>(vp + 68);
#pragma unroll
        for (int k = 0; k < 4; ++k) pair_mac2(wv[8 + 2 * k], wv[9 + 2 * k], o2[k], o3[k], a0, a1);
      }
      const float s0 = rs8_hi(a0, lane), s1 = rs8_hi(a1, lane);
      const int col = (lane & 7) * 8 + (lane >> 3);
      float* r2 = ldsf + L_R2;
      r2[w * 128 + col] = s0; r2[w * 128 + 64 + col] = s1;
      __syncthreads();
      if (w == 0) {
        const float2 u0 = *reinterpret_cast<const float2*>(r2 + lane * 2), u1 = *reinterpret_cast<const float2*>(r2 + 128 + lane * 2);
        const float2 u2 = *reinterpret_cast<const float2*>(r2 + 256 + lane * 2), u3 = *reinterpret_cast<const float2*>(r2 + 384 + lane * 2);
        __builtin_amdgcn_raw_buffer_store_b128(u32x4{__float_as_uint((u0.x + u1.x) + (u2.x + u3.x)), tag, __float_as_uint((u0.y + u1.y) + (u2.y + u3.y)), tag},
                                               mkrs(ws + WS_P2, 9 * 25 * 1024), lane * 16, (slice * 25 + (is_wo ? kvh : 16 + drb)) * 1024, AUX_SC1);
      }
    }
    return;
  }

  {
    const u32 tag = __builtin_amdgcn_readfirstlane(lds[L_FLAG]);
    float a0[8], a1[8];
    STAMP(11);

    if (task >= 0) {
      const int rr = lane >> 3, cc = lane & 7;
      const int vo = rr * 72 * (FF * 2) + cc * 16;
      const u32* xp = lds + L_XP + rr * 36;
      u32x4 A[8], Bf[8];
      float c0[8], c1[8];
      const __amdgpu_buffer_rsrc_t rs_g = mkrs(wg, DM * FF * 2), rs_u = mkrs(wu, DM * FF * 2);
#define FISSUE(BUF, RS, KB) { const int so_ = (KB) < 9 ? task * 128 + (KB) * 8 * (FF * 2) : DM * FF * 2; \
      _Pragma("unroll") for (int j_ = 0; j_ < 8; ++j_) BUF[j_] = bld<AUX_NT>(RS, vo, so_ + j_ * (FF * 2)); } SB
#define MAC4(BUF, X0, X1, C0, C1) { _Pragma("unroll") for (int j_ = 0; j_ < 4; ++j_) { pair_mac2(BUF[2 * j_], BUF[2 * j_ + 1], X0[j_], X1[j_], C0, C1); pin8(C0); pin8(C1); SB; } }
#define FZERO(C0, C1) _Pragma("unroll") for (int k_ = 0; k_ < 8; ++k_) { C0[k_] = 0.f; C1[k_] = 0.f; }
      int tf = TF0;
#define FPACE() { const u32 ta_ = tstart + ((u32)tf >> 8); while ((int)((u32)__builtin_amdgcn_s_memrealtime() - ta_) < 0) __builtin_amdgcn_s_sleep(8); tf += TFS; }
      FPACE(); FISSUE(A, rs_g, 0); FPACE(); FISSUE(Bf, rs_u, 0);
      FZERO(a0, a1); FZERO(c0, c1);
#pragma unroll 1
      for (int k = 0; k < 9; ++k) {
        const u32x4 x0 = *reinterpret_cast<const u32x4*>(xp + 4 * k), x1 = *reinterpret_cast<const u32x4*>(xp + 288 + 4 * k);
        MAC4(A, x0, x1, a0, a1); if (k < 8) { FPACE(); } FISSUE(A, rs_g, k + 1);
        MAC4(Bf, x0, x1, c0, c1); if (k < 8) { FPACE(); } FISSUE(Bf, rs_u, k + 1);
      }
      {
        const float gs0 = rs8_hi(a0, lane), gs1 = rs8_hi(a1, lane), us0 = rs8_hi(c0, lane), us1 = rs8_hi(c1, lane);
        const int col = cc * 8 + rr;
        const float g0 = rbf(gs0), g1 = rbf(gs1);
        const float e0 = rbf(0.5f * g0 * (1.0f + tanhf(0.7978845608028654f * (g0 + 0.044715f * g0 * g0 * g0))));
        const float e1 = rbf(0.5f * g1 * (1.0f + tanhf(0.7978845608028654f * (g1 + 0.044715f * g1 * g1 * g1))));
        reinterpret_cast<u16*>(lds + L_H)[col] = (u16)f2bf(e0 * rbf(us0));
        reinterpret_cast<u16*>(lds + L_H)[64 + col] = (u16)f2bf(e1 * rbf(us1));
      }
      __builtin_amdgcn_fence(__ATOMIC_RELEASE, "wavefront"); __builtin_amdgcn_wave_barrier(); __builtin_amdgcn_fence(__ATOMIC_ACQUIRE, "wavefront");
      st_gran((gu64*)(ws + WS_HG) + task * 64 + lane, tag, lds[L_H + lane]);
      STAMP(12);
    }
    __builtin_amdgcn_s_setprio(0);
    if (is_dn) {
      { const u32 ta_ = tstart + (FEND + 3000) / 10; while ((int)((u32)__builtin_amdgcn_s_memrealtime() - ta_) < 0) __builtin_amdgcn_s_sleep(32); }
      const __amdgpu_buffer_rsrc_t rs_h = mkrs(ws + WS_HG + (size_t)drb * 4096, 4096);
      u32x4 v[4];
      for (u32 spins = 0;; ++spins) {
        asm volatile("" ::: "memory");
        bool ok = true;
#pragma unroll
        for (int k = 0; k < 4; ++k) v[k] = bld<AUX_SC1>(rs_h, lane * 16, k * 1024);
#pragma unroll
        for (int k = 0; k < 4; ++k) ok &= v[k][1] == tag && v[k][3] == tag;
        if (__all(ok) || spins > SPIN_CAP) break;
        __builtin_amdgcn_s_sleep(16);
      }
#pragma unroll
      for (int k = 0; k < 4; ++k) *reinterpret_cast<u32x2*>(lds + L_VEC + k * 128 + (l5 >> 4) * 64 + hf * 32 + (l5 & 15) * 2) = u32x2{v[k][0], v[k][2]};
      STAMP(13);
    }
    u32 xpair = 0;
    if (is_red) xpair = reinterpret_cast<const u32*>(x)[hf * 288 + slice * 32 + l5];
    __syncthreads();
    if (is_red) {
      final_rows(0);
      __syncthreads();
      f32x4 t = *reinterpret_cast<const f32x4*>(ldsf + L_SC + lane * 4);
#pragma unroll
      for (int k = 1; k < 5; ++k) t += *reinterpret_cast<const f32x4*>(ldsf + L_SC + (k * 64 + lane) * 4);
      const float y0 = t[0], y1 = t[1], f0 = t[2], f1 = t[3];
      const u32 o0 = f2bf(rbf(bf2f(xpair & 0xffffu) + rbf(y0)) + rbf(f0)), o1 = f2bf(rbf(bf2f(xpair >> 16) + rbf(y1)) + rbf(f1));
      reinterpret_cast<u32*>(out)[hf * 288 + slice * 32 + l5] = o0 | (o1 << 16);
      if (slice == 0 && lane == 0) __hip_atomic_store((gu32*)(ws + WS_EPOCH), tag, RLX_AGENT);
      STAMP(14);
    }
  }
}

extern "C" void kernel_launch(void* const* d_in, const int* in_sizes, int n_in, void* d_out, int out_size, void* d_ws, size_t ws_size,
                              hipStream_t stream) {
  (void)in_sizes; (void)n_in; (void)out_size; (void)ws_size;
  hipLaunchKernelGGL(fused_decode_kernel, dim3(256), dim3(NTHR), 0, stream, (const u16*)d_in[0], (const u16*)d_in[1], (const u16*)d_in[2],
                     (const u16*)d_in[3], (const u16*)d_in[4], (const u16*)d_in[6], (const u16*)d_in[7], (const u16*)d_in[8], (u16*)d_out,
                     (unsigned char*)d_ws);
}
```

```cpp
#include <hip/hip_runtime.h>
#include <hip/hip_bf16.h>
#include <stdint.h>

typedef unsigned u32;
typedef unsigned short u16;
typedef unsigned long long u64;
typedef u32 u32x4 __attribute__((ext_vector_type(4)));
typedef u32 u32x2 __attribute__((ext_vector_type(2)));
typedef float f32x2 __attribute__((ext_vector_type(2)));
typedef float f32x4 __attribute__((ext_vector_type(4)));
typedef __bf16 bf16x2 __attribute__((ext_vector_type(2)));
#define GAS __attribute__((address_space(1)))
#define LAS __attribute__((address_space(3)))
typedef GAS u64 gu64;
typedef GAS u32 gu32;
#define RLX_AGENT __ATOMIC_RELAXED, __HIP_MEMORY_SCOPE_AGENT
#define SB __builtin_amdgcn_sched_barrier(0)

constexpr int DM = 576, SK = 32768, DH = 128, FF = 4608, NQ = 4096;
constexpr int NSW = 4, NTHR = (NSW + 1) * 64, U = 4;
constexpr int TB = U * 1024;
constexpr int NT = SK / 16;
constexpr int RT = NT / 8;
constexpr int TAU = 555;
constexpr int EQ = 1300;
constexpr int FEND = 62000;
constexpr int NX = 4, NE = 8;
constexpr int NCTR = 4;
constexpr int NS = 60;
constexpr int NSF = NS - 4;
constexpr int PEX = (NS - NSF) * NSW;
constexpr int PCOM = (RT - NS * NSW) * 8;
static_assert(PEX == 16 && NE % 2 == 0 && NSF >= 4 * (NE + NX) + 6 && NX <= NE, "");
constexpr int TK = TAU * 256 / 10, TKF = TK * (2 * NS - 12) / (2 * NSF - 12);
constexpr int TF0 = 8000 * 256 / 10, TFS = (FEND - 8000) * 256 / 10 / 17;
constexpr int NFT = 72;
constexpr int AUX_NT = 2, AUX_SC1 = 16;
constexpr float LOG2E = 1.4426950408889634f;
constexpr u32 SPIN_CAP = 1u << 20;

constexpr size_t WS_EPOCH = 0;
constexpr size_t WS_CTR = 4096;
constexpr size_t WS_QG = WS_CTR + 128 * 256;
constexpr size_t WS_P1 = WS_QG + 2 * 64 * 4 * 64 * 8;
constexpr int P1_STRIDE = 272;
constexpr size_t WS_P2 = WS_P1 + (size_t)256 * P1_STRIDE * 8;
constexpr size_t WS_HG = WS_P2 + (size_t)9 * 25 * 128 * 8;
constexpr size_t WS_END = WS_HG + (size_t)NFT * 64 * 8;

__device__ __forceinline__ float bf2f(u32 lo16) { return __uint_as_float(lo16 << 16); }
__device__ __forceinline__ float rbf(float f) { return (float)(__bf16)f; }
__device__ __forceinline__ u32 f2bf(float f) { const __bf16 h = (__bf16)f; return (u32)__builtin_bit_cast(u16, h); }
__device__ __forceinline__ float dot2(u32 a, u32 b, float c) {
  return __builtin_amdgcn_fdot2_f32_bf16(__builtin_bit_cast(bf16x2, a), __builtin_bit_cast(bf16x2, b), c, false);
}
template <int CTRL> __device__ __forceinline__ float dpp(float x) {
  return __int_as_float(__builtin_amdgcn_update_dpp(0, __float_as_int(x), CTRL, 0xf, 0xf, true));
}
constexpr int DPP_XOR1 = 0xB1, DPP_XOR2 = 0x4E, DPP_HMIR = 0x141, DPP_ROR8 = 0x128, DPP_ROR4 = 0x124, DPP_BC = 0x150;
__device__ __forceinline__ void swap32(float& a, float& b) {
  auto r = __builtin_amdgcn_permlane32_swap(__float_as_uint(a), __float_as_uint(b), false, false);
  a = __uint_as_float(r[0]); b = __uint_as_float(r[1]);
}
__device__ __forceinline__ void swap16(float& a, float& b) {
  auto r = __builtin_amdgcn_permlane16_swap(__float_as_uint(a), __float_as_uint(b), false, false);
  a = __uint_as_float(r[0]); b = __uint_as_float(r[1]);
}
__device__ __forceinline__ float half_sum(float x) {
  x += dpp<DPP_XOR1>(x); x += dpp<DPP_XOR2>(x); x += dpp<DPP_ROR4>(x); x += dpp<DPP_ROR8>(x);
  float a = x, b = x; swap16(a, b); return a + b;
}
__device__ __forceinline__ __amdgpu_buffer_rsrc_t mkrs(const void* p, u32 bytes) {
  return __builtin_amdgcn_make_buffer_rsrc(const_cast<void*>(p), 0, bytes, 0x00020000);
}
template <int AUX> __device__ __forceinline__ u32x4 bld(__amdgpu_buffer_rsrc_t rs, int voff, int soff) {
  return __builtin_amdgcn_raw_buffer_load_b128(rs, voff, soff, AUX);
}
__device__ __forceinline__ void pair_mac2(const u32x4& a, const u32x4& b, u32 xp0, u32 xp1, float* acc0, float* acc1) {
#pragma unroll
  for (int d = 0; d < 4; ++d) {
    const u32 lo = __builtin_amdgcn_perm(b[d], a[d], 0x05040100u), hi = __builtin_amdgcn_perm(b[d], a[d], 0x07060302u);
    acc0[2 * d] = dot2(lo, xp0, acc0[2 * d]); acc0[2 * d + 1] = dot2(hi, xp0, acc0[2 * d + 1]);
    acc1[2 * d] = dot2(lo, xp1, acc1[2 * d]); acc1[2 * d + 1] = dot2(hi, xp1, acc1[2 * d + 1]);
  }
}
__device__ __forceinline__ float rs8_hi(float* v, int lane) {
#pragma unroll
  for (int k = 0; k < 4; ++k) { swap32(v[k], v[k + 4]); v[k] += v[k + 4]; }
#pragma unroll
  for (int k = 0; k < 2; ++k) { swap16(v[k], v[k + 2]); v[k] += v[k + 2]; }
  const bool up = lane & 8;
  const float keep = up ? v[1] : v[0], send = up ? v[0] : v[1];
  return keep + dpp<DPP_ROR8>(send);
}
__device__ __forceinline__ void pin8(float* a) {
  asm volatile("" : "+v"(a[0]), "+v"(a[1]), "+v"(a[2]), "+v"(a[3]), "+v"(a[4]), "+v"(a[5]), "+v"(a[6]), "+v"(a[7]));
}
__device__ __forceinline__ void st_gran(gu64* g, u32 tag, u32 v) { __hip_atomic_store(g, ((u64)tag << 32) | (u64)v, RLX_AGENT); }

constexpr int L_XP = 0;
constexpr int L_Q = L_XP + 576;
constexpr int L_FLAG = L_Q + 128;
constexpr int L_H = L_FLAG + 4;
constexpr int L_VEC = L_H + 64;
constexpr int L_SC = L_VEC + 512;
constexpr int L_STASH = L_SC + NSW * 64 * 16;
constexpr int L_R2 = L_STASH;
constexpr int L_RED = 1024;
constexpr int L_TOTAL = L_STASH + NSW * NE * 1024;

__global__ void __launch_bounds__(NTHR, 2)
fused_decode_kernel(const u16* __restrict__ x, const u16* __restrict__ kc, const u16* __restrict__ vc, const u16* __restrict__ wq,
                    const u16* __restrict__ wo, const u16* __restrict__ wg, const u16* __restrict__ wu, const u16* __restrict__ wd,
                    u16* __restrict__ out, unsigned char* __restrict__ ws) {
  __shared__ __attribute__((aligned(16))) u32 lds[L_TOTAL];
  const u32 tstart = (u32)__builtin_amdgcn_s_memrealtime();
  asm volatile("" :: "s"(x), "s"(kc), "s"(vc), "s"(wq), "s"(ws), "s"(tstart));
#define PACE(T) { const u32 ta_ = tstart + ((u32)(T) >> 8); while ((int)((u32)__builtin_amdgcn_s_memrealtime() - ta_) < 0) __builtin_amdgcn_s_sleep(1); }
  float* ldsf = reinterpret_cast<float*>(lds);
  const int tid = (int)threadIdx.x - 64, lane = threadIdx.x & 63;
  const int w = __builtin_amdgcn_readfirstlane((int)(threadIdx.x >> 6)) - 1;
  const int bid = blockIdx.x;
  const int s = bid & 7, g = bid >> 3, b = g >> 4, kvh = g & 15;
  const int ii = b * 8 + s;
  const int ee = kvh * 7 + ii - 9;
  const bool is_wo = ii < 9, is_dn = !is_wo && ee < 81, is_red = !is_wo && ee >= 81 && ee < 90;
  const int drb = ee / 9, slice = is_wo ? ii : ee - drb * 9;

  const u16* kbase = kc + (size_t)g * SK * DH;
  const u16* vbase = vc + (size_t)g * SK * DH;
  const int kv_vo = lane * 16, cvo = lane * 4;
  const int t0 = s * RT + w;
  const int hf = lane >> 5, l5 = lane & 31;
  const int sub = (w + s) & (NCTR - 1);
  const int qcg = g + 32 * (s >> 2), qkq = s & 3;
  u32x4 EX[NX][U];
#define ISSUE0(BUF, T) { const __amdgpu_buffer_rsrc_t rs_ = mkrs(kbase, SK * DH * 2); const int so_ = (T) * TB; \
    _Pragma("unroll") for (int i_ = 0; i_ < U; ++i_) BUF[i_] = bld<AUX_NT>(rs_, kv_vo + i_ * 1024, so_); SB; }
#define ISSUE(BUF, CL, BASE, T, REAL) { CL = __builtin_amdgcn_raw_ptr_buffer_atomic_add_i32(1, mkrs(ws + WS_CTR + g * 1024 + sub * 256, (REAL) ? 4 : 0), cvo, 0, 0); SB; \
    const __amdgpu_buffer_rsrc_t rs_ = mkrs(BASE, SK * DH * 2); const int so_ = (T) * TB; \
    _Pragma("unroll") for (int i_ = 0; i_ < U; ++i_) BUF[i_] = bld<AUX_NT>(rs_, kv_vo + i_ * 1024, so_); SB; }
  if (w >= 0) { ISSUE0(EX[0], t0); ISSUE0(EX[1], t0 + NSW); }
  asm volatile("; ROLE SPLIT 0" ::: "memory");
  volatile LAS u32* lflag = (volatile LAS u32*)(lds + L_FLAG);
  if (threadIdx.x == 0) lflag[1] = 0u;
  asm volatile("; ROLE SPLIT 1" ::: "memory");
  if (w < 0) {
    __builtin_amdgcn_s_setprio(3);
    u32x4 wqv[18];
    u32 xr[9];
    {
      const __amdgpu_buffer_rsrc_t rs_x = mkrs(x, 2 * DM * 2);
      const u32x4 xa = bld<0>(rs_x, lane * 36, 0), xb = bld<0>(rs_x, lane * 36 + 16, 0);
      xr[8] = __builtin_amdgcn_raw_buffer_load_b32(rs_x, lane * 36 + 32, 0, 0);
#pragma unroll
      for (int j = 0; j < 4; ++j) { xr[j] = xa[j]; xr[4 + j] = xb[j]; }
    }
    const u32 epoch_v = __hip_atomic_load((gu32*)(ws + WS_EPOCH), RLX_AGENT);
    const __amdgpu_buffer_rsrc_t rs_wq = mkrs(wq, DM * NQ * 2);
    const int vo = (lane >> 3) * 18 * (NQ * 2) + (lane & 7) * 16;
#pragma unroll
    for (int j = 0; j < 18; ++j) wqv[j] = bld<AUX_NT>(rs_wq, vo, qcg * 128 + (qkq * 144 + j) * (NQ * 2));
    SB;
    {
      float v[18]; float sum = 0.f;
#pragma unroll
      for (int j = 0; j < 9; ++j) { v[2 * j] = bf2f(xr[j] & 0xffffu); v[2 * j + 1] = bf2f(xr[j] >> 16); sum += v[2 * j] + v[2 * j + 1]; }
      const float mean = half_sum(sum) * (1.0f / DM);
      float sq = 0.f;
#pragma unroll
      for (int j = 0; j < 18; ++j) { const float d = v[j] - mean; sq = fmaf(d, d, sq); }
      const float var = half_sum(sq) * (1.0f / DM);
      const float mb = rbf(mean), rsd = 1.0f / rbf(sqrtf(rbf(rbf(var) + 1e-5f)));
#pragma unroll
      for (int j = 0; j < 9; ++j)
        lds[L_XP + lane * 9 + j] = f2bf(rbf(v[2 * j] - mb) * rsd) | (f2bf(rbf(v[2 * j + 1] - mb) * rsd) << 16);
    }
    __builtin_amdgcn_fence(__ATOMIC_RELEASE, "wavefront"); __builtin_amdgcn_wave_barrier(); __builtin_amdgcn_fence(__ATOMIC_ACQUIRE, "wavefront");
    const u32 tag = __builtin_amdgcn_readfirstlane(epoch_v) + 0x9E3779B9u;
    float a0[8], a1[8];
#pragma unroll
    for (int k = 0; k < 8; ++k) { a0[k] = 0.f; a1[k] = 0.f; }
    {
      const u32* xp = lds + L_XP + qkq * 72 + (lane >> 3) * 9;
#pragma unroll
      for (int j = 0; j < 9; ++j) pair_mac2(wqv[2 * j], wqv[2 * j + 1], xp[j], xp[288 + j], a0, a1);
      const float s0 = rs8_hi(a0, lane), s1 = rs8_hi(a1, lane);
      gu64* qd = (gu64*)(ws + WS_QG) + (qcg * 4 + qkq) * 64 + (lane & 7) * 8 + (lane >> 3);
      st_gran(qd, tag, __float_as_uint(s0)); st_gran(qd + 16384, tag, __float_as_uint(s1));
    }
    if (s == 0 && lane < NCTR) st_gran((gu64*)(ws + WS_CTR + g * 1024 + lane * 256), tag, 0u);
    __syncthreads();
    {
      const unsigned char* qsrc = ws + WS_QG + (size_t)(b * 64 + kvh * 4) * 2048;
      __builtin_amdgcn_s_sleep(20);
      const __amdgpu_buffer_rsrc_t rs_q = mkrs(qsrc, 8192);
      u32x4 v[8];
      for (u32 spins = 0;; ++spins) {
        asm volatile("" ::: "memory");
#pragma unroll
        for (int k = 0; k < 8; ++k) v[k] = bld<AUX_SC1>(rs_q, hf * 2048 + l5 * 16, (k >> 2) * 4096 + (k & 3) * 512);
        bool ok = (u32)(__hip_atomic_load((const gu64*)(ws + WS_CTR + g * 1024 + (lane & (NCTR - 1)) * 256), RLX_AGENT) >> 32) == tag;
#pragma unroll
        for (int k = 0; k < 8; ++k) ok &= v[k][1] == tag && v[k][3] == tag;
        if (__all(ok) || spins > SPIN_CAP) break;
        __builtin_amdgcn_s_sleep(1);
      }
#pragma unroll
      for (int k = 0; k < 2; ++k) {
        const u32x4* u = v + 4 * k;
        const float qa = (__uint_as_float(u[0][0]) + __uint_as_float(u[1][0])) + (__uint_as_float(u[2][0]) + __uint_as_float(u[3][0]));
        const float qb = (__uint_as_float(u[0][2]) + __uint_as_float(u[1][2])) + (__uint_as_float(u[2][2]) + __uint_as_float(u[3][2]));
        lds[L_Q + k * 64 + lane] = f2bf(qa) | (f2bf(qb) << 16);
      }
      lds[L_FLAG] = tag;
      __builtin_amdgcn_fence(__ATOMIC_RELEASE, "workgroup");
      lflag[1] = 1u;
    }
  }
  asm volatile("; ROLE SPLIT 2" ::: "memory");

  const int task = (s & 3) == (g & 3) ? g * 2 + (s >> 2) : ((g >> 2) == s && (g & 3) == ((s + 1) & 3) ? 64 + s : -1);
  const int r = lane >> 4, c = lane & 15;
  auto final_rows = [&](int j) {
    const u32 tag = __builtin_amdgcn_readfirstlane(lds[L_FLAG]);
    const __amdgpu_buffer_rsrc_t rs_y = mkrs(ws + WS_P2 + (size_t)(slice * 25 + j * 5) * 1024, 5 * 1024);
    u32x4 v[5];
    for (u32 spins = 0;; ++spins) {
      asm volatile("" ::: "memory");
      bool ok = true;
#pragma unroll
      for (int k = 0; k < 5; ++k) v[k] = bld<AUX_SC1>(rs_y, lane * 16, k * 1024);
#pragma unroll
      for (int k = 0; k < 5; ++k) ok &= v[k][1] == tag && v[k][3] == tag;
      if (__all(ok) || spins > SPIN_CAP) break;
      __builtin_amdgcn_s_sleep(1);
    }
    const float a0 = __uint_as_float(v[0][0]), a1 = __uint_as_float(v[0][2]);
    const float b0 = (__uint_as_float(v[1][0]) + __uint_as_float(v[2][0])) + (__uint_as_float(v[3][0]) + __uint_as_float(v[4][0]));
    const float b1 = (__uint_as_float(v[1][2]) + __uint_as_float(v[2][2])) + (__uint_as_float(v[3][2]) + __uint_as_float(v[4][2]));
    *reinterpret_cast<f32x4*>(ldsf + L_SC + (j * 64 + lane) * 4) = j < 3 ? f32x4{a0 + b0, a1 + b1, 0.f, 0.f} : (j == 3 ? f32x4{a0, a1, b0, b1} : f32x4{0.f, 0.f, a0 + b0, a1 + b1});
  };
  if (w >= 0) {
    u32x4 B0[U], B1[U], B2[U];
    int c0, c1, c2;
    float acc0[8], acc1[8];
#pragma unroll
    for (int k = 0; k < 8; ++k) { acc0[k] = 0.f; acc1[k] = 0.f; }
    float mrun = -INFINITY, lrun = 0.f;
    u32* mylds = lds + L_STASH + w * NE * 1024;
#define WAITV(N) __builtin_amdgcn_s_waitcnt(0x0f70 | ((N) & 15) | (((N) >> 4) << 14)); SB
    int ne = 0, tnext = TK + EQ * 256 / 10;
#pragma unroll
    for (int e = 2; e < NX; ++e) { WAITV(4); PACE(tnext); tnext += TK; ISSUE0(EX[e], t0 + e * NSW); }
    WAITV(4); PACE(tnext); tnext += TK; ISSUE(B0, c0, kbase, t0 + NX * NSW, false);
    WAITV(5); PACE(tnext); tnext += TK; ISSUE(B1, c1, kbase, t0 + (NX + 1) * NSW, false);
    __syncthreads();
    u32x4* stash = reinterpret_cast<u32x4*>(mylds) + lane;
#pragma unroll 1
    while (ne < NE && lflag[1] == 0u) {
      WAITV(5);
#pragma unroll
      for (int i = 0; i < U; ++i) stash[ne * 256 + i * 64] = B0[i];
      SB; PACE(tnext); tnext += TK; ISSUE(B0, c0, kbase, t0 + (NX + ne + 2) * NSW, false);
      WAITV(5);
#pragma unroll
      for (int i = 0; i < U; ++i) stash[ne * 256 + 256 + i * 64] = B1[i];
      SB; PACE(tnext); tnext += TK; ISSUE(B1, c1, kbase, t0 + (NX + ne + 3) * NSW, false);
      ne += 2;
    }
    while (lflag[1] == 0u) __builtin_amdgcn_s_sleep(1);
    __builtin_amdgcn_fence(__ATOMIC_ACQUIRE, "workgroup");
    const u32x4 q0 = *reinterpret_cast<const u32x4*>(lds + L_Q + c * 4);
    const u32x4 q1 = *reinterpret_cast<const u32x4*>(lds + L_Q + 64 + c * 4);
    u16* scl = reinterpret_cast<u16*>(lds + L_SC + w * 64 * 16) + (lane >> 1);

    float mseen = -3.0e38f;
    auto kstep = [&](const u32x4* Kt, int slot) {
      float xv[8];
#pragma unroll
      for (int i = 0; i < U; ++i) {
        float d0 = 0.f, d1 = 0.f;
#pragma unroll
        for (int k = 0; k < 4; ++k) { d0 = dot2(Kt[i][k], q0[k], d0); d1 = dot2(Kt[i][k], q1[k], d1); }
        xv[2 * i] = d0; xv[2 * i + 1] = d1;
      }
#pragma unroll
      for (int k = 0; k < 4; ++k) { const bool up = c & 8; const float keep = up ? xv[k + 4] : xv[k], send = up ? xv[k] : xv[k + 4]; xv[k] = keep + dpp<DPP_ROR8>(send); }
#pragma unroll
      for (int k = 0; k < 2; ++k) { const bool up = c & 4; const float keep = up ? xv[k + 2] : xv[k], send = up ? xv[k] : xv[k + 2]; xv[k] = keep + dpp<DPP_HMIR>(send); }
      { const bool up = c & 2; const float keep = up ? xv[1] : xv[0], send = up ? xv[0] : xv[1]; xv[0] = keep + dpp<DPP_XOR2>(send); }
      const float sc = xv[0] + dpp<DPP_XOR1>(xv[0]);
      const u32 sb = f2bf(rbf(sc) * 0.08838834764831845f);
      scl[slot * 32] = (u16)sb;
      mseen = fmaxf(mseen, bf2f(sb));
    };
    auto pv1 = [&](const u32x4& Vt, float p0, float p1) {
#pragma unroll
      for (int k = 0; k < 4; ++k) {
        const f32x2 v = {__uint_as_float(Vt[k] << 16), __uint_as_float(Vt[k] & 0xffff0000u)};
        const f32x2 r0 = __builtin_elementwise_fma(v, f32x2{p0, p0}, f32x2{acc0[2 * k], acc0[2 * k + 1]});
        const f32x2 r1 = __builtin_elementwise_fma(v, f32x2{p1, p1}, f32x2{acc1[2 * k], acc1[2 * k + 1]});
        acc0[2 * k] = r0[0]; acc0[2 * k + 1] = r0[1]; acc1[2 * k] = r1[0]; acc1[2 * k + 1] = r1[1];
      }
    };
    auto vstep = [&](const u32x4* Vt, int slot) {
      const float sc = bf2f(scl[slot * 32]);
      if (__any(mseen > mrun)) {
        float mn = fmaxf(mseen, dpp<DPP_ROR4>(mseen)); mn = fmaxf(mn, dpp<DPP_ROR8>(mn)); mn = fmaxf(mn, mrun);
        const float al = __builtin_amdgcn_exp2f((mrun - mn) * LOG2E);
        mrun = mn; lrun *= al;
        const float al0 = dpp<DPP_BC + 0>(al), al1 = dpp<DPP_BC + 2>(al);
#pragma unroll
        for (int k = 0; k < 8; ++k) { acc0[k] *= al0; acc1[k] *= al1; }
      }
      const float p = __builtin_amdgcn_exp2f((sc - mrun) * LOG2E);
      lrun += p;
      pv1(Vt[0], dpp<DPP_BC + 0>(p), dpp<DPP_BC + 2>(p));
      pv1(Vt[1], dpp<DPP_BC + 4>(p), dpp<DPP_BC + 6>(p));
      pv1(Vt[2], dpp<DPP_BC + 8>(p), dpp<DPP_BC + 10>(p));
      pv1(Vt[3], dpp<DPP_BC + 12>(p), dpp<DPP_BC + 14>(p));
      pin8(acc0); pin8(acc1);
    };
    const int n = task >= 0 ? NSF : NS, tkb = task >= 0 ? TKF : TK;
    const int g3 = g & 3, nx = (g3 == (((g >> 2) + 1) & 3) ? 3 : 2) * PEX;
    int it = NX + ne, iend = 0x7fffffff, ptile = 0, sbase = NX;
#define STEP(BUF, CL, NBUF, NCL) { \
      const int i2_ = it + 2 - 2 * n;                        \
      if (it + 2 < iend) { PACE(tnext); } tnext += it >= 10 && i2_ < 0 ? tkb : TK; \
      if (it < iend) { WAITV(5); \
        if (i2_ >= 0 && !(i2_ & 1) && ptile < NT) { const int pi_ = (__builtin_amdgcn_readfirstlane(CL) & 0xffff) * NCTR + sub, pc_ = pi_ - nx; \
          const int e_ = pi_ >> 4, reg_ = e_ == 2 ? (g >> 2) : g3 + 4 * e_; \
          ptile = pc_ < 0 ? reg_ * RT + NSF * NSW + (pi_ & 15) : (pc_ < PCOM ? (pc_ & 7) * RT + NS * NSW + (pc_ >> 3) : NT); \
          if (ptile >= NT) iend = it + 2; } } \
      const bool isv_ = i2_ >= 0 ? (i2_ & 1) : (i2_ >= -n); \
      const int tl_ = i2_ >= 0 ? ptile : t0 + (isv_ ? i2_ + n : i2_ + 2 * n) * NSW; \
      ISSUE(NBUF, NCL, isv_ ? vbase : kbase, tl_, i2_ >= -2 && !(i2_ & 1) && ptile < NT); \
      if (it < iend) { const int i0_ = i2_ - 2; \
        if (i0_ >= 0 ? (i0_ & 1) : (i0_ >= -n)) vstep(BUF, i0_ >= 0 ? 63 : i0_ + n); else kstep(BUF, i0_ >= 0 ? 63 : it); } \
      SB; ++it; }
    do {
      STEP(B0, c0, B2, c2);
      if (ne == 0 && sbase != 0) {
#pragma unroll
        for (int e = 0; e < NX; ++e)
#pragma unroll
          for (int i = 0; i < U; ++i) stash[e * 256 + i * 64] = EX[e][i];
        ne = NX; sbase = 0;
      }
      if (ne > 0) {
        --ne;
        u32x4 KE[U];
#pragma unroll
        for (int i = 0; i < U; ++i) KE[i] = stash[ne * 256 + i * 64];
        kstep(KE, sbase + ne); SB;
      }
      STEP(B1, c1, B0, c0);
      STEP(B2, c2, B1, c1);
    } while (it < iend);
    const u32 tag = __builtin_amdgcn_readfirstlane(lds[L_FLAG]);
    const int h = w >> 1;
    {
      float lr = lrun + dpp<DPP_ROR4>(lrun); lr += dpp<DPP_ROR8>(lr);
      float a = mrun, bb = mrun; swap16(a, bb); float mw = fmaxf(a, bb);
      a = mw; bb = mw; swap32(a, bb); mw = fmaxf(a, bb);
      const float f = __builtin_amdgcn_exp2f((mrun - mw) * LOG2E);
      lr *= f;
      a = lr; bb = lr; swap16(a, bb); lr = a + bb;
      a = lr; bb = lr; swap32(a, bb); lr = a + bb;
      const float f0 = dpp<DPP_BC + 0>(f), f1 = dpp<DPP_BC + 2>(f);
#pragma unroll
      for (int k = 0; k < 8; ++k) { acc0[k] *= f0; acc1[k] *= f1; }
#pragma unroll
      for (int k = 0; k < 8; ++k) { swap32(acc0[k], acc1[k]); acc0[k] += acc1[k]; }
#pragma unroll
      for (int k = 0; k < 4; ++k) { swap16(acc0[k], acc0[k + 4]); acc0[k] += acc0[k + 4]; }
      *reinterpret_cast<f32x4*>(mylds + L_RED + (r >> 1) * 128 + c * 8 + (r & 1) * 4) = f32x4{acc0[0], acc0[1], acc0[2], acc0[3]};
      if (lane == 0 || lane == 2) { mylds[L_RED + 256 + lane] = __float_as_uint(mw); mylds[L_RED + 256 + lane + 1] = __float_as_uint(lr); }
    }
    __syncthreads();
    const __amdgpu_buffer_rsrc_t rs_p1 = mkrs(ws + WS_P1 + (size_t)kvh * 16 * P1_STRIDE * 8, 16 * P1_STRIDE * 8);
    {
      const float* red = ldsf + L_STASH + L_RED;
      float M = -INFINITY;
#pragma unroll
      for (int k = 0; k < NSW; ++k) M = fmaxf(M, red[k * NE * 1024 + 256 + h * 2]);
      float o = 0.f, L = 0.f;
#pragma unroll
      for (int k = 0; k < NSW; ++k) {
        const float f = __builtin_amdgcn_exp2f((red[k * NE * 1024 + 256 + h * 2] - M) * LOG2E);
        o = fmaf(f, red[k * NE * 1024 + tid], o); L = fmaf(f, red[k * NE * 1024 + 256 + h * 2 + 1], L);
      }
      __builtin_amdgcn_raw_buffer_store_b64(u32x2{__float_as_uint(o), tag}, rs_p1, tid * 8, ii * (P1_STRIDE * 8), AUX_SC1);
      __builtin_amdgcn_raw_buffer_store_b64(u32x2{__float_as_uint((lane & 1) ? L : M), tag}, rs_p1, (tid & 127) < 2 ? (256 + h * 2 + lane) * 8 : 0x7fff0000, ii * (P1_STRIDE * 8), AUX_SC1);
    }
    if (!is_wo && !is_dn) {
      if (is_red) { final_rows(w + 1); __syncthreads(); }
      return;
    }
    SB;
    u32x4 wv[16];
    {
      const int rw = is_wo ? 8 : 16;
      const u16* wb = is_wo ? wo + (size_t)kvh * 256 * DM : wd + (size_t)drb * 512 * DM;
      const __amdgpu_buffer_rsrc_t rs_a = mkrs(wb, is_wo ? 256 * DM * 2 : (is_dn ? 512 * DM * 2 : 0)), rs_b = mkrs(wb, is_dn ? 512 * DM * 2 : 0);
      const int vo = (lane >> 3) * rw * (DM * 2) + (lane & 7) * 16, so = 8 * w * rw * (DM * 2) + slice * 128;
#pragma unroll
      for (int k = 0; k < 16; ++k) wv[k] = bld<AUX_NT>(k < 8 ? rs_a : rs_b, vo, so + k * (DM * 2));
      SB;
    }
    u32* vec = lds + L_VEC;
    if (is_wo) {
      u32x4 av[8]; u32x2 ml;
      for (u32 spins = 0;; ++spins) {
        asm volatile("" ::: "memory");
#pragma unroll
        for (int k = 0; k < 8; ++k) av[k] = bld<AUX_SC1>(rs_p1, hf * (8 * P1_STRIDE * 8) + w * 512 + l5 * 16, k * (P1_STRIDE * 8));
        ml = __builtin_amdgcn_raw_buffer_load_b64(rs_p1, ((hf * 8 + (c >> 1)) * P1_STRIDE + 256 + h * 2 + (c & 1)) * 8, 0, AUX_SC1);
        bool ok = ml[1] == tag;
#pragma unroll
        for (int k = 0; k < 8; ++k) ok &= av[k][1] == tag && av[k][3] == tag;
        if (__all(ok) || spins > SPIN_CAP) break;
        __builtin_amdgcn_s_sleep(1);
      }
      const float mlv = __uint_as_float(ml[0]);
      const float mv = dpp<0xA0>(mlv);
      float M = fmaxf(mv, dpp<DPP_XOR2>(mv)); M = fmaxf(M, dpp<DPP_ROR4>(M)); M = fmaxf(M, dpp<DPP_ROR8>(M));
      const float f = __builtin_amdgcn_exp2f((mv - M) * LOG2E);
      float L = f * dpp<0xF5>(mlv);
      L += dpp<DPP_XOR2>(L); L += dpp<DPP_ROR4>(L); L += dpp<DPP_ROR8>(L);
      float oa = 0.f, ob = 0.f;
#define HOP1(K) { const float f_ = dpp<DPP_BC + 2 * (K)>(f); oa = fmaf(__uint_as_float(av[K][0]), f_, oa); ob = fmaf(__uint_as_float(av[K][2]), f_, ob); }
      HOP1(0) HOP1(1) HOP1(2) HOP1(3) HOP1(4) HOP1(5) HOP1(6) HOP1(7)
      const float rl = 1.0f / L;
      vec[w * 128 + hf * 64 + (l5 >> 2) * 8 + (l5 & 3)] = f2bf(oa * rl) | (f2bf(ob * rl) << 16);
      __builtin_amdgcn_fence(__ATOMIC_RELEASE, "wavefront"); __builtin_amdgcn_wave_barrier(); __builtin_amdgcn_fence(__ATOMIC_ACQUIRE, "wavefront");
    }
    {
      float a0[8], a1[8];
#pragma unroll
      for (int k = 0; k < 8; ++k) { a0[k] = 0.f; a1[k] = 0.f; }
      const u32* vp = vec + w * 128 + (lane >> 3) * 8;
      const u32x4 o0 = *reinterpret_cast<const u32x4*>(vp), o1 = *reinterpret_cast<const u32x4*>(vp + 64);
#pragma unroll
      for (int k = 0; k < 4; ++k) pair_mac2(wv[2 * k], wv[2 * k + 1], o0[k], o1[k], a0, a1);
      if (is_dn) {
        const u32x4 o2 = *reinterpret_cast<const u32x4*>(vp + 4), o3 = *reinterpret_cast<const u32x4*>(vp + 68);
#pragma unroll
        for (int k = 0; k < 4; ++k) pair_mac2(wv[8 + 2 * k], wv[9 + 2 * k], o2[k], o3[k], a0, a1);
      }
      const float s0 = rs8_hi(a0, lane), s1 = rs8_hi(a1, lane);
      const int col = (lane & 7) * 8 + (lane >> 3);
      float* r2 = ldsf + L_R2;
      r2[w * 128 + col] = s0; r2[w * 128 + 64 + col] = s1;
      __syncthreads();
      if (w == 0) {
        const float2 u0 = *reinterpret_cast<const float2*>(r2 + lane * 2), u1 = *reinterpret_cast<const float2*>(r2 + 128 + lane * 2);
        const float2 u2 = *reinterpret_cast<const float2*>(r2 + 256 + lane * 2), u3 = *reinterpret_cast<const float2*>(r2 + 384 + lane * 2);
        __builtin_amdgcn_raw_buffer_store_b128(u32x4{__float_as_uint((u0.x + u1.x) + (u2.x + u3.x)), tag, __float_as_uint((u0.y + u1.y) + (u2.y + u3.y)), tag},
                                               mkrs(ws + WS_P2, 9 * 25 * 1024), lane * 16, (slice * 25 + (is_wo ? kvh : 16 + drb)) * 1024, AUX_SC1);
      }
    }
    return;
  }

  {
    const u32 tag = __builtin_amdgcn_readfirstlane(lds[L_FLAG]);
    float a0[8], a1[8];

    if (task >= 0) {
      const int rr = lane >> 3, cc = lane & 7;
      const int vo = rr * 72 * (FF * 2) + cc * 16;
      const u32* xp = lds + L_XP + rr * 36;
      u32x4 A[8], Bf[8];
      float c0[8], c1[8];
      const __amdgpu_buffer_rsrc_t rs_g = mkrs(wg, DM * FF * 2), rs_u = mkrs(wu, DM * FF * 2);
#define FISSUE(BUF, RS, KB) { const int so_ = (KB) < 9 ? task * 128 + (KB) * 8 * (FF * 2) : DM * FF * 2; \
      _Pragma("unroll") for (int j_ = 0; j_ < 8; ++j_) BUF[j_] = bld<AUX_NT>(RS, vo, so_ + j_ * (FF * 2)); } SB
#define MAC4(BUF, X0, X1, C0, C1) { _Pragma("unroll") for (int j_ = 0; j_ < 4; ++j_) { pair_mac2(BUF[2 * j_], BUF[2 * j_ + 1], X0[j_], X1[j_], C0, C1); pin8(C0); pin8(C1); SB; } }
#define FZERO(C0, C1) _Pragma("unroll") for (int k_ = 0; k_ < 8; ++k_) { C0[k_] = 0.f; C1[k_] = 0.f; }
      int tf = TF0;
#define FPACE() { const u32 ta_ = tstart + ((u32)tf >> 8); while ((int)((u32)__builtin_amdgcn_s_memrealtime() - ta_) < 0) __builtin_amdgcn_s_sleep(8); tf += TFS; }
      FPACE(); FISSUE(A, rs_g, 0); FPACE(); FISSUE(Bf, rs_u, 0);
      FZERO(a0, a1); FZERO(c0, c1);
#pragma unroll 1
      for (int k = 0; k < 9; ++k) {
        const u32x4 x0 = *reinterpret_cast<const u32x4*>(xp + 4 * k), x1 = *reinterpret_cast<const u32x4*>(xp + 288 + 4 * k);
        MAC4(A, x0, x1, a0, a1); if (k < 8) { FPACE(); } FISSUE(A, rs_g, k + 1);
        MAC4(Bf, x0, x1, c0, c1); if (k < 8) { FPACE(); } FISSUE(Bf, rs_u, k + 1);
      }
      {
        const float gs0 = rs8_hi(a0, lane), gs1 = rs8_hi(a1, lane), us0 = rs8_hi(c0, lane), us1 = rs8_hi(c1, lane);
        const int col = cc * 8 + rr;
        const float g0 = rbf(gs0), g1 = rbf(gs1);
        const float e0 = rbf(0.5f * g0 * (1.0f + tanhf(0.7978845608028654f * (g0 + 0.044715f * g0 * g0 * g0))));
        const float e1 = rbf(0.5f * g1 * (1.0f + tanhf(0.7978845608028654f * (g1 + 0.044715f * g1 * g1 * g1))));
        reinterpret_cast<u16*>(lds + L_H)[col] = (u16)f2bf(e0 * rbf(us0));
        reinterpret_cast<u16*>(lds + L_H)[64 + col] = (u16)f2bf(e1 * rbf(us1));
      }
      __builtin_amdgcn_fence(__ATOMIC_RELEASE, "wavefront"); __builtin_amdgcn_wave_barrier(); __builtin_amdgcn_fence(__ATOMIC_ACQUIRE, "wavefront");
      st_gran((gu64*)(ws + WS_HG) + task * 64 + lane, tag, lds[L_H + lane]);
    }
    __builtin_amdgcn_s_setprio(0);
    if (is_dn) {
      { const u32 ta_ = tstart + (FEND + 3000) / 10; while ((int)((u32)__builtin_amdgcn_s_memrealtime() - ta_) < 0) __builtin_amdgcn_s_sleep(32); }
      const __amdgpu_buffer_rsrc_t rs_h = mkrs(ws + WS_HG + (size_t)drb * 4096, 4096);
      u32x4 v[4];
      for (u32 spins = 0;; ++spins) {
        asm volatile("" ::: "memory");
        bool ok = true;
#pragma unroll
        for (int k = 0; k < 4; ++k) v[k] = bld<AUX_SC1>(rs_h, lane * 16, k * 1024);
#pragma unroll
        for (int k = 0; k < 4; ++k) ok &= v[k][1] == tag && v[k][3] == tag;
        if (__all(ok) || spins > SPIN_CAP) break;
        __builtin_amdgcn_s_sleep(16);
      }
#pragma unroll
      for (int k = 0; k < 4; ++k) *reinterpret_cast<u32x2*>(lds + L_VEC + k * 128 + (l5 >> 4) * 64 + hf * 32 + (l5 & 15) * 2) = u32x2{v[k][0], v[k][2]};
    }
    u32 xpair = 0;
    if (is_red) xpair = reinterpret_cast<const u32*>(x)[hf * 288 + slice * 32 + l5];
    __syncthreads();
    if (is_red) {
      final_rows(0);
      __syncthreads();
      f32x4 t = *reinterpret_cast<const f32x4*>(ldsf + L_SC + lane * 4);
#pragma unroll
      for (int k = 1; k < 5; ++k) t += *reinterpret_cast<const f32x4*>(ldsf + L_SC + (k * 64 + lane) * 4);
      const float y0 = t[0], y1 = t[1], f0 = t[2], f1 = t[3];
      const u32 o0 = f2bf(rbf(bf2f(xpair & 0xffffu) + rbf(y0)) + rbf(f0)), o1 = f2bf(rbf(bf2f(xpair >> 16) + rbf(y1)) + rbf(f1));
      reinterpret_cast<u32*>(out)[hf * 288 + slice * 32 + l5] = o0 | (o1 << 16);
      if (slice == 0 && lane == 0) __hip_atomic_store((gu32*)(ws + WS_EPOCH), tag, RLX_AGENT);
    }
  }
}

extern "C" void kernel_launch(void* const* d_in, const int* in_sizes, int n_in, void* d_out, int out_size, void* d_ws, size_t ws_size,
                              hipStream_t stream) {
  (void)in_sizes; (void)n_in; (void)out_size;
  if (ws_size < WS_END) return;
  hipLaunchKernelGGL(fused_decode_kernel, dim3(256), dim3(NTHR), 0, stream, (const u16*)d_in[0], (const u16*)d_in[1], (const u16*)d_in[2],
                     (const u16*)d_in[3], (const u16*)d_in[4], (const u16*)d_in[6], (const u16*)d_in[7], (const u16*)d_in[8], (u16*)d_out,
                     (unsigned char*)d_ws);
}
```

```cpp
#include <hip/hip_runtime.h>
#include <hip/hip_bf16.h>
#include <stdint.h>

typedef unsigned u32;
typedef unsigned short u16;
typedef unsigned long long u64;
typedef u32 u32x4 __attribute__((ext_vector_type(4)));
typedef u32 u32x2 __attribute__((ext_vector_type(2)));
typedef float f32x2 __attribute__((ext_vector_type(2)));
typedef float f32x4 __attribute__((ext_vector_type(4)));
typedef __bf16 bf16x2 __attribute__((ext_vector_type(2)));
#define GAS __attribute__((address_space(1)))
#define LAS __attribute__((address_space(3)))
typedef GAS u64 gu64;
typedef GAS u32 gu32;
#define RLX_AGENT __ATOMIC_RELAXED, __HIP_MEMORY_SCOPE_AGENT
#define SB __builtin_amdgcn_sched_barrier(0)

constexpr int DM = 576, SK = 32768, DH = 128, FF = 4608, NQ = 4096;
constexpr int NSW = 4, NTHR = (NSW + 1) * 64, U = 4;
constexpr int TB = U * 1024;
constexpr int NT = SK / 16;
constexpr int RT = NT / 8;
constexpr int TAU = 555;
constexpr int EQ = 1300;
constexpr int FEND = 62000;
constexpr int NX = 4, NE = 8;
constexpr int NCTR = 4;
constexpr int NS = 60;
constexpr int NSF = NS - 4;
constexpr int PEX = (NS - NSF) * NSW;
constexpr int PCOM = (RT - NS * NSW) * 8;
static_assert(PEX == 16 && NE % 2 == 0 && NSF >= 4 * (NE + NX) + 6 && NX <= NE, "");
constexpr int TK = TAU * 256 / 10, TKF = TK * (2 * NS - 12) / (2 * NSF - 12);
constexpr int TF0 = 8000 * 256 / 10, TFS = (FEND - 8000) * 256 / 10 / 17;
constexpr int NFT = 72;
constexpr int AUX_NT = 2, AUX_SC1 = 16;
constexpr float LOG2E = 1.4426950408889634f;
constexpr u32 SPIN_CAP = 1u << 20;

constexpr size_t WS_EPOCH = 0;
constexpr size_t WS_CTR = 4096;
constexpr size_t WS_QG = WS_CTR + 128 * 256;
constexpr size_t WS_P1 = WS_QG + 2 * 64 * 4 * 64 * 8;
constexpr int P1_STRIDE = 272;
constexpr size_t WS_P2 = WS_P1 + (size_t)256 * P1_STRIDE * 8;
constexpr size_t WS_HG = WS_P2 + (size_t)9 * 25 * 128 * 8;
constexpr size_t WS_END = WS_HG + (size_t)NFT * 64 * 8;

__device__ __forceinline__ float bf2f(u32 lo16) { return __uint_as_float(lo16 << 16); }
__device__ __forceinline__ float rbf(float f) { return (float)(__bf16)f; }
__device__ __forceinline__ u32 f2bf(float f) { const __bf16 h = (__bf16)f; return (u32)__builtin_bit_cast(u16, h); }
__device__ __forceinline__ float dot2(u32 a, u32 b, float c) {
  return __builtin_amdgcn_fdot2_f32_bf16(__builtin_bit_cast(bf16x2, a), __builtin_bit_cast(bf16x2, b), c, false);
}
template <int CTRL> __device__ __forceinline__ float dpp(float x) {
  return __int_as_float(__builtin_amdgcn_update_dpp(0, __float_as_int(x), CTRL, 0xf, 0xf, true));
}
constexpr int DPP_XOR1 = 0xB1, DPP_XOR2 = 0x4E, DPP_HMIR = 0x141, DPP_ROR8 = 0x128, DPP_ROR4 = 0x124, DPP_BC = 0x150;
__device__ __forceinline__ void swap32(float& a, float& b) {
  auto r = __builtin_amdgcn_permlane32_swap(__float_as_uint(a), __float_as_uint(b), false, false);
  a = __uint_as_float(r[0]); b = __uint_as_float(r[1]);
}
__device__ __forceinline__ void swap16(float& a, float& b) {
  auto r = __builtin_amdgcn_permlane16_swap(__float_as_uint(a), __float_as_uint(b), false, false);
  a = __uint_as_float(r[0]); b = __uint_as_float(r[1]);
}
__device__ __forceinline__ float half_sum(float x) {
  x += dpp<DPP_XOR1>(x); x += dpp<DPP_XOR2>(x); x += dpp<DPP_ROR4>(x); x += dpp<DPP_ROR8>(x);
  float a = x, b = x; swap16(a, b); return a + b;
}
__device__ __forceinline__ __amdgpu_buffer_rsrc_t mkrs(const void* p, u32 bytes) {
  return __builtin_amdgcn_make_buffer_rsrc(const_cast<void*>(p), 0, bytes, 0x00020000);
}
template <int AUX> __device__ __forceinline__ u32x4 bld(__amdgpu_buffer_rsrc_t rs, int voff, int soff) {
  return __builtin_amdgcn_raw_buffer_load_b128(rs, voff, soff, AUX);
}
__device__ __forceinline__ void pair_mac2(const u32x4& a, const u32x4& b, u32 xp0, u32 xp1, float* acc0, float* acc1) {
#pragma unroll
  for (int d = 0; d < 4; ++d) {
    const u32 lo = __builtin_amdgcn_perm(b[d], a[d], 0x05040100u), hi = __builtin_amdgcn_perm(b[d], a[d], 0x07060302u);
    acc0[2 * d] = dot2(lo, xp0, acc0[2 * d]); acc0[2 * d + 1] = dot2(hi, xp0, acc0[2 * d + 1]);
    acc1[2 * d] = dot2(lo, xp1, acc1[2 * d]); acc1[2 * d + 1] = dot2(hi, xp1, acc1[2 * d + 1]);
  }
}
__device__ __forceinline__ float rs8_hi(float* v, int lane) {
#pragma unroll
  for (int k = 0; k < 4; ++k) { swap32(v[k], v[k + 4]); v[k] += v[k + 4]; }
#pragma unroll
  for (int k = 0; k < 2; ++k) { swap16(v[k], v[k + 2]); v[k] += v[k + 2]; }
  const bool up = lane & 8;
  const float keep = up ? v[1] : v[0], send = up ? v[0] : v[1];
  return keep + dpp<DPP_ROR8>(send);
}
__device__ __forceinline__ void pin8(float* a) {
  asm volatile("" : "+v"(a[0]), "+v"(a[1]), "+v"(a[2]), "+v"(a[3]), "+v"(a[4]), "+v"(a[5]), "+v"(a[6]), "+v"(a[7]));
}
__device__ __forceinline__ void st_gran(gu64* g, u32 tag, u32 v) { __hip_atomic_store(g, ((u64)tag << 32) | (u64)v, RLX_AGENT); }

constexpr int L_XP = 0;
constexpr int L_Q = L_XP + 576;
constexpr int L_FLAG = L_Q + 128;
constexpr int L_H = L_FLAG + 4;
constexpr int L_VEC = L_H + 64;
constexpr int L_SC = L_VEC + 512;
constexpr int L_STASH = L_SC + NSW * 64 * 16;
constexpr int L_R2 = L_STASH;
constexpr int L_RED = 1024;
constexpr int L_TOTAL = L_STASH + NSW * NE * 1024;

__global__ void __launch_bounds__(NTHR, 2)
fused_decode_kernel(const u16* __restrict__ x, const u16* __restrict__ kc, const u16* __restrict__ vc, const u16* __restrict__ wq,
                    const u16* __restrict__ wo, const u16* __restrict__ wg, const u16* __restrict__ wu, const u16* __restrict__ wd,
                    u16* __restrict__ out, unsigned char* __restrict__ ws) {
  __shared__ __attribute__((aligned(16))) u32 lds[L_TOTAL];
  const u32 tstart = (u32)__builtin_amdgcn_s_memrealtime();
  asm volatile("" :: "s"(x), "s"(kc), "s"(vc), "s"(wq), "s"(ws), "s"(tstart));
#define PACE(T) { const u32 ta_ = tstart + ((u32)(T) >> 8); while ((int)((u32)__builtin_amdgcn_s_memrealtime() - ta_) < 0) __builtin_amdgcn_s_sleep(1); }
  float* ldsf = reinterpret_cast<float*>(lds);
  const int tid = (int)threadIdx.x - 64, lane = threadIdx.x & 63;
  const int w = __builtin_amdgcn_readfirstlane((int)(threadIdx.x >> 6)) - 1;
  const int bid = blockIdx.x;
  const int s = bid & 7, g = bid >> 3, b = g >> 4, kvh = g & 15;
  const int ii = b * 8 + s;
  const int ee = kvh * 7 + ii - 9;
  const bool is_wo = ii < 9, is_dn = !is_wo && ee < 81, is_red = !is_wo && ee >= 81 && ee < 90;
  const int drb = ee / 9, slice = is_wo ? ii : ee - drb * 9;

  const u16* kbase = kc + (size_t)g * SK * DH;
  const u16* vbase = vc + (size_t)g * SK * DH;
  const int kv_vo = lane * 16, cvo = lane * 4;
  const int t0 = s * RT + w;
  const int hf = lane >> 5, l5 = lane & 31;
  const int sub = (w + s) & (NCTR - 1);
  const int qcg = g + 32 * (s >> 2), qkq = s & 3;
  u32x4 EX[NX][U];
#define ISSUE0(BUF, T) { const __amdgpu_buffer_rsrc_t rs_ = mkrs(kbase, SK * DH * 2); const int so_ = (T) * TB; \
    _Pragma("unroll") for (int i_ = 0; i_ < U; ++i_) BUF[i_] = bld<AUX_NT>(rs_, kv_vo + i_ * 1024, so_); SB; }
#define ISSUE(BUF, CL, BASE, T, REAL) { CL = __builtin_amdgcn_raw_ptr_buffer_atomic_add_i32(1, mkrs(ws + WS_CTR + g * 1024 + sub * 256, (REAL) ? 4 : 0), cvo, 0, 0); SB; \
    const __amdgpu_buffer_rsrc_t rs_ = mkrs(BASE, SK * DH * 2); const int so_ = (T) * TB; \
    _Pragma("unroll") for (int i_ = 0; i_ < U; ++i_) BUF[i_] = bld<AUX_NT>(rs_, kv_vo + i_ * 1024, so_); SB; }
  if (w >= 0) { ISSUE0(EX[0], t0); ISSUE0(EX[1], t0 + NSW); }
  asm volatile("; ROLE SPLIT 0" ::: "memory");
  volatile LAS u32* lflag = (volatile LAS u32*)(lds + L_FLAG);
  if (threadIdx.x == 0) lflag[1] = 0u;
  asm volatile("; ROLE SPLIT 1" ::: "memory");
  if (w < 0) {
    __builtin_amdgcn_s_setprio(3);
    u32x4 wqv[18];
    u32 xr[9];
    {
      const __amdgpu_buffer_rsrc_t rs_x = mkrs(x, 2 * DM * 2);
      const u32x4 xa = bld<0>(rs_x, lane * 36, 0), xb = bld<0>(rs_x, lane * 36 + 16, 0);
      xr[8] = __builtin_amdgcn_raw_buffer_load_b32(rs_x, lane * 36 + 32, 0, 0);
#pragma unroll
      for (int j = 0; j < 4; ++j) { xr[j] = xa[j]; xr[4 + j] = xb[j]; }
    }
    const u32 epoch_v = __hip_atomic_load((gu32*)(ws + WS_EPOCH), RLX_AGENT);
    const __amdgpu_buffer_rsrc_t rs_wq = mkrs(wq, DM * NQ * 2);
    const int vo = (lane >> 3) * 18 * (NQ * 2) + (lane & 7) * 16;
#pragma unroll
    for (int j = 0; j < 18; ++j) wqv[j] = bld<AUX_NT>(rs_wq, vo, qcg * 128 + (qkq * 144 + j) * (NQ * 2));
    SB;
    {
      float v[18]; float sum = 0.f;
#pragma unroll
      for (int j = 0; j < 9; ++j) { v[2 * j] = bf2f(xr[j] & 0xffffu); v[2 * j + 1] = bf2f(xr[j] >> 16); sum += v[2 * j] + v[2 * j + 1]; }
      const float mean = half_sum(sum) * (1.0f / DM);
      float sq = 0.f;
#pragma unroll
      for (int j = 0; j < 18; ++j) { const float d = v[j] - mean; sq = fmaf(d, d, sq); }
      const float var = half_sum(sq) * (1.0f / DM);
      const float mb = rbf(mean), rsd = 1.0f / rbf(sqrtf(rbf(rbf(var) + 1e-5f)));
#pragma unroll
      for (int j = 0; j < 9; ++j)
        lds[L_XP + lane * 9 + j] = f2bf(rbf(v[2 * j] - mb) * rsd) | (f2bf(rbf(v[2 * j + 1] - mb) * rsd) << 16);
    }
    __builtin_amdgcn_fence(__ATOMIC_RELEASE, "wavefront"); __builtin_amdgcn_wave_barrier(); __builtin_amdgcn_fence(__ATOMIC_ACQUIRE, "wavefront");
    const u32 tag = __builtin_amdgcn_readfirstlane(epoch_v) + 0x9E3779B9u;
    float a0[8], a1[8];
#pragma unroll
    for (int k = 0; k < 8; ++k) { a0[k] = 0.f; a1[k] = 0.f; }
    {
      const u32* xp = lds + L_XP + qkq * 72 + (lane >> 3) * 9;
#pragma unroll
      for (int j = 0; j < 9; ++j) pair_mac2(wqv[2 * j], wqv[2 * j + 1], xp[j], xp[288 + j], a0, a1);
      const float s0 = rs8_hi(a0, lane), s1 = rs8_hi(a1, lane);
      gu64* qd = (gu64*)(ws + WS_QG) + (qcg * 4 + qkq) * 64 + (lane & 7) * 8 + (lane >> 3);
      st_gran(qd, tag, __float_as_uint(s0)); st_gran(qd + 16384, tag, __float_as_uint(s1));
    }
    if (s == 0 && lane < NCTR) st_gran((gu64*)(ws + WS_CTR + g * 1024 + lane * 256), tag, 0u);
    __syncthreads();
    {
      const unsigned char* qsrc = ws + WS_QG + (size_t)(b * 64 + kvh * 4) * 2048;
      __builtin_amdgcn_s_sleep(20);
      const __amdgpu_buffer_rsrc_t rs_q = mkrs(qsrc, 8192);
      u32x4 v[8];
      for (u32 spins = 0;; ++spins) {
        asm volatile("" ::: "memory");
#pragma unroll
        for (int k = 0; k < 8; ++k) v[k] = bld<AUX_SC1>(rs_q, hf * 2048 + l5 * 16, (k >> 2) * 4096 + (k & 3) * 512);
        bool ok = (u32)(__hip_atomic_load((const gu64*)(ws + WS_CTR + g * 1024 + (lane & (NCTR - 1)) * 256), RLX_AGENT) >> 32) == tag;
#pragma unroll
        for (int k = 0; k < 8; ++k) ok &= v[k][1] == tag && v[k][3] == tag;
        if (__all(ok) || spins > SPIN_CAP) break;
        __builtin_amdgcn_s_sleep(1);
      }
#pragma unroll
      for (int k = 0; k < 2; ++k) {
        const u32x4* u = v + 4 * k;
        const float qa = (__uint_as_float(u[0][0]) + __uint_as_float(u[1][0])) + (__uint_as_float(u[2][0]) + __uint_as_float(u[3][0]));
        const float qb = (__uint_as_float(u[0][2]) + __uint_as_float(u[1][2])) + (__uint_as_float(u[2][2]) + __uint_as_float(u[3][2]));
        lds[L_Q + k * 64 + lane] = f2bf(qa) | (f2bf(qb) << 16);
      }
      lds[L_FLAG] = tag;
      __builtin_amdgcn_fence(__ATOMIC_RELEASE, "workgroup");
      lflag[1] = 1u;
    }
  }
  asm volatile("; ROLE SPLIT 2" ::: "memory");

  const int task = (s & 3) == (g & 3) ? g * 2 + (s >> 2) : ((g >> 2) == s && (g & 3) == ((s + 1) & 3) ? 64 + s : -1);
  const int r = lane >> 4, c = lane & 15;
  auto final_rows = [&](int j) {
    const u32 tag = __builtin_amdgcn_readfirstlane(lds[L_FLAG]);
    const __amdgpu_buffer_rsrc_t rs_y = mkrs(ws + WS_P2 + (size_t)(slice * 25 + j * 5) * 1024, 5 * 1024);
    u32x4 v[5];
    for (u32 spins = 0;; ++spins) {
      asm volatile("" ::: "memory");
      bool ok = true;
#pragma unroll
      for (int k = 0; k < 5; ++k) v[k] = bld<AUX_SC1>(rs_y, lane * 16, k * 1024);
#pragma unroll
      for (int k = 0; k < 5; ++k) ok &= v[k][1] == tag && v[k][3] == tag;
      if (__all(ok) || spins > SPIN_CAP) break;
      __builtin_amdgcn_s_sleep(1);
    }
    const float a0 = __uint_as_float(v[0][0]), a1 = __uint_as_float(v[0][2]);
    const float b0 = (__uint_as_float(v[1][0]) + __uint_as_float(v[2][0])) + (__uint_as_float(v[3][0]) + __uint_as_float(v[4][0]));
    const float b1 = (__uint_as_float(v[1][2]) + __uint_as_float(v[2][2])) + (__uint_as_float(v[3][2]) + __uint_as_float(v[4][2]));
    *reinterpret_cast<f32x4*>(ldsf + L_SC + (j * 64 + lane) * 4) = j < 3 ? f32x4{a0 + b0, a1 + b1, 0.f, 0.f} : (j == 3 ? f32x4{a0, a1, b0, b1} : f32x4{0.f, 0.f, a0 + b0, a1 + b1});
  };
  if (w >= 0) {
    u32x4 B0[U], B1[U], B2[U];
    int c0, c1, c2;
    float acc0[8], acc1[8];
#pragma unroll
    for (int k = 0; k < 8; ++k) { acc0[k] = 0.f; acc1[k] = 0.f; }
    float mrun = -INFINITY, lrun = 0.f;
    u32* mylds = lds + L_STASH + w * NE * 1024;
#define WAITV(N) __builtin_amdgcn_s_waitcnt(0x0f70 | ((N) & 15) | (((N) >> 4) << 14)); SB
    int ne = 0, tnext = TK + EQ * 256 / 10;
#pragma unroll
    for (int e = 2; e < NX; ++e) { WAITV(4); PACE(tnext); tnext += TK; ISSUE0(EX[e], t0 + e * NSW); }
    WAITV(4); PACE(tnext); tnext += TK; ISSUE(B0, c0, kbase, t0 + NX * NSW, false);
    WAITV(5); PACE(tnext); tnext += TK; ISSUE(B1, c1, kbase, t0 + (NX + 1) * NSW, false);
    __syncthreads();
    u32x4* stash = reinterpret_cast<u32x4*>(mylds) + lane;
#pragma unroll 1
    while (ne < NE && lflag[1] == 0u) {
      WAITV(5);
#pragma unroll
      for (int i = 0; i < U; ++i) stash[ne * 256 + i * 64] = B0[i];
      SB; PACE(tnext); tnext += TK; ISSUE(B0, c0, kbase, t0 + (NX + ne + 2) * NSW, false);
      WAITV(5);
#pragma unroll
      for (int i = 0; i < U; ++i) stash[ne * 256 + 256 + i * 64] = B1[i];
      SB; PACE(tnext); tnext += TK; ISSUE(B1, c1, kbase, t0 + (NX + ne + 3) * NSW, false);
      ne += 2;
    }
    while (lflag[1] == 0u) __builtin_amdgcn_s_sleep(1);
    __builtin_amdgcn_fence(__ATOMIC_ACQUIRE, "workgroup");
    const u32x4 q0 = *reinterpret_cast<const u32x4*>(lds + L_Q + c * 4);
    const u32x4 q1 = *reinterpret_cast<const u32x4*>(lds + L_Q + 64 + c * 4);
    u16* scl = reinterpret_cast<u16*>(lds + L_SC + w * 64 * 16) + (lane >> 1);

    float mseen = -3.0e38f;
    auto kstep = [&](const u32x4* Kt, int slot) {
      float xv[8];
#pragma unroll
      for (int i = 0; i < U; ++i) {
        float d0 = 0.f, d1 = 0.f;
#pragma unroll
        for (int k = 0; k < 4; ++k) { d0 = dot2(Kt[i][k], q0[k], d0); d1 = dot2(Kt[i][k], q1[k], d1); }
        xv[2 * i] = d0; xv[2 * i + 1] = d1;
      }
#pragma unroll
      for (int k = 0; k < 4; ++k) { const bool up = c & 8; const float keep = up ? xv[k + 4] : xv[k], send = up ? xv[k] : xv[k + 4]; xv[k] = keep + dpp<DPP_ROR8>(send); }
#pragma unroll
      for (int k = 0; k < 2; ++k) { const bool up = c & 4; const float keep = up ? xv[k + 2] : xv[k], send = up ? xv[k] : xv[k + 2]; xv[k] = keep + dpp<DPP_HMIR>(send); }
      { const bool up = c & 2; const float keep = up ? xv[1] : xv[0], send = up ? xv[0] : xv[1]; xv[0] = keep + dpp<DPP_XOR2>(send); }
      const float sc = xv[0] + dpp<DPP_XOR1>(xv[0]);
      const u32 sb = f2bf(rbf(sc) * 0.08838834764831845f);
      scl[slot * 32] = (u16)sb;
      mseen = fmaxf(mseen, bf2f(sb));
    };
    auto pv1 = [&](const u32x4& Vt, float p0, float p1) {
#pragma unroll
      for (int k = 0; k < 4; ++k) {
        const f32x2 v = {__uint_as_float(Vt[k] << 16), __uint_as_float(Vt[k] & 0xffff0000u)};
        const f32x2 r0 = __builtin_elementwise_fma(v, f32x2{p0, p0}, f32x2{acc0[2 * k], acc0[2 * k + 1]});
        const f32x2 r1 = __builtin_elementwise_fma(v, f32x2{p1, p1}, f32x2{acc1[2 * k], acc1[2 * k + 1]});
        acc0[2 * k] = r0[0]; acc0[2 * k + 1] = r0[1]; acc1[2 * k] = r1[0]; acc1[2 * k + 1] = r1[1];
      }
    };
    auto vstep = [&](const u32x4* Vt, int slot) {
      const float sc = bf2f(scl[slot * 32]);
      if (__any(mseen > mrun)) {
        float mn = fmaxf(mseen, dpp<DPP_ROR4>(mseen)); mn = fmaxf(mn, dpp<DPP_ROR8>(mn)); mn = fmaxf(mn, mrun);
        const float al = __builtin_amdgcn_exp2f((mrun - mn) * LOG2E);
        mrun = mn; lrun *= al;
        const float al0 = dpp<DPP_BC + 0>(al), al1 = dpp<DPP_BC + 2>(al);
#pragma unroll
        for (int k = 0; k < 8; ++k) { acc0[k] *= al0; acc1[k] *= al1; }
      }
      const float p = __builtin_amdgcn_exp2f((sc - mrun) * LOG2E);
      lrun += p;
      pv1(Vt[0], dpp<DPP_BC + 0>(p), dpp<DPP_BC + 2>(p));
      pv1(Vt[1], dpp<DPP_BC + 4>(p), dpp<DPP_BC + 6>(p));
      pv1(Vt[2], dpp<DPP_BC + 8>(p), dpp<DPP_BC + 10>(p));
      pv1(Vt[3], dpp<DPP_BC + 12>(p), dpp<DPP_BC + 14>(p));
      pin8(acc0); pin8(acc1);
    };
    const int g3 = g & 3, nx = (g3 == (((g >> 2) + 1) & 3) ? 3 : 2) * PEX;
    const int n = task >= 0 ? NSF : NS, tkb = (task >= 0 ? TKF : TK) - (nx > 2 * PEX ? 6 * 256 / 10 : 0);
    int it = NX + ne, iend = 0x7fffffff, ptile = 0, sbase = NX;
#define STEP(BUF, CL, NBUF, NCL) { \
      const int i2_ = it + 2 - 2 * n;                        \
      if (it + 2 < iend) { PACE(tnext); } tnext += it >= 10 && i2_ < 0 ? tkb : TK; \
      if (it < iend) { WAITV(5); \
        if (i2_ >= 0 && !(i2_ & 1) && ptile < NT) { const int pi_ = (__builtin_amdgcn_readfirstlane(CL) & 0xffff) * NCTR + sub, pc_ = pi_ - nx; \
          const int e_ = pi_ >> 4, reg_ = e_ == 2 ? (g >> 2) : g3 + 4 * e_; \
          ptile = pc_ < 0 ? reg_ * RT + NSF * NSW + (pi_ & 15) : (pc_ < PCOM ? (pc_ & 7) * RT + NS * NSW + (pc_ >> 3) : NT); \
          if (ptile >= NT) iend = it + 2; } } \
      const bool isv_ = i2_ >= 0 ? (i2_ & 1) : (i2_ >= -n); \
      const int tl_ = i2_ >= 0 ? ptile : t0 + (isv_ ? i2_ + n : i2_ + 2 * n) * NSW; \
      ISSUE(NBUF, NCL, isv_ ? vbase : kbase, tl_, i2_ >= -2 && !(i2_ & 1) && ptile < NT); \
      if (it < iend) { const int i0_ = i2_ - 2; \
        if (i0_ >= 0 ? (i0_ & 1) : (i0_ >= -n)) vstep(BUF, i0_ >= 0 ? 63 : i0_ + n); else kstep(BUF, i0_ >= 0 ? 63 : it); } \
      SB; ++it; }
    do {
      STEP(B0, c0, B2, c2);
      if (ne == 0 && sbase != 0) {
#pragma unroll
        for (int e = 0; e < NX; ++e)
#pragma unroll
          for (int i = 0; i < U; ++i) stash[e * 256 + i * 64] = EX[e][i];
        ne = NX; sbase = 0;
      }
      if (ne > 0) {
        --ne;
        u32x4 KE[U];
#pragma unroll
        for (int i = 0; i < U; ++i) KE[i] = stash[ne * 256 + i * 64];
        kstep(KE, sbase + ne); SB;
      }
      STEP(B1, c1, B0, c0);
      STEP(B2, c2, B1, c1);
    } while (it < iend);
    const u32 tag = __builtin_amdgcn_readfirstlane(lds[L_FLAG]);
    const int h = w >> 1;
    {
      float lr = lrun + dpp<DPP_ROR4>(lrun); lr += dpp<DPP_ROR8>(lr);
      float a = mrun, bb = mrun; swap16(a, bb); float mw = fmaxf(a, bb);
      a = mw; bb = mw; swap32(a, bb); mw = fmaxf(a, bb);
      const float f = __builtin_amdgcn_exp2f((mrun - mw) * LOG2E);
      lr *= f;
      a = lr; bb = lr; swap16(a, bb); lr = a + bb;
      a = lr; bb = lr; swap32(a, bb); lr = a + bb;
      const float f0 = dpp<DPP_BC + 0>(f), f1 = dpp<DPP_BC + 2>(f);
#pragma unroll
      for (int k = 0; k < 8; ++k) { acc0[k] *= f0; acc1[k] *= f1; }
#pragma unroll
      for (int k = 0; k < 8; ++k) { swap32(acc0[k], acc1[k]); acc0[k] += acc1[k]; }
#pragma unroll
      for (int k = 0; k < 4; ++k) { swap16(acc0[k], acc0[k + 4]); acc0[k] += acc0[k + 4]; }
      *reinterpret_cast<f32x4*>(mylds + L_RED + (r >> 1) * 128 + c * 8 + (r & 1) * 4) = f32x4{acc0[0], acc0[1], acc0[2], acc0[3]};
      if (lane == 0 || lane == 2) { mylds[L_RED + 256 + lane] = __float_as_uint(mw); mylds[L_RED + 256 + lane + 1] = __float_as_uint(lr); }
    }
    __syncthreads();
    const __amdgpu_buffer_rsrc_t rs_p1 = mkrs(ws + WS_P1 + (size_t)kvh * 16 * P1_STRIDE * 8, 16 * P1_STRIDE * 8);
    {
      const float* red = ldsf + L_STASH + L_RED;
      float M = -INFINITY;
#pragma unroll
      for (int k = 0; k < NSW; ++k) M = fmaxf(M, red[k * NE * 1024 + 256 + h * 2]);
      float o = 0.f, L = 0.f;
#pragma unroll
      for (int k = 0; k < NSW; ++k) {
        const float f = __builtin_amdgcn_exp2f((red[k * NE * 1024 + 256 + h * 2] - M) * LOG2E);
        o = fmaf(f, red[k * NE * 1024 + tid], o); L = fmaf(f, red[k * NE * 1024 + 256 + h * 2 + 1], L);
      }
      __builtin_amdgcn_raw_buffer_store_b64(u32x2{__float_as_uint(o), tag}, rs_p1, tid * 8, ii * (P1_STRIDE * 8), AUX_SC1);
      __builtin_amdgcn_raw_buffer_store_b64(u32x2{__float_as_uint((lane & 1) ? L : M), tag}, rs_p1, (tid & 127) < 2 ? (256 + h * 2 + lane) * 8 : 0x7fff0000, ii * (P1_STRIDE * 8), AUX_SC1);
    }
    if (!is_wo && !is_dn) {
      if (is_red) { final_rows(w + 1); __syncthreads(); }
      return;
    }
    SB;
    u32x4 wv[16];
    {
      const int rw = is_wo ? 8 : 16;
      const u16* wb = is_wo ? wo + (size_t)kvh * 256 * DM : wd + (size_t)drb * 512 * DM;
      const __amdgpu_buffer_rsrc_t rs_a = mkrs(wb, is_wo ? 256 * DM * 2 : (is_dn ? 512 * DM * 2 : 0)), rs_b = mkrs(wb, is_dn ? 512 * DM * 2 : 0);
      const int vo = (lane >> 3) * rw * (DM * 2) + (lane & 7) * 16, so = 8 * w * rw * (DM * 2) + slice * 128;
#pragma unroll
      for (int k = 0; k < 16; ++k) wv[k] = bld<AUX_NT>(k < 8 ? rs_a : rs_b, vo, so + k * (DM * 2));
      SB;
    }
    u32* vec = lds + L_VEC;
    if (is_wo) {
      u32x4 av[8]; u32x2 ml;
      for (u32 spins = 0;; ++spins) {
        asm volatile("" ::: "memory");
#pragma unroll
        for (int k = 0; k < 8; ++k) av[k] = bld<AUX_SC1>(rs_p1, hf * (8 * P1_STRIDE * 8) + w * 512 + l5 * 16, k * (P1_STRIDE * 8));
        ml = __builtin_amdgcn_raw_buffer_load_b64(rs_p1, ((hf * 8 + (c >> 1)) * P1_STRIDE + 256 + h * 2 + (c & 1)) * 8, 0, AUX_SC1);
        bool ok = ml[1] == tag;
#pragma unroll
        for (int k = 0; k < 8; ++k) ok &= av[k][1] == tag && av[k][3] == tag;
        if (__all(ok) || spins > SPIN_CAP) break;
        __builtin_amdgcn_s_sleep(1);
      }
      const float mlv = __uint_as_float(ml[0]);
      const float mv = dpp<0xA0>(mlv);
      float M = fmaxf(mv, dpp<DPP_XOR2>(mv)); M = fmaxf(M, dpp<DPP_ROR4>(M)); M = fmaxf(M, dpp<DPP_ROR8>(M));
      const float f = __builtin_amdgcn_exp2f((mv - M) * LOG2E);
      float L = f * dpp<0xF5>(mlv);
      L += dpp<DPP_XOR2>(L); L += dpp<DPP_ROR4>(L); L += dpp<DPP_ROR8>(L);
      float oa = 0.f, ob = 0.f;
#define HOP1(K) { const float f_ = dpp<DPP_BC + 2 * (K)>(f); oa = fmaf(__uint_as_float(av[K][0]), f_, oa); ob = fmaf(__uint_as_float(av[K][2]), f_, ob); }
      HOP1(0) HOP1(1) HOP1(2) HOP1(3) HOP1(4) HOP1(5) HOP1(6) HOP1(7)
      const float rl = 1.0f / L;
      vec[w * 128 + hf * 64 + (l5 >> 2) * 8 + (l5 & 3)] = f2bf(oa * rl) | (f2bf(ob * rl) << 16);
      __builtin_amdgcn_fence(__ATOMIC_RELEASE, "wavefront"); __builtin_amdgcn_wave_barrier(); __builtin_amdgcn_fence(__ATOMIC_ACQUIRE, "wavefront");
    }
    {
      float a0[8], a1[8];
#pragma unroll
      for (int k = 0; k < 8; ++k) { a0[k] = 0.f; a1[k] = 0.f; }
      const u32* vp = vec + w * 128 + (lane >> 3) * 8;
      const u32x4 o0 = *reinterpret_cast<const u32x4*>(vp), o1 = *reinterpret_cast<const u32x4*>(vp + 64);
#pragma unroll
      for (int k = 0; k < 4; ++k) pair_mac2(wv[2 * k], wv[2 * k + 1], o0[k], o1[k], a0, a1);
      if (is_dn) {
        const u32x4 o2 = *reinterpret_cast<const u32x4*>(vp + 4), o3 = *reinterpret_cast<const u32x4*>(vp + 68);
#pragma unroll
        for (int k = 0; k < 4; ++k) pair_mac2(wv[8 + 2 * k], wv[9 + 2 * k], o2[k], o3[k], a0, a1);
      }
      const float s0 = rs8_hi(a0, lane), s1 = rs8_hi(a1, lane);
      const int col = (lane & 7) * 8 + (lane >> 3);
      float* r2 = ldsf + L_R2;
      r2[w * 128 + col] = s0; r2[w * 128 + 64 + col] = s1;
      __syncthreads();
      if (w == 0) {
        const float2 u0 = *reinterpret_cast<const float2*>(r2 + lane * 2), u1 = *reinterpret_cast<const float2*>(r2 + 128 + lane * 2);
        const float2 u2 = *reinterpret_cast<const float2*>(r2 + 256 + lane * 2), u3 = *reinterpret_cast<const float2*>(r2 + 384 + lane * 2);
        __builtin_amdgcn_raw_buffer_store_b128(u32x4{__float_as_uint((u0.x + u1.x) + (u2.x + u3.x)), tag, __float_as_uint((u0.y + u1.y) + (u2.y + u3.y)), tag},
                                               mkrs(ws + WS_P2, 9 * 25 * 1024), lane * 16, (slice * 25 + (is_wo ? kvh : 16 + drb)) * 1024, AUX_SC1);
      }
    }
    return;
  }

  {
    const u32 tag = __builtin_amdgcn_readfirstlane(lds[L_FLAG]);
    float a0[8], a1[8];

    if (task >= 0) {
      const int rr = lane >> 3, cc = lane & 7;
      const int vo = rr * 8 * (FF * 2) + cc * 16;
      const u32* xp = lds + L_XP + rr * 4;
      u32x4 A[8], Bf[8];
      float c0[8], c1[8];
      const __amdgpu_buffer_rsrc_t rs_g = mkrs(wg, DM * FF * 2), rs_u = mkrs(wu, DM * FF * 2);
#define FISSUE(BUF, RS, KB) { const int so_ = (KB) < 9 ? task * 128 + (KB) * 64 * (FF * 2) : DM * FF * 2; \
      _Pragma("unroll") for (int j_ = 0; j_ < 8; ++j_) BUF[j_] = bld<AUX_NT>(RS, vo, so_ + j_ * (FF * 2)); } SB
#define MAC4(BUF, X0, X1, C0, C1) { _Pragma("unroll") for (int j_ = 0; j_ < 4; ++j_) { pair_mac2(BUF[2 * j_], BUF[2 * j_ + 1], X0[j_], X1[j_], C0, C1); pin8(C0); pin8(C1); SB; } }
#define FZERO(C0, C1) _Pragma("unroll") for (int k_ = 0; k_ < 8; ++k_) { C0[k_] = 0.f; C1[k_] = 0.f; }
      int tf = TF0;
#define FPACE() { const u32 ta_ = tstart + ((u32)tf >> 8); while ((int)((u32)__builtin_amdgcn_s_memrealtime() - ta_) < 0) __builtin_amdgcn_s_sleep(8); tf += TFS; }
      FPACE(); FISSUE(A, rs_g, 0); FPACE(); FISSUE(Bf, rs_u, 0);
      FZERO(a0, a1); FZERO(c0, c1);
#pragma unroll 1
      for (int k = 0; k < 9; ++k) {
        const u32x4 x0 = *reinterpret_cast<const u32x4*>(xp + 32 * k), x1 = *reinterpret_cast<const u32x4*>(xp + 288 + 32 * k);
        MAC4(A, x0, x1, a0, a1); if (k < 8) { FPACE(); } FISSUE(A, rs_g, k + 1);
        MAC4(Bf, x0, x1, c0, c1); if (k < 8) { FPACE(); } FISSUE(Bf, rs_u, k + 1);
      }
      {
        const float gs0 = rs8_hi(a0, lane), gs1 = rs8_hi(a1, lane), us0 = rs8_hi(c0, lane), us1 = rs8_hi(c1, lane);
        const int col = cc * 8 + rr;
        const float g0 = rbf(gs0), g1 = rbf(gs1);
        const float e0 = rbf(0.5f * g0 * (1.0f + tanhf(0.7978845608028654f * (g0 + 0.044715f * g0 * g0 * g0))));
        const float e1 = rbf(0.5f * g1 * (1.0f + tanhf(0.7978845608028654f * (g1 + 0.044715f * g1 * g1 * g1))));
        reinterpret_cast<u16*>(lds + L_H)[col] = (u16)f2bf(e0 * rbf(us0));
        reinterpret_cast<u16*>(lds + L_H)[64 + col] = (u16)f2bf(e1 * rbf(us1));
      }
      __builtin_amdgcn_fence(__ATOMIC_RELEASE, "wavefront"); __builtin_amdgcn_wave_barrier(); __builtin_amdgcn_fence(__ATOMIC_ACQUIRE, "wavefront");
      st_gran((gu64*)(ws + WS_HG) + task * 64 + lane, tag, lds[L_H + lane]);
    }
    __builtin_amdgcn_s_setprio(0);
    if (is_dn) {
      { const u32 ta_ = tstart + (FEND + 3000) / 10; while ((int)((u32)__builtin_amdgcn_s_memrealtime() - ta_) < 0) __builtin_amdgcn_s_sleep(32); }
      const __amdgpu_buffer_rsrc_t rs_h = mkrs(ws + WS_HG + (size_t)drb * 4096, 4096);
      u32x4 v[4];
      for (u32 spins = 0;; ++spins) {
        asm volatile("" ::: "memory");
        bool ok = true;
#pragma unroll
        for (int k = 0; k < 4; ++k) v[k] = bld<AUX_SC1>(rs_h, lane * 16, k * 1024);
#pragma unroll
        for (int k = 0; k < 4; ++k) ok &= v[k][1] == tag && v[k][3] == tag;
        if (__all(ok) || spins > SPIN_CAP) break;
        __builtin_amdgcn_s_sleep(16);
      }
#pragma unroll
      for (int k = 0; k < 4; ++k) *reinterpret_cast<u32x2*>(lds + L_VEC + k * 128 + (l5 >> 4) * 64 + hf * 32 + (l5 & 15) * 2) = u32x2{v[k][0], v[k][2]};
    }
    u32 xpair = 0;
    if (is_red) xpair = reinterpret_cast<const u32*>(x)[hf * 288 + slice * 32 + l5];
    __syncthreads();
    if (is_red) {
      final_rows(0);
      __syncthreads();
      f32x4 t = *reinterpret_cast<const f32x4*>(ldsf + L_SC + lane * 4);
#pragma unroll
      for (int k = 1; k < 5; ++k) t += *reinterpret_cast<const f32x4*>(ldsf + L_SC + (k * 64 + lane) * 4);
      const float y0 = t[0], y1 = t[1], f0 = t[2], f1 = t[3];
      const u32 o0 = f2bf(rbf(bf2f(xpair & 0xffffu) + rbf(y0)) + rbf(f0)), o1 = f2bf(rbf(bf2f(xpair >> 16) + rbf(y1)) + rbf(f1));
      reinterpret_cast<u32*>(out)[hf * 288 + slice * 32 + l5] = o0 | (o1 << 16);
      if (slice == 0 && lane == 0) __hip_atomic_store((gu32*)(ws + WS_EPOCH), tag, RLX_AGENT);
    }
  }
}

extern "C" void kernel_launch(void* const* d_in, const int* in_sizes, int n_in, void* d_out, int out_size, void* d_ws, size_t ws_size,
                              hipStream_t stream) {
  (void)in_sizes; (void)n_in; (void)out_size;
  if (ws_size < WS_END) return;
  hipLaunchKernelGGL(fused_decode_kernel, dim3(256), dim3(NTHR), 0, stream, (const u16*)d_in[0], (const u16*)d_in[1], (const u16*)d_in[2],
                     (const u16*)d_in[3], (const u16*)d_in[4], (const u16*)d_in[6], (const u16*)d_in[7], (const u16*)d_in[8], (u16*)d_out,
                     (unsigned char*)d_ws);
}
```

```cpp
#include <hip/hip_runtime.h>
#include <hip/hip_bf16.h>
#include <stdint.h>

typedef unsigned u32;
typedef unsigned short u16;
typedef unsigned long long u64;
typedef u32 u32x4 __attribute__((ext_vector_type(4)));
typedef u32 u32x2 __attribute__((ext_vector_type(2)));
typedef float f32x2 __attribute__((ext_vector_type(2)));
typedef float f32x4 __attribute__((ext_vector_type(4)));
typedef __bf16 bf16x2 __attribute__((ext_vector_type(2)));
#define GAS __attribute__((address_space(1)))
#define LAS __attribute__((address_space(3)))
typedef GAS u64 gu64;
typedef GAS u32 gu32;
#define RLX_AGENT __ATOMIC_RELAXED, __HIP_MEMORY_SCOPE_AGENT
#define SB __builtin_amdgcn_sched_barrier(0)

constexpr int DM = 576, SK = 32768, DH = 128, FF = 4608, NQ = 4096;
constexpr int NSW = 4, NTHR = (NSW + 1) * 64, U = 4;
constexpr int TB = U * 1024;
constexpr int NT = SK / 16;
constexpr int NX = 4, NE = 8;
constexpr int TAU = 545;
constexpr int TAUS = 570, EQ = 900;
constexpr int LATE1 = 400, LATE2 = 1100;
constexpr int STALE = 450;
constexpr int FB0 = 11000, FBP = 8000;
constexpr int FBR = 60, FBN = 15, FBD = 20;
constexpr int KDZ = 80;
constexpr u32 W_NEAR = 0x40000000u, W_LAST = 0x40000001u;
constexpr int NS = NT / (8 * NSW);
constexpr int NSF = NS - 3;
static_assert(NE % 2 == 0 && NSF >= 4 * (NE + NX) + 6 && NX <= NE, "");
constexpr int PW0 = 0, PW1 = 1, PS0 = 9, PS1 = 10, PS2 = 3;
constexpr int PUSED = (1 << PW0) | (1 << PW1) | (1 << PS0) | (1 << PS1) | (1 << PS2);
__device__ __forceinline__ int tbase(int s, int w) { return ((w & 1) << PW0) | (((w >> 1) & 1) << PW1) | ((s & 1) << PS0) | (((s >> 1) & 1) << PS1) | (((s >> 2) & 1) << PS2); }
__device__ __forceinline__ int tpos(int j) {
  int t = 0, jb = 0;
#pragma unroll
  for (int k = 0; k < 11; ++k) if (!((PUSED >> k) & 1)) { t |= ((j >> jb) & 1) << k; ++jb; }
  return t;
}
constexpr int TK = TAU * 256 / 10, TKS = TAUS * 256 / 10, FTK = TKS * 8 / 9;
constexpr int T1 = TK + EQ * 256 / 10 + 12 * TKS + (5253 * TK) / 100, T2 = T1 + NS * TK;
constexpr int NFT = 72;
constexpr int AUX_NT = 2, AUX_SC1 = 16;
constexpr float LOG2E = 1.4426950408889634f;
constexpr u32 SPIN_CAP = 1u << 20;

constexpr size_t WS_EPOCH = 0;
constexpr size_t WS_PG = 4096;
constexpr size_t WS_QG = WS_PG + 256 * 256;
constexpr size_t WS_P1 = WS_QG + 2 * 64 * 4 * 64 * 8;
constexpr int P1_STRIDE = 272;
constexpr size_t WS_P2 = WS_P1 + (size_t)256 * P1_STRIDE * 8;
constexpr size_t WS_HG = WS_P2 + (size_t)9 * 25 * 128 * 8;
constexpr size_t WS_END = WS_HG + (size_t)NFT * 64 * 8;

__device__ __forceinline__ float bf2f(u32 lo16) { return __uint_as_float(lo16 << 16); }
__device__ __forceinline__ float rbf(float f) { return (float)(__bf16)f; }
__device__ __forceinline__ u32 f2bf(float f) { const __bf16 h = (__bf16)f; return (u32)__builtin_bit_cast(u16, h); }
__device__ __forceinline__ float dot2(u32 a, u32 b, float c) {
  return __builtin_amdgcn_fdot2_f32_bf16(__builtin_bit_cast(bf16x2, a), __builtin_bit_cast(bf16x2, b), c, false);
}
template <int CTRL> __device__ __forceinline__ float dpp(float x) {
  return __int_as_float(__builtin_amdgcn_update_dpp(0, __float_as_int(x), CTRL, 0xf, 0xf, true));
}
constexpr int DPP_XOR1 = 0xB1, DPP_XOR2 = 0x4E, DPP_HMIR = 0x141, DPP_ROR8 = 0x128, DPP_ROR4 = 0x124, DPP_BC = 0x150;
__device__ __forceinline__ void swap32(float& a, float& b) {
  auto r = __builtin_amdgcn_permlane32_swap(__float_as_uint(a), __float_as_uint(b), false, false);
  a = __uint_as_float(r[0]); b = __uint_as_float(r[1]);
}
__device__ __forceinline__ void swap16(float& a, float& b) {
  auto r = __builtin_amdgcn_permlane16_swap(__float_as_uint(a), __float_as_uint(b), false, false);
  a = __uint_as_float(r[0]); b = __uint_as_float(r[1]);
}
__device__ __forceinline__ float half_sum(float x) {
  x += dpp<DPP_XOR1>(x); x += dpp<DPP_XOR2>(x); x += dpp<DPP_ROR4>(x); x += dpp<DPP_ROR8>(x);
  float a = x, b = x; swap16(a, b); return a + b;
}
__device__ __forceinline__ __amdgpu_buffer_rsrc_t mkrs(const void* p, u32 bytes) {
  return __builtin_amdgcn_make_buffer_rsrc(const_cast<void*>(p), 0, bytes, 0x00020000);
}
template <int AUX> __device__ __forceinline__ u32x4 bld(__amdgpu_buffer_rsrc_t rs, int voff, int soff) {
  return __builtin_amdgcn_raw_buffer_load_b128(rs, voff, soff, AUX);
}
__device__ __forceinline__ void pair_mac2(const u32x4& a, const u32x4& b, u32 xp0, u32 xp1, float* acc0, float* acc1) {
#pragma unroll
  for (int d = 0; d < 4; ++d) {
    const u32 lo = __builtin_amdgcn_perm(b[d], a[d], 0x05040100u), hi = __builtin_amdgcn_perm(b[d], a[d], 0x07060302u);
    acc0[2 * d] = dot2(lo, xp0, acc0[2 * d]); acc0[2 * d + 1] = dot2(hi, xp0, acc0[2 * d + 1]);
    acc1[2 * d] = dot2(lo, xp1, acc1[2 * d]); acc1[2 * d + 1] = dot2(hi, xp1, acc1[2 * d + 1]);
  }
}
__device__ __forceinline__ float rs8_hi(float* v, int lane) {
#pragma unroll
  for (int k = 0; k < 4; ++k) { swap32(v[k], v[k + 4]); v[k] += v[k + 4]; }
#pragma unroll
  for (int k = 0; k < 2; ++k) { swap16(v[k], v[k + 2]); v[k] += v[k + 2]; }
  const bool up = lane & 8;
  const float keep = up ? v[1] : v[0], send = up ? v[0] : v[1];
  return keep + dpp<DPP_ROR8>(send);
}
__device__ __forceinline__ void pin8(float* a) {
  asm volatile("" : "+v"(a[0]), "+v"(a[1]), "+v"(a[2]), "+v"(a[3]), "+v"(a[4]), "+v"(a[5]), "+v"(a[6]), "+v"(a[7]));
}
__device__ __forceinline__ void glds16_nt(const void* gsrc, u32 lds_dst) {
  u32 keep;
  asm volatile("s_mov_b32 %0, m0\n\ts_mov_b32 m0, %2\n\ts_nop 0\n\tglobal_load_lds_dwordx4 %1, off nt\n\ts_mov_b32 m0, %0" : "=&s"(keep) : "v"(gsrc), "s"(lds_dst) : "memory");
}
__device__ __forceinline__ void st_gran(gu64* g, u32 tag, u32 v) { __hip_atomic_store(g, ((u64)tag << 32) | (u64)v, RLX_AGENT); }

constexpr int L_XP = 0;
constexpr int L_Q = L_XP + 576;
constexpr int L_FLAG = L_Q + 128;
constexpr int L_FP = L_FLAG + 16;
constexpr int L_H = L_FP + 1024;
constexpr int L_VEC = L_H + 64;
constexpr int L_SC = L_VEC + 512;
constexpr int L_STASH = L_SC + NSW * 66 * 16;
constexpr int L_R2 = L_STASH;
constexpr int L_WST = 2048;
constexpr int L_RED = 1024;
constexpr int L_TOTAL = L_STASH + NSW * NE * 1024;

__global__ void __launch_bounds__(NTHR, 2)
fused_decode_kernel(const u16* __restrict__ x, const u16* __restrict__ kc, const u16* __restrict__ vc, const u16* __restrict__ wq,
                    const u16* __restrict__ wo, const u16* __restrict__ wg, const u16* __restrict__ wu, const u16* __restrict__ wd,
                    u16* __restrict__ out, unsigned char* __restrict__ ws) {
  __shared__ __attribute__((aligned(16))) u32 lds[L_TOTAL];
  const u32 tstart = (u32)__builtin_amdgcn_s_memrealtime();
  asm volatile("" :: "s"(x), "s"(kc), "s"(vc), "s"(wq), "s"(ws), "s"(tstart));
#define PACE(T) { const u32 ta_ = tstart + ((u32)(T) >> 8); while ((int)((u32)__builtin_amdgcn_s_memrealtime() - ta_) < 0) __builtin_amdgcn_s_sleep(1); }
  float* ldsf = reinterpret_cast<float*>(lds);
  const int tid = (int)threadIdx.x - 64, lane = threadIdx.x & 63;
  const int w = __builtin_amdgcn_readfirstlane((int)(threadIdx.x >> 6)) - 1;
  const int bid = blockIdx.x;
  const int s = bid & 7, g = bid >> 3, b = g >> 4, kvh = g & 15;
  const int ii = b * 8 + s;
  const int ee = (g - 18) * 8 + s, pk = 2 * s + (g >= 9 ? 1 : 0);
  const bool is_wo = g < 18, is_dn = !is_wo && ee < 81, is_red = !is_wo && ee >= 81 && ee < 90;
  const int drb = ee / 9, slice = is_wo ? (g >= 9 ? g - 9 : g) : ee - drb * 9;

  const u16* kbase = kc + (size_t)g * SK * DH;
  const u16* vbase = vc + (size_t)g * SK * DH;
  const int kv_vo = lane * 16;
  const int t0 = tbase(s, w);
  const int hf = lane >> 5, l5 = lane & 31;
  const int qcg = g + 32 * (s >> 2), qkq = s & 3;
  u32x4 EX[NX][U];
#define ISSUE(BUF, BASE, T) { const __amdgpu_buffer_rsrc_t rs_ = mkrs(BASE, SK * DH * 2); const int so_ = (T) * TB; \
    _Pragma("unroll") for (int i_ = 0; i_ < U; ++i_) BUF[i_] = bld<AUX_NT>(rs_, kv_vo + i_ * 1024, so_); SB; }
#define ISSUE0(BUF, T) ISSUE(BUF, kbase, T)
  if (w >= 0) { ISSUE0(EX[0], t0); ISSUE0(EX[1], t0 + tpos(1)); }
  asm volatile("; ROLE SPLIT 0" ::: "memory");
  volatile LAS u32* lflag = (volatile LAS u32*)(lds + L_FLAG);
  if (threadIdx.x < 16) lflag[threadIdx.x] = 0u;
  asm volatile("; ROLE SPLIT 1" ::: "memory");
  if (w < 0) {
    __builtin_amdgcn_s_setprio(3);
    u32x4 wqv[18];
    u32 xr[9];
    {
      const __amdgpu_buffer_rsrc_t rs_x = mkrs(x, 2 * DM * 2);
      const u32x4 xa = bld<0>(rs_x, lane * 36, 0), xb = bld<0>(rs_x, lane * 36 + 16, 0);
      xr[8] = __builtin_amdgcn_raw_buffer_load_b32(rs_x, lane * 36 + 32, 0, 0);
#pragma unroll
      for (int j = 0; j < 4; ++j) { xr[j] = xa[j]; xr[4 + j] = xb[j]; }
    }
    const u32 epoch_v = __hip_atomic_load((gu32*)(ws + WS_EPOCH), RLX_AGENT);
    const __amdgpu_buffer_rsrc_t rs_wq = mkrs(wq, DM * NQ * 2);
    const int vo = (lane >> 3) * 18 * (NQ * 2) + (lane & 7) * 16;
#pragma unroll
    for (int j = 0; j < 18; ++j) wqv[j] = bld<AUX_NT>(rs_wq, vo, qcg * 128 + (qkq * 144 + j) * (NQ * 2));
    SB;
    {
      float v[18]; float sum = 0.f;
#pragma unroll
      for (int j = 0; j < 9; ++j) { v[2 * j] = bf2f(xr[j] & 0xffffu); v[2 * j + 1] = bf2f(xr[j] >> 16); sum += v[2 * j] + v[2 * j + 1]; }
      const float mean = half_sum(sum) * (1.0f / DM);
      float sq = 0.f;
#pragma unroll
      for (int j = 0; j < 18; ++j) { const float d = v[j] - mean; sq = fmaf(d, d, sq); }
      const float var = half_sum(sq) * (1.0f / DM);
      const float mb = rbf(mean), rsd = 1.0f / rbf(sqrtf(rbf(rbf(var) + 1e-5f)));
#pragma unroll
      for (int j = 0; j < 9; ++j)
        lds[L_XP + lane * 9 + j] = f2bf(rbf(v[2 * j] - mb) * rsd) | (f2bf(rbf(v[2 * j + 1] - mb) * rsd) << 16);
    }
    __builtin_amdgcn_fence(__ATOMIC_RELEASE, "wavefront"); __builtin_amdgcn_wave_barrier(); __builtin_amdgcn_fence(__ATOMIC_ACQUIRE, "wavefront");
    const u32 tag = __builtin_amdgcn_readfirstlane(epoch_v) + 0x9E3779B9u;
    float a0[8], a1[8];
#pragma unroll
    for (int k = 0; k < 8; ++k) { a0[k] = 0.f; a1[k] = 0.f; }
    {
      const u32* xp = lds + L_XP + qkq * 72 + (lane >> 3) * 9;
#pragma unroll
      for (int j = 0; j < 9; ++j) pair_mac2(wqv[2 * j], wqv[2 * j + 1], xp[j], xp[288 + j], a0, a1);
      const float s0 = rs8_hi(a0, lane), s1 = rs8_hi(a1, lane);
      gu64* qd = (gu64*)(ws + WS_QG) + (qcg * 4 + qkq) * 64 + (lane & 7) * 8 + (lane >> 3);
      st_gran(qd, tag, __float_as_uint(s0)); st_gran(qd + 16384, tag, __float_as_uint(s1));
    }
    __syncthreads();
    {
      const unsigned char* qsrc = ws + WS_QG + (size_t)(b * 64 + kvh * 4) * 2048;
      __builtin_amdgcn_s_sleep(20);
      const __amdgpu_buffer_rsrc_t rs_q = mkrs(qsrc, 8192);
      u32x4 v[8];
      for (u32 spins = 0;; ++spins) {
        asm volatile("" ::: "memory");
#pragma unroll
        for (int k = 0; k < 8; ++k) v[k] = bld<AUX_SC1>(rs_q, hf * 2048 + l5 * 16, (k >> 2) * 4096 + (k & 3) * 512);
        bool ok = true;
#pragma unroll
        for (int k = 0; k < 8; ++k) ok &= v[k][1] == tag && v[k][3] == tag;
        if (__all(ok) || spins > SPIN_CAP) break;
        __builtin_amdgcn_s_sleep(1);
      }
#pragma unroll
      for (int k = 0; k < 2; ++k) {
        const u32x4* u = v + 4 * k;
        const float qa = (__uint_as_float(u[0][0]) + __uint_as_float(u[1][0])) + (__uint_as_float(u[2][0]) + __uint_as_float(u[3][0]));
        const float qb = (__uint_as_float(u[0][2]) + __uint_as_float(u[1][2])) + (__uint_as_float(u[2][2]) + __uint_as_float(u[3][2]));
        lds[L_Q + k * 64 + lane] = f2bf(qa) | (f2bf(qb) << 16);
      }
      lds[L_FLAG] = tag;
      __builtin_amdgcn_fence(__ATOMIC_RELEASE, "workgroup");
      lflag[1] = 1u;
    }
  }
  asm volatile("; ROLE SPLIT 2" ::: "memory");

  const int task = (s & 3) == (g & 3) ? g * 2 + (s >> 2) : ((g >> 2) == s && (g & 3) == ((s + 1) & 3) ? 64 + s : -1);
  const int r = lane >> 4, c = lane & 15;
  auto final_rows = [&](int j) {
    const u32 tag = __builtin_amdgcn_readfirstlane(lds[L_FLAG]);
    const __amdgpu_buffer_rsrc_t rs_y = mkrs(ws + WS_P2 + (size_t)(slice * 25 + j * 5) * 1024, 5 * 1024);
    u32x4 v[5];
    for (u32 spins = 0;; ++spins) {
      asm volatile("" ::: "memory");
      bool ok = true;
#pragma unroll
      for (int k = 0; k < 5; ++k) v[k] = bld<AUX_SC1>(rs_y, lane * 16, k * 1024);
#pragma unroll
      for (int k = 0; k < 5; ++k) ok &= v[k][1] == tag && v[k][3] == tag;
      if (__all(ok) || spins > SPIN_CAP) break;
      __builtin_amdgcn_s_sleep(1);
    }
    const float a0 = __uint_as_float(v[0][0]), a1 = __uint_as_float(v[0][2]);
    const float b0 = (__uint_as_float(v[1][0]) + __uint_as_float(v[2][0])) + (__uint_as_float(v[3][0]) + __uint_as_float(v[4][0]));
    const float b1 = (__uint_as_float(v[1][2]) + __uint_as_float(v[2][2])) + (__uint_as_float(v[3][2]) + __uint_as_float(v[4][2]));
    *reinterpret_cast<f32x4*>(ldsf + L_SC + (j * 64 + lane) * 4) = j < 3 ? f32x4{a0 + b0, a1 + b1, 0.f, 0.f} : (j == 3 ? f32x4{a0, a1, b0, b1} : f32x4{0.f, 0.f, a0 + b0, a1 + b1});
  };
  if (w >= 0) {
    u32x4 B0[U], B1[U], B2[U];
    float acc0[8], acc1[8];
#pragma unroll
    for (int k = 0; k < 8; ++k) { acc0[k] = 0.f; acc1[k] = 0.f; }
    float mrun = -INFINITY, lrun = 0.f;
    u32* mylds = lds + L_STASH + w * NE * 1024;
#define WAITV(N) __builtin_amdgcn_s_waitcnt(0x0f70 | ((N) & 15) | (((N) >> 4) << 14)); SB
    auto posk = [&](int j) { if (task < 0 || j < 4) return j; const int m = j - 4, q = (m * 57) >> 9; return m - q * 9 == 8 ? 7 + q : 13 + m - q; };
    auto posv = [&](int j) { return task < 0 || j < 4 ? j : j + 3; };
    int ne = 0, tnext = TK + EQ * 256 / 10;
#pragma unroll
    for (int e = 2; e < NX; ++e) { WAITV(4); PACE(tnext); tnext += TKS; ISSUE0(EX[e], t0 + tpos(e)); }
    int slip = 0;
    slip = max((int)(min((u32)__builtin_amdgcn_s_memrealtime() - tstart, 1000u) << 8) - (tnext - TKS), 0) * 3 / 4; tnext += slip;
    if (task >= 0) {
      u32x4 F[5][4];
      float ga[8], gb[8], ua[8], ub[8];
#pragma unroll
      for (int k = 0; k < 8; ++k) { ga[k] = 0.f; gb[k] = 0.f; ua[k] = 0.f; ub[k] = 0.f; }
      const __amdgpu_buffer_rsrc_t rs_g = mkrs(wg, DM * FF * 2), rs_u = mkrs(wu, DM * FF * 2);
      const int fvo = (lane >> 3) * 2 * (FF * 2) + (lane & 7) * 16, fso = w * 16 * (FF * 2) + task * 128;
      const u32* xp = lds + L_XP + w * 8 + (lane >> 3);
#define FI(J) { PACE(tnext); tnext += FTK; const int so_ = fso + (J) * 64 * (FF * 2); u32x4* f_ = F[(J) % 5]; f_[0] = bld<AUX_NT>(rs_g, fvo, so_); f_[1] = bld<AUX_NT>(rs_g, fvo, so_ + FF * 2); \
        f_[2] = bld<AUX_NT>(rs_u, fvo, so_); f_[3] = bld<AUX_NT>(rs_u, fvo, so_ + FF * 2); SB; }
#define FM(J) { const u32 x0_ = xp[32 * (J)], x1_ = xp[288 + 32 * (J)]; const u32x4* f_ = F[(J) % 5]; pair_mac2(f_[0], f_[1], x0_, x1_, ga, gb); pair_mac2(f_[2], f_[3], x0_, x1_, ua, ub); \
        pin8(ga); pin8(gb); pin8(ua); pin8(ub); SB; }
      FI(0); FI(1); FI(2); FI(3);
      __syncthreads();
      FI(4); FM(0); FI(5); FM(1); FI(6); FM(2); FI(7); FM(3); FI(8); FM(4);
      PACE(tnext); tnext += TKS; ISSUE0(B0, t0 + tpos(posk(NX))); FM(5); FM(6);
      PACE(tnext); tnext += TKS; ISSUE0(B1, t0 + tpos(posk(NX + 1))); FM(7); FM(8);
      float* fp = ldsf + L_FP + w * 256 + (lane & 7) * 8 + (lane >> 3);
      fp[0] = rs8_hi(ga, lane); fp[64] = rs8_hi(gb, lane); fp[128] = rs8_hi(ua, lane); fp[192] = rs8_hi(ub, lane);
      __builtin_amdgcn_fence(__ATOMIC_RELEASE, "workgroup");
      lflag[4 + w] = 1u;
    } else {
      WAITV(4); PACE(tnext); tnext += TKS; ISSUE0(B0, t0 + tpos(posk(NX)));
      WAITV(4); PACE(tnext); tnext += TKS; ISSUE0(B1, t0 + tpos(posk(NX + 1)));
      __syncthreads();
    }
    u32x4* stash = reinterpret_cast<u32x4*>(mylds) + lane;
#pragma unroll 1
    while (ne < NE && lflag[1] == 0u) {
      WAITV(4);
#pragma unroll
      for (int i = 0; i < U; ++i) stash[ne * 256 + i * 64] = B0[i];
      SB; PACE(tnext); tnext += TKS; ISSUE0(B0, t0 + tpos(posk(NX + ne + 2)));
      WAITV(4);
#pragma unroll
      for (int i = 0; i < U; ++i) stash[ne * 256 + 256 + i * 64] = B1[i];
      SB; PACE(tnext); tnext += TKS; ISSUE0(B1, t0 + tpos(posk(NX + ne + 3)));
      ne += 2;
    }
    while (lflag[1] == 0u) __builtin_amdgcn_s_sleep(1);
    __builtin_amdgcn_fence(__ATOMIC_ACQUIRE, "workgroup");
    const u32x4 q0 = *reinterpret_cast<const u32x4*>(lds + L_Q + c * 4);
    const u32x4 q1 = *reinterpret_cast<const u32x4*>(lds + L_Q + 64 + c * 4);
    u16* scl = reinterpret_cast<u16*>(lds + L_SC + w * 66 * 16) + (lane >> 1);

    float mseen = -3.0e38f;
    auto kstep = [&](const u32x4* Kt, int slot) {
      float xv[8];
#pragma unroll
      for (int i = 0; i < U; ++i) {
        float d0 = 0.f, d1 = 0.f;
#pragma unroll
        for (int k = 0; k < 4; ++k) { d0 = dot2(Kt[i][k], q0[k], d0); d1 = dot2(Kt[i][k], q1[k], d1); }
        xv[2 * i] = d0; xv[2 * i + 1] = d1;
      }
#pragma unroll
      for (int k = 0; k < 4; ++k) { const bool up = c & 8; const float keep = up ? xv[k + 4] : xv[k], send = up ? xv[k] : xv[k + 4]; xv[k] = keep + dpp<DPP_ROR8>(send); }
#pragma unroll
      for (int k = 0; k < 2; ++k) { const bool up = c & 4; const float keep = up ? xv[k + 2] : xv[k], send = up ? xv[k] : xv[k + 2]; xv[k] = keep + dpp<DPP_HMIR>(send); }
      { const bool up = c & 2; const float keep = up ? xv[1] : xv[0], send = up ? xv[0] : xv[1]; xv[0] = keep + dpp<DPP_XOR2>(send); }
      const float sc = xv[0] + dpp<DPP_XOR1>(xv[0]);
      const u32 sb = f2bf(rbf(sc) * 0.08838834764831845f);
      scl[slot * 32] = (u16)sb;
      mseen = fmaxf(mseen, bf2f(sb));
    };
    auto pv1 = [&](const u32x4& Vt, float p0, float p1) {
#pragma unroll
      for (int k = 0; k < 4; ++k) {
        const f32x2 v = {__uint_as_float(Vt[k] << 16), __uint_as_float(Vt[k] & 0xffff0000u)};
        const f32x2 r0 = __builtin_elementwise_fma(v, f32x2{p0, p0}, f32x2{acc0[2 * k], acc0[2 * k + 1]});
        const f32x2 r1 = __builtin_elementwise_fma(v, f32x2{p1, p1}, f32x2{acc1[2 * k], acc1[2 * k + 1]});
        acc0[2 * k] = r0[0]; acc0[2 * k + 1] = r0[1]; acc1[2 * k] = r1[0]; acc1[2 * k + 1] = r1[1];
      }
    };
    auto vstep = [&](const u32x4* Vt, float p) {
      lrun += p;
      pv1(Vt[0], dpp<DPP_BC + 0>(p), dpp<DPP_BC + 2>(p));
      pv1(Vt[1], dpp<DPP_BC + 4>(p), dpp<DPP_BC + 6>(p));
      pv1(Vt[2], dpp<DPP_BC + 8>(p), dpp<DPP_BC + 10>(p));
      pv1(Vt[3], dpp<DPP_BC + 12>(p), dpp<DPP_BC + 14>(p));
      pin8(acc0); pin8(acc1);
    };
    const int g3 = g & 3, s3 = g3 == (((g >> 2) + 1) & 3) ? (g >> 2) : 8;
    const int nu = s3 < 8 ? 20 : 24, nl = s3 < 8 ? 36 : 24;
    const int u = (s - (s > g3) - (s > g3 + 4) - (s > s3)) * NSW + w;
    const int n = task >= 0 ? NSF : NS + 1 + (u + nu < nl ? 1 : 0);
    auto left = [&](int p) { const int e = p / 12, q = p - e * 12; return tbase(e == 2 ? s3 : g3 + 4 * e, q & 3) + tpos(4 + (q >> 2)); };
    const int xt0 = left(u), xt1 = left(u + nu < nl ? u + nu : 0);
    const int it0 = NX + ne;
    int sbase = NX;
    const int tkk = (T1 + slip - tnext) / (n - it0 - 2), tkv = (T2 - T1) / n;
    constexpr int E_NONE = NT * TB + 0x80;
    auto entry = [&](int pos) { return (pos < NS ? t0 + tpos(pos) : (pos == NS ? xt0 : xt1)) * TB + pos; };
    const int npad = (3 - (n - it0) % 3) % 3, nk = n - it0 + npad;
    const int jk = it0 + 2 + lane - npad;
    const int tabk = lane < npad ? E_NONE : entry(jk < n ? posk(jk) : posv(jk - n));
    const int tabv = lane + 2 < n ? entry(posv(lane + 2)) : E_NONE;
    const __amdgpu_buffer_rsrc_t rs_v = mkrs(vbase, SK * DH * 2);
    int d0 = posk(it0), d1 = posk(it0 + 1), d2, late = 0, i = 0;
    u64 tnow = __builtin_amdgcn_s_memrealtime();
#define GATE() { const u32 ta_ = tstart + ((u32)tnext >> 8); asm volatile("" : "+s"(tnow)); late = (int)((u32)tnow - ta_); if (late >= 0) late += STALE / 10; \
      while (late < 0) { late = (int)((u32)__builtin_amdgcn_s_memrealtime() - ta_); if (late < -6) __builtin_amdgcn_s_sleep(1); } \
      if (late <= LATE2 / 10) { if (late > LATE1 / 10) { WAITV(6); } else { WAITV(4); } } }
#define LOADS(BUF, RS, E) { const int so_ = (E) & ~0xfff; _Pragma("unroll") for (int i_ = 0; i_ < U; ++i_) BUF[i_] = bld<AUX_NT>(RS, kv_vo + i_ * 1024, so_); SB; \
      tnow = __builtin_amdgcn_s_memrealtime(); SB; }
#define FEEDBACK(V) { lflag[8 + w] = (V); const int d_ = tg ? min(max(tg * 256 - slip, -3 * FBN * 256 / 10), max(3 * FBR * 256 / 10, min(late, 1000) * 256)) : 0; slip += d_; tnext += d_; }
#define KSTEP(BUF, D, NBUF, ND) { \
      const int e_ = __builtin_amdgcn_readlane(tabk, i); \
      if (!(e_ & 0x80)) { GATE(); tnext += i + 2 < nk ? tkk : tkv; } \
      LOADS(NBUF, mkrs(i + 2 < nk ? kbase : vbase, SK * DH * 2), e_); ND = e_ & 0xff; \
      if (!(D & 0x80)) kstep(BUF, D); \
      SB; ++i; }
#pragma unroll 1
    do {
      const u32 tgv = lflag[12];
      KSTEP(B0, d0, B2, d2);
      if (ne == 0 && sbase != 0) {
#pragma unroll
        for (int e = 0; e < NX; ++e)
#pragma unroll
          for (int k = 0; k < U; ++k) stash[e * 256 + k * 64] = EX[e][k];
        ne = NX; sbase = 0;
      }
      if (ne > 0) {
        --ne;
        u32x4 KE[U];
#pragma unroll
        for (int k = 0; k < U; ++k) KE[k] = stash[ne * 256 + k * 64];
        kstep(KE, posk(sbase + ne)); SB;
      }
      KSTEP(B1, d1, B0, d0);
      KSTEP(B2, d2, B1, d1);
      const int tg = (int)__builtin_amdgcn_readfirstlane(tgv);
      FEEDBACK((u32)(late + (slip >> 8)));
    } while (i < nk);
    mrun = fmaxf(mseen, dpp<DPP_ROR4>(mseen)); mrun = fmaxf(mrun, dpp<DPP_ROR8>(mrun));
    u32 sn = scl[d0 * 32];
    i = 0;
#define VSTEP(BUF, DN, NBUF, ND) { \
      const int e_ = i < 64 ? __builtin_amdgcn_readlane(tabv, i & 63) : E_NONE; \
      if (!(e_ & 0x80)) { GATE(); tnext += tkv; } else lflag[8 + w] = W_LAST; \
      LOADS(NBUF, rs_v, e_); ND = e_ & 0x7f; \
      const float p_ = __builtin_amdgcn_exp2f((bf2f(sn) - mrun) * LOG2E); \
      sn = scl[DN * 32]; \
      if (i < n) vstep(BUF, p_); \
      SB; ++i; }
#pragma unroll 1
    do {
      const u32 tgv = lflag[12];
      VSTEP(B0, d1, B2, d2);
      VSTEP(B1, d2, B0, d0);
      VSTEP(B2, d0, B1, d1);
      const int tg = (int)__builtin_amdgcn_readfirstlane(tgv);
      if (i + 2 <= n) FEEDBACK(i + 6 < n ? (u32)(late + (slip >> 8)) : W_NEAR);
    } while (i < n);
    const u32 tag = __builtin_amdgcn_readfirstlane(lds[L_FLAG]);
    const int h = w >> 1;
    {
      float lr = lrun + dpp<DPP_ROR4>(lrun); lr += dpp<DPP_ROR8>(lr);
      float a = mrun, bb = mrun; swap16(a, bb); float mw = fmaxf(a, bb);
      a = mw; bb = mw; swap32(a, bb); mw = fmaxf(a, bb);
      const float f = __builtin_amdgcn_exp2f((mrun - mw) * LOG2E);
      lr *= f;
      a = lr; bb = lr; swap16(a, bb); lr = a + bb;
      a = lr; bb = lr; swap32(a, bb); lr = a + bb;
      const float f0 = dpp<DPP_BC + 0>(f), f1 = dpp<DPP_BC + 2>(f);
#pragma unroll
      for (int k = 0; k < 8; ++k) { acc0[k] *= f0; acc1[k] *= f1; }
#pragma unroll
      for (int k = 0; k < 8; ++k) { swap32(acc0[k], acc1[k]); acc0[k] += acc1[k]; }
#pragma unroll
      for (int k = 0; k < 4; ++k) { swap16(acc0[k], acc0[k + 4]); acc0[k] += acc0[k + 4]; }
      *reinterpret_cast<f32x4*>(mylds + L_RED + (r >> 1) * 128 + c * 8 + (r & 1) * 4) = f32x4{acc0[0], acc0[1], acc0[2], acc0[3]};
      if (lane == 0 || lane == 2) { mylds[L_RED + 256 + lane] = __float_as_uint(mw); mylds[L_RED + 256 + lane + 1] = __float_as_uint(lr); }
    }
    __syncthreads();
    const __amdgpu_buffer_rsrc_t rs_p1 = mkrs(ws + WS_P1 + (size_t)kvh * 16 * P1_STRIDE * 8, 16 * P1_STRIDE * 8);
    {
      const float* red = ldsf + L_STASH + L_RED;
      float2 mlw[NSW]; float ow[NSW];
#pragma unroll
      for (int k = 0; k < NSW; ++k) { mlw[k] = *reinterpret_cast<const float2*>(red + k * NE * 1024 + 256 + h * 2); ow[k] = red[k * NE * 1024 + tid]; }
      SB;
      const float M = fmaxf(fmaxf(mlw[0].x, mlw[1].x), fmaxf(mlw[2].x, mlw[3].x));
      float o = 0.f, L = 0.f;
#pragma unroll
      for (int k = 0; k < NSW; ++k) {
        const float f = __builtin_amdgcn_exp2f((mlw[k].x - M) * LOG2E);
        o = fmaf(f, ow[k], o); L = fmaf(f, mlw[k].y, L);
      }
      __builtin_amdgcn_raw_buffer_store_b64(u32x2{__float_as_uint(o), tag}, rs_p1, tid * 8, ii * (P1_STRIDE * 8), AUX_SC1);
      __builtin_amdgcn_raw_buffer_store_b64(u32x2{__float_as_uint((lane & 1) ? L : M), tag}, rs_p1, (tid & 127) < 2 ? (256 + h * 2 + lane) * 8 : 0x7fff0000, ii * (P1_STRIDE * 8), AUX_SC1);
    }
    SB;
#pragma unroll
    for (int k = 0; k < U; ++k) asm volatile("" :: "v"(B0[k]), "v"(B1[k]), "v"(B2[k]));
    if (!is_wo && !is_dn) {
      if (is_red) { final_rows(w + 1); __syncthreads(); }
      return;
    }
    u32* vec = lds + L_VEC;
    const __amdgpu_buffer_rsrc_t rs_s = mkrs(ws + WS_P1 + (size_t)pk * 16 * P1_STRIDE * 8, 16 * P1_STRIDE * 8);
    __syncthreads();
    u32x4 wv[16];
#pragma unroll
    for (int k = 0; k < 8; ++k) wv[k] = *reinterpret_cast<const u32x4*>(mylds + L_WST + k * 256 + lane * 4);
    SB;
    if (is_wo) {
      u32x4 av[8]; u32x2 ml;
      for (u32 spins = 0;; ++spins) {
        asm volatile("" ::: "memory");
#pragma unroll
        for (int k = 0; k < 8; ++k) av[k] = bld<AUX_SC1>(rs_s, hf * (8 * P1_STRIDE * 8) + w * 512 + l5 * 16, k * (P1_STRIDE * 8));
        ml = __builtin_amdgcn_raw_buffer_load_b64(rs_s, ((hf * 8 + (c >> 1)) * P1_STRIDE + 256 + h * 2 + (c & 1)) * 8, 0, AUX_SC1);
        bool ok = ml[1] == tag;
#pragma unroll
        for (int k = 0; k < 8; ++k) ok &= av[k][1] == tag && av[k][3] == tag;
        if (__all(ok) || spins > SPIN_CAP) break;
        __builtin_amdgcn_s_sleep(1);
      }
      const float mlv = __uint_as_float(ml[0]);
      const float mv = dpp<0xA0>(mlv);
      float M = fmaxf(mv, dpp<DPP_XOR2>(mv)); M = fmaxf(M, dpp<DPP_ROR4>(M)); M = fmaxf(M, dpp<DPP_ROR8>(M));
      const float f = __builtin_amdgcn_exp2f((mv - M) * LOG2E);
      float L = f * dpp<0xF5>(mlv);
      L += dpp<DPP_XOR2>(L); L += dpp<DPP_ROR4>(L); L += dpp<DPP_ROR8>(L);
      float oa = 0.f, ob = 0.f;
#define HOP1(K) { const float f_ = dpp<DPP_BC + 2 * (K)>(f); oa = fmaf(__uint_as_float(av[K][0]), f_, oa); ob = fmaf(__uint_as_float(av[K][2]), f_, ob); }
      HOP1(0) HOP1(1) HOP1(2) HOP1(3) HOP1(4) HOP1(5) HOP1(6) HOP1(7)
      const float rl = __builtin_amdgcn_rcpf(L);
      vec[w * 128 + hf * 64 + (l5 >> 2) * 8 + (l5 & 3)] = f2bf(oa * rl) | (f2bf(ob * rl) << 16);
      __builtin_amdgcn_fence(__ATOMIC_RELEASE, "wavefront"); __builtin_amdgcn_wave_barrier(); __builtin_amdgcn_fence(__ATOMIC_ACQUIRE, "wavefront");
    }
    {
      float a0[8], a1[8];
#pragma unroll
      for (int k = 0; k < 8; ++k) { a0[k] = 0.f; a1[k] = 0.f; }
      const u32* vp = vec + w * 128 + (lane >> 3) * 8;
      const u32x4 o0 = *reinterpret_cast<const u32x4*>(vp), o1 = *reinterpret_cast<const u32x4*>(vp + 64);
#pragma unroll
      for (int k = 0; k < 4; ++k) pair_mac2(wv[2 * k], wv[2 * k + 1], o0[k], o1[k], a0, a1);
      if (is_dn) {
#pragma unroll
        for (int k = 8; k < 16; ++k) wv[k] = *reinterpret_cast<const u32x4*>(mylds + L_WST + k * 256 + lane * 4);
        const u32x4 o2 = *reinterpret_cast<const u32x4*>(vp + 4), o3 = *reinterpret_cast<const u32x4*>(vp + 68);
#pragma unroll
        for (int k = 0; k < 4; ++k) pair_mac2(wv[8 + 2 * k], wv[9 + 2 * k], o2[k], o3[k], a0, a1);
      }
      const float s0 = rs8_hi(a0, lane), s1 = rs8_hi(a1, lane);
      const int col = (lane & 7) * 8 + (lane >> 3);
      float* r2 = ldsf + L_R2;
      r2[w * 128 + col] = s0; r2[w * 128 + 64 + col] = s1;
      __syncthreads();
      if (w == 0) {
        const float2 u0 = *reinterpret_cast<const float2*>(r2 + lane * 2), u1 = *reinterpret_cast<const float2*>(r2 + 128 + lane * 2);
        const float2 u2 = *reinterpret_cast<const float2*>(r2 + 256 + lane * 2), u3 = *reinterpret_cast<const float2*>(r2 + 384 + lane * 2);
        __builtin_amdgcn_raw_buffer_store_b128(u32x4{__float_as_uint((u0.x + u1.x) + (u2.x + u3.x)), tag, __float_as_uint((u0.y + u1.y) + (u2.y + u3.y)), tag},
                                               mkrs(ws + WS_P2, 9 * 25 * 1024), lane * 16, (slice * 25 + (is_wo ? pk : 16 + drb)) * 1024, AUX_SC1);
      }
    }
    return;
  }

  {
    const u32 tag = __builtin_amdgcn_readfirstlane(lds[L_FLAG]);
    __builtin_amdgcn_s_setprio(0);
    if (task >= 0) {
      while (!__all(lflag[4 + (lane & 3)] != 0u)) __builtin_amdgcn_s_sleep(8);
      __builtin_amdgcn_fence(__ATOMIC_ACQUIRE, "workgroup");
      const float* fp = ldsf + L_FP + lane;
#pragma unroll
      for (int bb = 0; bb < 2; ++bb) {
        const float gg = rbf((fp[bb * 64] + fp[256 + bb * 64]) + (fp[512 + bb * 64] + fp[768 + bb * 64]));
        const float uu = rbf((fp[128 + bb * 64] + fp[384 + bb * 64]) + (fp[640 + bb * 64] + fp[896 + bb * 64]));
        const float ge = rbf(0.5f * gg * (1.0f + tanhf(0.7978845608028654f * (gg + 0.044715f * gg * gg * gg))));
        reinterpret_cast<u16*>(lds + L_H)[bb * 64 + lane] = (u16)f2bf(ge * uu);
      }
      __builtin_amdgcn_fence(__ATOMIC_RELEASE, "wavefront"); __builtin_amdgcn_wave_barrier(); __builtin_amdgcn_fence(__ATOMIC_ACQUIRE, "wavefront");
      st_gran((gu64*)(ws + WS_HG) + task * 64 + lane, tag, lds[L_H + lane]);
    }
    auto gather_h = [&]() {
      const __amdgpu_buffer_rsrc_t rs_h = mkrs(ws + WS_HG + (size_t)drb * 4096, 4096);
      u32x4 v[4];
      for (u32 spins = 0;; ++spins) {
        asm volatile("" ::: "memory");
        bool ok = true;
#pragma unroll
        for (int k = 0; k < 4; ++k) v[k] = bld<AUX_SC1>(rs_h, lane * 16, k * 1024);
#pragma unroll
        for (int k = 0; k < 4; ++k) ok &= v[k][1] == tag && v[k][3] == tag;
        if (__all(ok) || spins > SPIN_CAP) break;
        __builtin_amdgcn_s_sleep(16);
      }
#pragma unroll
      for (int k = 0; k < 4; ++k) *reinterpret_cast<u32x2*>(lds + L_VEC + k * 128 + (l5 >> 4) * 64 + hf * 32 + (l5 & 15) * 2) = u32x2{v[k][0], v[k][2]};
    };
    bool need_h = is_dn;
    {
      gu64* pg = (gu64*)(ws + WS_PG + g * 2048);
      u32 tn = tstart + FB0 / 10;
      {
        const u32 tv = __builtin_nontemporal_load(reinterpret_cast<const u32*>(lane < 8 ? (lane & 4 ? vbase : kbase) + (size_t)(lane & 3) * (1 << 20) + (1 << 20) - 64 : vbase));
        asm volatile("" :: "v"(tv));
      }
      float tgt_at_pub = 0.f; int nround = 0;
      int ph = 0;
      float shift = 0.f, rate = 0.f;
      u32 tx = tn; bool have = false;
      for (;;) {
        const u32 lv = lflag[8 + (lane & 3)];
        const bool fin = __any(lv >= W_NEAR);
        if (need_h && (fin || (int)(tn - tstart) > 2400)) { gather_h(); need_h = false; }
        if (fin) break;
        const u32 now = (u32)__builtin_amdgcn_s_memrealtime();
        const float cur = shift + rate * (float)(int)(now - tx);
        if ((int)(now - tn) < 0) { if (have) lflag[12] = (u32)max((int)cur, 1); __builtin_amdgcn_s_sleep(16); continue; }
        tn += FBP / 20; ph ^= 1;
        if (ph) {
          float a = (float)(int)lv; a += dpp<DPP_XOR1>(a); a += dpp<DPP_XOR2>(a);
          if (lane == 0) st_gran(pg + s * 32, tag, (u32)(int)(a * 0.25f));
          tgt_at_pub = cur;
          continue;
        }
        const u64 pv = __hip_atomic_load((gu64*)(ws + WS_PG) + ((((g + ((lane >> 3) & 3) * 8) & 31) * 8 + (lane & 7)) * 32), RLX_AGENT);
        if (!__all((u32)(pv >> 32) == tag)) continue;
        float m = (float)(int)(u32)pv; m += dpp<DPP_XOR1>(m); m += dpp<DPP_XOR2>(m); m += dpp<DPP_HMIR>(m); m += dpp<DPP_ROR8>(m);
        { float a = m, bb = m; swap16(a, bb); m = a + bb; a = m; bb = m; swap32(a, bb); m = a + bb; }
        const float err = m * (1.0f / 64) - (float)(FBD / 10) - tgt_at_pub;
        if (nround++ >= 2) rate = fminf(fmaxf(rate + (err - KDZ * 0.1f) * (8.0f / FBP), 0.f), 0.05f);
        shift = cur + err; tx = now; have = true;
        lflag[12] = (u32)max((int)shift, 1);
      }
    }
    u32 xpair = 0;
    if (is_red) xpair = reinterpret_cast<const u32*>(x)[hf * 288 + slice * 32 + l5];
    if (is_wo || is_dn) {
      const int rw = is_wo ? 8 : 16;
      const u16* src = (is_wo ? wo + (size_t)pk * 256 * DM : wd + (size_t)drb * 512 * DM) + (lane >> 3) * rw * DM + slice * 64 + (lane & 7) * 8;
      const u32 dst = (u32)(uintptr_t)(LAS u32*)(lds + L_STASH + L_WST);
      while (!__all(lflag[8 + (lane & 3)] == W_LAST)) __builtin_amdgcn_s_sleep(1);
#pragma unroll 1
      for (int q8 = 0; q8 < rw / 2; ++q8) {
        const int ww = q8 & 3, half = (q8 >> 2) * 8;
#pragma unroll
        for (int k = 0; k < 8; ++k) glds16_nt(src + (8 * ww * rw + half + k) * DM, dst + ww * NE * 4096 + (half + k) * 1024);
        if (q8 == 2) { asm volatile("s_waitcnt lgkmcnt(0)\n\ts_barrier" ::: "memory"); __builtin_amdgcn_s_sleep(3); }
      }
      asm volatile("s_waitcnt vmcnt(0)\n\ts_barrier" ::: "memory");
      return;
    }
    __syncthreads();
    if (is_red) {
      final_rows(0);
      __syncthreads();
      f32x4 t = *reinterpret_cast<const f32x4*>(ldsf + L_SC + lane * 4);
#pragma unroll
      for (int k = 1; k < 5; ++k) t += *reinterpret_cast<const f32x4*>(ldsf + L_SC + (k * 64 + lane) * 4);
      const float y0 = t[0], y1 = t[1], f0 = t[2], f1 = t[3];
      const u32 o0 = f2bf(rbf(bf2f(xpair & 0xffffu) + rbf(y0)) + rbf(f0)), o1 = f2bf(rbf(bf2f(xpair >> 16) + rbf(y1)) + rbf(f1));
      reinterpret_cast<u32*>(out)[hf * 288 + slice * 32 + l5] = o0 | (o1 << 16);
      if (slice == 0 && lane == 0) __hip_atomic_store((gu32*)(ws + WS_EPOCH), tag, RLX_AGENT);
    }
  }
}

extern "C" void kernel_launch(void* const* d_in, const int* in_sizes, int n_in, void* d_out, int out_size, void* d_ws, size_t ws_size,
                              hipStream_t stream) {
  (void)in_sizes; (void)n_in; (void)out_size;
  if (ws_size < WS_END) return;
  hipLaunchKernelGGL(fused_decode_kernel, dim3(256), dim3(NTHR), 0, stream, (const u16*)d_in[0], (const u16*)d_in[1], (const u16*)d_in[2],
                     (const u16*)d_in[3], (const u16*)d_in[4], (const u16*)d_in[6], (const u16*)d_in[7], (const u16*)d_in[8], (u16*)d_out,
                     (unsigned char*)d_ws);
}
```
